```python
import torch

def _softmax(x, axis=-1):
    return torch.softmax(x, dim=axis)
def _gelu(x):
    return torch.nn.functional.gelu(x, approximate="tanh")
def _silu(x):
    return torch.nn.functional.silu(x)
def _sigmoid(x):
    return torch.sigmoid(x)
def _layernorm(x, axis=-1, eps=1e-5):
    m = x.mean(dim=axis, keepdim=True)
    v = x.var(dim=axis, keepdim=True, unbiased=False)
    return (x - m) / torch.sqrt(v + eps)
def _scatter_add(tgt, idx, src, axis=0):
    return tgt.clone().index_add_(axis, idx, src)
def _segment_sum(src, seg, n):
    out = torch.zeros((n,) + src.shape[1:], dtype=src.dtype)
    return out.index_add_(0, seg, src)
def _pad(x, pw):
    flat = tuple(v for pair in reversed(pw) for v in pair)
    return torch.nn.functional.pad(x, flat)

kernel_name = 'sequential_preLN__spectral_gfnet__geglu_ffn__prompt'

def setup_inputs():

    _w = (lambda a: (lambda t: t.to(torch.bfloat16) if t.is_floating_point() else t)(torch.as_tensor(a)))
    def _A_setup():
        B, S, D = 1, 2048, 576
        Df = D // 2 + 1
        return {
            "x": _w((lambda *s: torch.randn(*s, dtype=torch.bfloat16) * 0.05)(B, S, D)),
            "w_r": _w((lambda *s: torch.randn(*s, dtype=torch.bfloat16) * 0.4)(S, Df) * 0.02),
            "w_i": _w((lambda *s: torch.randn(*s, dtype=torch.bfloat16) * 0.4)(S, Df) * 0.02),
        }
    _A_in = _A_setup()

    def _B_setup():
        B, S, D, F = 1, 2048, 576, 4608
        return {
            "x": _w((lambda *s: torch.randn(*s, dtype=torch.bfloat16) * 0.05)(B, S, D)),
            "w_gate": _w((lambda *s: torch.randn(*s, dtype=torch.bfloat16) * 0.05)(D, F)),
            "w_up": _w((lambda *s: torch.randn(*s, dtype=torch.bfloat16) * 0.05)(D, F)),
            "w_down": _w((lambda *s: torch.randn(*s, dtype=torch.bfloat16) * 0.05)(F, D)),
        }
    _B_in = _B_setup()

    return {
        "A_x": _A_in["x"],
        "A_w_r": _A_in["w_r"],
        "A_w_i": _A_in["w_i"],
        "B_x": _B_in["x"],
        "B_w_gate": _B_in["w_gate"],
        "B_w_up": _B_in["w_up"],
        "B_w_down": _B_in["w_down"],
    }

def reference(A_x, A_w_r, A_w_i, B_x, B_w_gate, B_w_up, B_w_down):
    def _A_ref(x, w_r, w_i):
        B, S, D = x.shape
        X = (lambda x, axes=(-2,-1): torch.fft.rfft2(x.float(), dim=axes))(x, axes=(-2, -1))
        W = w_r + 1j * w_i
        X = X * W
        y = (lambda x, s, axes=(-2,-1): torch.fft.irfft2(x, s=s, dim=axes).to(torch.bfloat16))(X, (S, D), axes=(-2, -1))
        return y

    def _B_ref(x, w_gate, w_up, w_down):
        return (_gelu(x @ w_gate) * (x @ w_up)) @ w_down

    x = A_x
    h = x + _A_ref(_layernorm(x, axis=-1), A_w_r, A_w_i)
    h2 = h + _B_ref(_layernorm(h, axis=-1), B_w_gate, B_w_up, B_w_down)
    return h2
```

```cpp
typedef __bf16   bf16x8 __attribute__((ext_vector_type(8)));
typedef __bf16   bf16x4 __attribute__((ext_vector_type(4)));
typedef __bf16   bf16x2 __attribute__((ext_vector_type(2)));
typedef short    s16x4  __attribute__((ext_vector_type(4)));
typedef float    f32x2  __attribute__((ext_vector_type(2)));
typedef float    f32x4  __attribute__((ext_vector_type(4)));
typedef unsigned u32x2  __attribute__((ext_vector_type(2)));
typedef unsigned u32x4  __attribute__((ext_vector_type(4)));
#define DI __device__ __forceinline__
#define LAS __attribute__((address_space(3)))

constexpr int S_ = 2048, D_ = 576, NB_ = 289, F_ = 4608;
constexpr int TSTRIDE = 289 * 64 + 64;
constexpr size_t WS_Z1 = 0;
constexpr size_t WS_WT = WS_Z1 + 289 * 8192;
constexpr size_t WS_Z2 = WS_WT + 289 * 8192;
constexpr size_t WS_XN = WS_Z2 + 128 * TSTRIDE;
constexpr size_t WS_H  = WS_XN + 128 * 18 * 1024;
constexpr size_t WS_A  = WS_H + 2048 * 576 * 2;
constexpr size_t WS_WD = WS_A + (size_t)128 * 144 * 1024;
constexpr size_t WS_P  = WS_WD + (size_t)144 * 36 * 1024;
constexpr size_t WS_END = WS_P + (size_t)4 * 2048 * 576 * 4;

DI unsigned pk(float lo, float hi) { f32x2 v = {lo, hi}; bf16x2 b = __builtin_convertvector(v, bf16x2); return __builtin_bit_cast(unsigned, b); }
DI float bflo(unsigned u) { return __uint_as_float(u << 16); }
DI float bfhi(unsigned u) { return __uint_as_float(u & 0xffff0000u); }
DI float rbf(float x) { return bflo(pk(x, x)); }
DI float sinr(float rev) { return __builtin_amdgcn_sinf(rev); }
DI float cosr(float rev) { return __builtin_amdgcn_cosf(rev); }
DI f32x4 mfma32(u32x4 a, u32x4 b, f32x4 c) { return __builtin_amdgcn_mfma_f32_16x16x32_bf16(__builtin_bit_cast(bf16x8, a), __builtin_bit_cast(bf16x8, b), c, 0, 0, 0); }
DI f32x4 mfma16(u32x2 a, u32x2 b, f32x4 c) { const u32x4 a4 = {a[0], a[1], 0u, 0u}, b4 = {b[0], b[1], 0u, 0u}; return mfma32(a4, b4, c); }
DI u32x2 trread(const char* p) { return __builtin_bit_cast(u32x2, __builtin_amdgcn_ds_read_tr16_b64_v4i16((LAS s16x4*)p)); }
template <int CTRL> DI float dpp_add(float v) { return v + __uint_as_float(__builtin_amdgcn_update_dpp(0u, __float_as_uint(v), CTRL, 0xf, 0xf, true)); }
DI float row16_sum(float v) {
  v = dpp_add<0x128>(v);
  v = dpp_add<0x124>(v);
  v = dpp_add<0x122>(v);
  v = dpp_add<0x121>(v);
  return v;
}
DI float wave_sum(float v) {
  v = row16_sum(v);
  auto a = __builtin_amdgcn_permlane16_swap(__float_as_uint(v), __float_as_uint(v), false, false);
  v = __uint_as_float(a[0]) + __uint_as_float(a[1]);
  auto b = __builtin_amdgcn_permlane32_swap(__float_as_uint(v), __float_as_uint(v), false, false);
  return __uint_as_float(b[0]) + __uint_as_float(b[1]);
}
DI float ln_rsd(float var) { float t = rbf(rbf(var) + 1e-5f); return 1.0f / rbf(__builtin_amdgcn_sqrtf(t)); }

constexpr int K1_ROWB = 1152;
__global__ __launch_bounds__(256) void k1_kernel(const unsigned short* __restrict__ x, const unsigned short* __restrict__ wr,
                                                 const unsigned short* __restrict__ wi, char* __restrict__ ws) {
  __shared__ __attribute__((aligned(16))) char lds[16 * K1_ROWB + 289 * 64];
  char* lin = lds;
  char* lout = lds + 16 * K1_ROWB;
  const int tid = threadIdx.x, v = tid >> 6, l = tid & 63, c = l & 15, g = l >> 4;
  const int blk = blockIdx.x;
  char* dst;
  if (blk < 128) {
    const int s2 = blk >> 2, wq = blk & 3;
    dst = ws + WS_Z1 + ((s2 >> 4) * 4 + wq) * 1024 + (s2 & 15) * 64;
    const int row = 512 * wq + 32 * (4 * v + g) + s2;
    const u32x2* src = (const u32x2*)(x + (size_t)row * D_) + c;
    u32x2 raw[9];
#pragma unroll
    for (int p = 0; p < 9; ++p) raw[p] = src[p * 16];
    u32x4 bc[2], bs[2]; u32x2 bc1[2], bs1[2]; float twc[2][4], tws[2][4]; u32x4 p16, q16;
#pragma unroll
    for (int ct = 0; ct < 2; ++ct) {
      const float k1 = (float)(c + 16 * ct);
      const float stp = k1 * (1.0f / 36.0f);
#pragma unroll
      for (int e = 0; e < 4; ++e) {
        const float a0 = stp * (float)(8 * g + 2 * e), a1 = stp * (float)(8 * g + 2 * e + 1);
        bc[ct][e] = pk(cosr(a0), cosr(a1)); bs[ct][e] = pk(-sinr(a0), -sinr(a1));
      }
#pragma unroll
      for (int e = 0; e < 2; ++e) {
        const float a0 = stp * (float)(32 + 2 * e), a1 = stp * (float)(33 + 2 * e);
        bc1[ct][e] = g == 0 ? pk(cosr(a0), cosr(a1)) : 0u; bs1[ct][e] = g == 0 ? pk(-sinr(a0), -sinr(a1)) : 0u;
      }
#pragma unroll
      for (int j = 0; j < 4; ++j) { const float a = k1 * (float)(4 * g + j) * (1.0f / 576.0f); twc[ct][j] = cosr(a); tws[ct][j] = sinr(a); }
    }
#pragma unroll
    for (int j = 0; j < 4; ++j) { const float a = (float)(c * (4 * g + j)) * (1.0f / 16.0f); const float cs = cosr(a), sn = sinr(a); p16[j] = pk(cs, sn); q16[j] = pk(-sn, cs); }
    float xv[36]; float sum = 0.f;
#pragma unroll
    for (int p = 0; p < 9; ++p) { xv[4 * p] = bflo(raw[p][0]); xv[4 * p + 1] = bfhi(raw[p][0]); xv[4 * p + 2] = bflo(raw[p][1]); xv[4 * p + 3] = bfhi(raw[p][1]); }
#pragma unroll
    for (int e = 0; e < 36; ++e) sum += xv[e];
    const float mean = row16_sum(sum) * (1.0f / 576.0f);
    float sq = 0.f;
#pragma unroll
    for (int e = 0; e < 36; ++e) { const float d = xv[e] - mean; sq += d * d; }
    const float rsd = ln_rsd(row16_sum(sq) * (1.0f / 576.0f)), mb = rbf(mean);
    char* myrow = lin + (4 * v + g) * K1_ROWB + c * 8;
#pragma unroll
    for (int p = 0; p < 9; ++p) {
      u32x2 o; o[0] = pk((xv[4 * p] - mb) * rsd, (xv[4 * p + 1] - mb) * rsd); o[1] = pk((xv[4 * p + 2] - mb) * rsd, (xv[4 * p + 3] - mb) * rsd);
      *(u32x2*)(myrow + p * 128) = o;
    }
    __builtin_amdgcn_wave_barrier();
    u32x4 outv[2][4];
#pragma unroll
    for (int i = 0; i < 4; ++i) {
      const char* rb = lin + (4 * v + i) * K1_ROWB + c * 8;
      const u32x2 a0 = trread(rb + g * 256), a1 = trread(rb + g * 256 + 128), a2 = trread(rb + 1024);
      const u32x4 a = {a0[0], a0[1], a1[0], a1[1]};
#pragma unroll
      for (int ct = 0; ct < 2; ++ct) {
        const f32x4 z = {0.f, 0.f, 0.f, 0.f};
        f32x4 yr = mfma16(a2, bc1[ct], mfma32(a, bc[ct], z)), yi = mfma16(a2, bs1[ct], mfma32(a, bs[ct], z));
        u32x4 b;
#pragma unroll
        for (int j = 0; j < 4; ++j) b[j] = pk(yr[j] * twc[ct][j] + yi[j] * tws[ct][j], yi[j] * twc[ct][j] - yr[j] * tws[ct][j]);
        const f32x4 xr = mfma32(p16, b, z), xi = mfma32(q16, b, z);
#pragma unroll
        for (int j = 0; j < 4; ++j) outv[ct][j][i] = pk(xr[j], xi[j]);
      }
    }
#pragma unroll
    for (int ct = 0; ct < 2; ++ct)
#pragma unroll
      for (int j = 0; j < 4; ++j) {
        const int k1 = c + 16 * ct, k = k1 + 36 * (4 * g + j);
        const bool mir = k > 288;
        const int bin = mir ? 576 - k : k;
        const bool ok = k1 <= 18 && !(mir && (k1 == 0 || k1 == 18));
        u32x4 o = outv[ct][j];
        const unsigned m = mir ? 0x80000000u : 0u;
        o[0] ^= m; o[1] ^= m; o[2] ^= m; o[3] ^= m;
        if (ok) *(u32x4*)(lout + bin * 64 + ((v ^ (bin >> 2)) & 3) * 16) = o;
      }
  } else {
    const int b2 = blk - 128, f1 = b2 >> 1, u = b2 & 1;
    dst = ws + WS_WT + ((f1 >> 4) * 2 + u) * 1024 + (f1 & 15) * 64;
    unsigned short a[5][4], b[5][4];
#pragma unroll
    for (int it = 0; it < 5; ++it)
#pragma unroll
      for (int i = 0; i < 4; ++i) {
        const int f = f1 + 64 * (16 * u + 4 * v + i); const int bin = min(l + 64 * it, 288);
        a[it][i] = wr[f * NB_ + bin]; b[it][i] = wi[f * NB_ + bin];
      }
#pragma unroll
    for (int it = 0; it < 5; ++it) {
      const int bin = min(l + 64 * it, 288);
      u32x4 o;
#pragma unroll
      for (int i = 0; i < 4; ++i) o[i] = (unsigned)a[it][i] | ((unsigned)b[it][i] << 16);
      *(u32x4*)(lout + bin * 64 + ((v ^ (bin >> 2)) & 3) * 16) = o;
    }
  }
  __syncthreads();
#pragma unroll
  for (int it = 0; it < 5; ++it) {
    const int bin = (tid >> 2) + 64 * it, q = tid & 3;
    if (bin < NB_) *(u32x4*)(dst + (size_t)bin * 8192 + q * 16) = *(const u32x4*)(lout + bin * 64 + ((q ^ (bin >> 2)) & 3) * 16);
  }
}

__global__ __launch_bounds__(256) void k2_kernel(char* __restrict__ ws) {
  __shared__ __attribute__((aligned(16))) u32x4 lds[8 * 64];
  const int tid = threadIdx.x, w = tid >> 6, l = tid & 63, c = l & 15, g = l >> 4;
  const int bin = blockIdx.x;
  const char* z1 = ws + WS_Z1 + (size_t)bin * 8192 + c * 64 + g * 16;
  u32x4 a[2][4], wf[2];
#pragma unroll
  for (int t = 0; t < 2; ++t)
#pragma unroll
    for (int q = 0; q < 4; ++q) a[t][q] = *(const u32x4*)(z1 + (t * 4 + q) * 1024);
#pragma unroll
  for (int u = 0; u < 2; ++u) wf[u] = *(const u32x4*)(ws + WS_WT + (size_t)bin * 8192 + (w * 2 + u) * 1024 + c * 64 + g * 16);
  u32x4 p64[4], q64[4], p32[2][2], q32[2][2]; float t1c[2][4], t1s[2][4], t2c[2][4], t2s[2][4];
  const float fl = (float)(16 * w + c);
#pragma unroll
  for (int q = 0; q < 4; ++q)
#pragma unroll
    for (int j = 0; j < 4; ++j) { const float an = fl * (float)(16 * q + 4 * g + j) * (1.0f / 64.0f); const float cs = cosr(an), sn = sinr(an); p64[q][j] = pk(cs, sn); q64[q][j] = pk(-sn, cs); }
#pragma unroll
  for (int u = 0; u < 2; ++u)
#pragma unroll
    for (int t = 0; t < 2; ++t)
#pragma unroll
      for (int j = 0; j < 4; ++j) { const float an = (float)((16 * u + c) * (16 * t + 4 * g + j)) * (1.0f / 32.0f); const float cs = cosr(an), sn = sinr(an); p32[u][t][j] = pk(cs, sn); q32[u][t][j] = pk(-sn, cs); }
  constexpr float NRM = 1.0f / (2048.0f * 576.0f);
#pragma unroll
  for (int t = 0; t < 2; ++t)
#pragma unroll
    for (int j = 0; j < 4; ++j) {
      const float a1 = fl * (float)(16 * t + 4 * g + j) * (1.0f / 2048.0f); t1c[t][j] = cosr(a1); t1s[t][j] = sinr(a1);
      const float a2 = (float)((16 * t + c) * (16 * w + 4 * g + j)) * (1.0f / 2048.0f); t2c[t][j] = cosr(a2) * NRM; t2s[t][j] = sinr(a2) * NRM;
    }
  const f32x4 z = {0.f, 0.f, 0.f, 0.f};
  u32x4 b[2];
#pragma unroll
  for (int t = 0; t < 2; ++t) {
    f32x4 yr = z, yi = z;
#pragma unroll
    for (int q = 0; q < 4; ++q) { yr = mfma32(a[t][q], p64[q], yr); yi = mfma32(a[t][q], q64[q], yi); }
#pragma unroll
    for (int j = 0; j < 4; ++j) b[t][j] = pk(yr[j] * t1c[t][j] + yi[j] * t1s[t][j], yi[j] * t1c[t][j] - yr[j] * t1s[t][j]);
  }
  u32x4 xa[2];
#pragma unroll
  for (int u = 0; u < 2; ++u) {
    const f32x4 xr = mfma32(p32[u][1], b[1], mfma32(p32[u][0], b[0], z)), xi = mfma32(q32[u][1], b[1], mfma32(q32[u][0], b[0], z));
#pragma unroll
    for (int j = 0; j < 4; ++j) { const float wr = bflo(wf[u][j]), wi = bfhi(wf[u][j]); xa[u][j] = pk(xr[j] * wi + xi[j] * wr, xr[j] * wr - xi[j] * wi); }
  }
#pragma unroll
  for (int t = 0; t < 2; ++t) {
    const f32x4 ur = mfma32(xa[1], q32[t][1], mfma32(xa[0], q32[t][0], z)), ui = mfma32(xa[1], p32[t][1], mfma32(xa[0], p32[t][0], z));
    u32x4 o;
#pragma unroll
    for (int j = 0; j < 4; ++j) o[j] = pk(ui[j] * t2c[t][j] + ur[j] * t2s[t][j], ur[j] * t2c[t][j] - ui[j] * t2s[t][j]);
    lds[(w * 2 + t) * 64 + l] = o;
  }
  __syncthreads();
#pragma unroll
  for (int t = 0; t < 2; ++t) {
    f32x4 zr = z, zi = z;
#pragma unroll
    for (int q = 0; q < 4; ++q) { const u32x4 bb = lds[(q * 2 + t) * 64 + l]; zr = mfma32(q64[q], bb, zr); zi = mfma32(p64[q], bb, zi); }
    u32x4 o;
#pragma unroll
    for (int j = 0; j < 4; ++j) o[j] = pk(zr[j], zi[j]);
    const int tile = 4 * (16 * t + c) + w;
    *(u32x4*)(ws + WS_Z2 + (size_t)tile * TSTRIDE + bin * 64 + ((g ^ (bin >> 2)) & 3) * 16) = o;
  }
}

constexpr int K3_RS = 1152 + 16;
__global__ __launch_bounds__(256) void k3_kernel(const unsigned short* __restrict__ x, char* __restrict__ ws) {
  __shared__ __attribute__((aligned(16))) char lds[TSTRIDE + 16 * K3_RS];
  char* lin = lds; char* lout = lds + TSTRIDE;
  const int tid = threadIdx.x, v = tid >> 6, l = tid & 63, c = l & 15, g = l >> 4;
  const int tile = blockIdx.x, s2 = tile >> 2, wq = tile & 3;
  u32x4 st[5];
#pragma unroll
  for (int it = 0; it < 5; ++it) { const int o = min(tid + 256 * it, 289 * 4 - 1) * 16; st[it] = *(const u32x4*)(ws + WS_Z2 + (size_t)tile * TSTRIDE + o); }
  const int cc = min(c, 11);
  u32x2 xr[4][3];
#pragma unroll
  for (int i = 0; i < 4; ++i)
#pragma unroll
    for (int ct = 0; ct < 3; ++ct) xr[i][ct] = *(const u32x2*)(x + (size_t)(512 * wq + 32 * (4 * v + i) + s2) * D_ + 16 * (12 * ct + cc) + 4 * g);
  u32x4 b1r, b1i, b2[3]; u32x2 b2x[3]; float twc[5], tws[5];
#pragma unroll
  for (int j = 0; j < 4; ++j) { const float a = (float)(c * (4 * g + j)) * (1.0f / 16.0f); const float cs = cosr(a), sn = sinr(a); b1r[j] = pk(cs, -sn); b1i[j] = pk(sn, cs); }
#pragma unroll
  for (int j = 0; j < 5; ++j) { const int k1 = j < 4 ? 4 * g + j : 16 + g; const float a = (float)(c * k1) * (1.0f / 576.0f);
    const float ck = (k1 == 0 || k1 == 18) ? 1.f : (k1 > 18 ? 0.f : 2.f); twc[j] = cosr(a) * ck; tws[j] = sinr(a) * ck; }
#pragma unroll
  for (int ct = 0; ct < 3; ++ct) {
    const float stp = (float)(12 * ct + c) * (1.0f / 36.0f);
#pragma unroll
    for (int j = 0; j < 4; ++j) { const float a = stp * (float)(4 * g + j); b2[ct][j] = pk(cosr(a), -sinr(a)); }
    const float a = stp * (float)(16 + g); b2x[ct][0] = pk(cosr(a), -sinr(a)); b2x[ct][1] = 0u;
  }
#pragma unroll
  for (int it = 0; it < 5; ++it) { const int o = min(tid + 256 * it, 289 * 4 - 1) * 16; *(u32x4*)(lin + o) = st[it]; }
  __syncthreads();
  u32x4 zin[2][4];
#pragma unroll
  for (int rt = 0; rt < 2; ++rt)
#pragma unroll
    for (int j = 0; j < 4; ++j) {
      const int k1 = rt == 0 ? c : min(16 + (c >> 2), 18), k = k1 + 36 * (4 * g + j);
      const bool mir = k > 288; const int bin = mir ? 576 - k : k;
      u32x4 t = *(const u32x4*)(lin + bin * 64 + ((v ^ (bin >> 2)) & 3) * 16);
      const unsigned m = mir ? 0x80000000u : 0u;
      t[0] ^= m; t[1] ^= m; t[2] ^= m; t[3] ^= m; zin[rt][j] = t;
    }
  const f32x4 z = {0.f, 0.f, 0.f, 0.f};
  float hv[4][12], sum[4];
#pragma unroll
  for (int i = 0; i < 4; ++i) {
    const u32x4 a0 = {zin[0][0][i], zin[0][1][i], zin[0][2][i], zin[0][3][i]}, a1 = {zin[1][0][i], zin[1][1][i], zin[1][2][i], zin[1][3][i]};
    const f32x4 vr = mfma32(a0, b1r, z), vi = mfma32(a0, b1i, z), vr1 = mfma32(a1, b1r, z), vi1 = mfma32(a1, b1i, z);
    u32x4 a; u32x2 ax;
#pragma unroll
    for (int j = 0; j < 4; ++j) a[j] = pk(vr[j] * twc[j] - vi[j] * tws[j], vi[j] * twc[j] + vr[j] * tws[j]);
    ax[0] = pk(vr1[0] * twc[4] - vi1[0] * tws[4], vi1[0] * twc[4] + vr1[0] * tws[4]); ax[1] = 0u;
    float s = 0.f;
#pragma unroll
    for (int ct = 0; ct < 3; ++ct) {
      const f32x4 y = mfma16(ax, b2x[ct], mfma32(a, b2[ct], z));
      const unsigned y01 = pk(y[0], y[1]), y23 = pk(y[2], y[3]);
      u32x2 h; h[0] = pk(bflo(xr[i][ct][0]) + bflo(y01), bfhi(xr[i][ct][0]) + bfhi(y01)); h[1] = pk(bflo(xr[i][ct][1]) + bflo(y23), bfhi(xr[i][ct][1]) + bfhi(y23));
      if (c < 12) *(u32x2*)(ws + WS_H + ((size_t)(512 * wq + 32 * (4 * v + i) + s2) * D_ + 16 * (12 * ct + c) + 4 * g) * 2) = h;
      hv[i][4 * ct] = bflo(h[0]); hv[i][4 * ct + 1] = bfhi(h[0]); hv[i][4 * ct + 2] = bflo(h[1]); hv[i][4 * ct + 3] = bfhi(h[1]);
      s += (hv[i][4 * ct] + hv[i][4 * ct + 1]) + (hv[i][4 * ct + 2] + hv[i][4 * ct + 3]);
    }
    sum[i] = c < 12 ? s : 0.f;
  }
#pragma unroll
  for (int i = 0; i < 4; ++i) {
    const float mean = wave_sum(sum[i]) * (1.0f / 576.0f);
    float sq = 0.f;
#pragma unroll
    for (int e = 0; e < 12; ++e) { const float d = hv[i][e] - mean; sq += d * d; }
    const float rsd = ln_rsd(wave_sum(c < 12 ? sq : 0.f) * (1.0f / 576.0f)), mb = rbf(mean);
#pragma unroll
    for (int ct = 0; ct < 3; ++ct) {
      u32x2 o; o[0] = pk(rbf(hv[i][4 * ct] - mb) * rsd, rbf(hv[i][4 * ct + 1] - mb) * rsd); o[1] = pk(rbf(hv[i][4 * ct + 2] - mb) * rsd, rbf(hv[i][4 * ct + 3] - mb) * rsd);
      if (c < 12) *(u32x2*)(lout + (4 * v + i) * K3_RS + (16 * (12 * ct + c) + 4 * g) * 2) = o;
    }
  }
  __syncthreads();
#pragma unroll
  for (int it = 0; it < 5; ++it) {
    const int ks = v + 4 * it;
    if (ks < 18) *(u32x4*)(ws + WS_XN + ((size_t)tile * 18 + ks) * 1024 + l * 16) = *(const u32x4*)(lout + c * K3_RS + ks * 64 + g * 16);
  }
}
__global__ __launch_bounds__(64) void g1_naive(const unsigned short* __restrict__ wg, const unsigned short* __restrict__ wu, char* __restrict__ ws) {
  const int l = threadIdx.x, c = l & 15, g = l >> 4; const int tile = blockIdx.x & 127, fk = blockIdx.x >> 7;
  f32x4 acc[2][2] = {};
  for (int ks = 0; ks < 18; ++ks) {
    const u32x4 xb = *(const u32x4*)(ws + WS_XN + ((size_t)tile * 18 + ks) * 1024 + l * 16);
    for (int h = 0; h < 2; ++h) {
      const int f = 32 * fk + 8 * (c >> 2) + 4 * h + (c & 3);
      u32x4 a, b;
      for (int e = 0; e < 4; ++e) { const int k = 32 * ks + 8 * g + 2 * e;
        a[e] = wg[(size_t)k * F_ + f] | ((unsigned)wg[(size_t)(k + 1) * F_ + f] << 16);
        b[e] = wu[(size_t)k * F_ + f] | ((unsigned)wu[(size_t)(k + 1) * F_ + f] << 16); }
      acc[0][h] = mfma32(a, xb, acc[0][h]); acc[1][h] = mfma32(b, xb, acc[1][h]);
    }
  }
  u32x4 o; float r[8];
  for (int h = 0; h < 2; ++h) for (int j = 0; j < 4; ++j) { const float gg = acc[0][h][j];
    const float e = __builtin_amdgcn_exp2f(gg * (gg * gg * (-0.10294324f) + (-2.3022082f))); r[4 * h + j] = gg / (1.f + e) * acc[1][h][j]; }
  for (int e = 0; e < 4; ++e) o[e] = pk(r[2 * e], r[2 * e + 1]);
  *(u32x4*)(ws + WS_A + ((size_t)tile * 144 + fk) * 1024 + l * 16) = o;
}
__global__ __launch_bounds__(64) void g2_naive(const unsigned short* __restrict__ wd, char* __restrict__ ws, unsigned short* __restrict__ out) {
  const int l = threadIdx.x, c = l & 15, g = l >> 4; const int tile = blockIdx.x & 127, nt = blockIdx.x >> 7;
  f32x4 acc = {};
  for (int ks = 0; ks < 144; ++ks) {
    const u32x4 hb = *(const u32x4*)(ws + WS_A + ((size_t)tile * 144 + ks) * 1024 + l * 16);
    u32x4 a;
    for (int e = 0; e < 4; ++e) { const int k = 32 * ks + 8 * g + 2 * e; a[e] = wd[(size_t)k * D_ + 16 * nt + c] | ((unsigned)wd[(size_t)(k + 1) * D_ + 16 * nt + c] << 16); }
    acc = mfma32(a, hb, acc);
  }
  const int row = 512 * (tile & 3) + 32 * c + (tile >> 2);
  const size_t o = (size_t)row * D_ + 16 * nt + 4 * g;
  const u32x2 h = *(const u32x2*)(ws + WS_H + o * 2);
  u32x2 r; r[0] = pk(bflo(h[0]) + rbf(acc[0]), bfhi(h[0]) + rbf(acc[1])); r[1] = pk(bflo(h[1]) + rbf(acc[2]), bfhi(h[1]) + rbf(acc[3]));
  *(u32x2*)(out + o) = r;
}
extern "C" void kernel_launch(void* const* d_in, const int* in_sizes, int n_in, void* d_out, int out_size, void* d_ws, size_t ws_size, hipStream_t stream) {
  const unsigned short* x = (const unsigned short*)d_in[0]; const unsigned short* wr = (const unsigned short*)d_in[1]; const unsigned short* wi = (const unsigned short*)d_in[2];
  const unsigned short* wg = (const unsigned short*)d_in[4]; const unsigned short* wu = (const unsigned short*)d_in[5]; const unsigned short* wd = (const unsigned short*)d_in[6];
  char* ws = (char*)d_ws;
  k1_kernel<<<256, 256, 0, stream>>>(x, wr, wi, ws);
  k2_kernel<<<289, 256, 0, stream>>>(ws);
  k3_kernel<<<128, 256, 0, stream>>>(x, ws);
  g1_naive<<<128 * 144, 64, 0, stream>>>(wg, wu, ws);
  g2_naive<<<128 * 36, 64, 0, stream>>>(wd, ws, (unsigned short*)d_out);
}
```

```cpp
#include <hip/hip_runtime.h>
#include <hip/hip_bf16.h>

static __device__ const unsigned unit_table[256] asm("mega_unit_table") __attribute__((used, aligned(128))) = {
  0x11c14006, 0x09894095, 0x11514124, 0x091941b3, 0x00e14242, 0x08a942d1, 0x00714368, 0x083943f7,
  0x11c16006, 0x09896095, 0x11516124, 0x091961b3, 0x00e16242, 0x08a962d1, 0x00716368, 0x083963f7,
  0x11c3006e, 0x098b00e0, 0x11530152, 0x091b01c4, 0x00e30236, 0x08ab02a8, 0x0073031a, 0x083b038c,
  0x11c1806e, 0x098980e0, 0x11518152, 0x091981c4, 0x00e18236, 0x08a982a8, 0x0071831a, 0x0839838c,
  0x11c3206f, 0x078b20e1, 0x11532153, 0x071b21c5, 0x10e32237, 0x06ab22a9, 0x1073231b, 0x083b238d,
  0x11c1a06f, 0x0789a0e1, 0x1151a153, 0x0719a1c5, 0x10e1a237, 0x06a9a2a9, 0x1071a31b, 0x0839a38d,
  0x11cb04f0, 0x07930562, 0x115b05d4, 0x07230646, 0x10eb06b8, 0x06b3072a, 0x107b079c, 0x0843080e,
  0x11c1c070, 0x0789c0e2, 0x1151c154, 0x0719c1c6, 0x10e1c238, 0x06a9c2aa, 0x1071c31c, 0x0839c38e,
  0x0fc94487, 0x07914516, 0x0f5945a5, 0x07214634, 0x0ee946c3, 0x06b1475a, 0x107947e9, 0x06414878,
  0x0fc96487, 0x07916516, 0x0f5965a5, 0x07216634, 0x0ee966c3, 0x06b1675a, 0x107967e9, 0x06416878,
  0x0fcb24f1, 0x07932563, 0x0f5b25d5, 0x07232647, 0x0eeb26b9, 0x06b3272b, 0x107b279d, 0x0643280f,
  0x0fc984f1, 0x07918563, 0x0f5985d5, 0x07218647, 0x0ee986b9, 0x06b1872b, 0x1079879d, 0x0641880f,
  0x0fd30972, 0x059b09e4, 0x0f630a56, 0x052b0ac8, 0x0ef30b3a, 0x04bb0bac, 0x0e830c1e, 0x044b0c90,
  0x0fc9a4f2, 0x0591a564, 0x0f59a5d6, 0x0521a648, 0x0ee9a6ba, 0x04b1a72c, 0x0e79a79e, 0x0441a810,
  0x0fd32973, 0x059b29e5, 0x0f632a57, 0x052b2ac9, 0x0ef32b3b, 0x04bb2bad, 0x0e832c1f, 0x044b2c91,
  0x0fc9c4f3, 0x0591c565, 0x0f59c5d7, 0x0521c649, 0x0ee9c6bb, 0x04b1c72d, 0x0e79c79f, 0x0441c811,
  0x0dd14908, 0x03994997, 0x0d614a26, 0x05294ab5, 0x0cf14b4c, 0x04b94bdb, 0x0c814c6a, 0x04494cf9,
  0x0dd16908, 0x03996997, 0x0d616a26, 0x05296ab5, 0x0cf16b4c, 0x04b96bdb, 0x0c816c6a, 0x04496cf9,
  0x0ddb0df4, 0x03a30e66, 0x0d6b0ed8, 0x05330f4a, 0x0cfb0fbc, 0x04c3102e, 0x0c8b10a0, 0x04531112,
  0x0dd18974, 0x039989e6, 0x0d618a58, 0x05298aca, 0x0cf18b3c, 0x04b98bae, 0x0c818c20, 0x04498c92,
  0x0bdb2df5, 0x03a32e67, 0x0d6b2ed9, 0x03332f4b, 0x0cfb2fbd, 0x02c3302f, 0x0c8b30a1, 0x02533113,
  0x0bd1a975, 0x0399a9e7, 0x0d61aa59, 0x0329aacb, 0x0cf1ab3d, 0x02b9abaf, 0x0c81ac21, 0x0249ac93,
  0x0a03047e, 0x0203247f, 0x0c0b0880, 0x020b2881, 0x0c130d02, 0x02132d03, 0x0c1b1184, 0x021b3185,
  0x0bd1c97e, 0x0399c9ff, 0x0d61ca00, 0x0329ca81, 0x0cf1cb02, 0x02b9cb83, 0x0c81cc04, 0x0249cc85,
  0x0bd94d89, 0x01a14e18, 0x0b694ea7, 0x01314f3e, 0x0af94fcd, 0x02c1505c, 0x0a8950eb, 0x0251517a,
  0x0a01c409, 0x01c0c018, 0x0a09c8a7, 0x01c0c03e, 0x0a11cd4d, 0x03c0c05c, 0x0a19d1eb, 0x03c0c07a,
  0x0bd96d8a, 0x01a16e19, 0x0b696eb0, 0x01316f3f, 0x0af96fce, 0x02c1705d, 0x0a8970ec, 0x0251717b,
  0x0bd98d8a, 0x01a18e19, 0x0b698eb0, 0x01318f3f, 0x0af98fce, 0x02c1905d, 0x0a8990ec, 0x0251917b,
  0x09d9ad8b, 0x01a1ae22, 0x0969aeb1, 0x0131af40, 0x0af9afcf, 0x00c1b05e, 0x0a89b0ed, 0x0051b17c,
  0x09d9cd8b, 0x01a1ce22, 0x0969ceb1, 0x0131cf40, 0x0af9cfcf, 0x00c1d05e, 0x0a89d0ed, 0x0051d17c,
  0x08014414, 0x00016423, 0x080948b2, 0x000968c1, 0x0a114d50, 0x00116d5f, 0x0a1951f6, 0x001971fd,
  0x08018414, 0x0001a423, 0x080988b2, 0x0009a8c1, 0x0a118d50, 0x0011ad5f, 0x0a1991f6, 0x0019b1fd
};

__global__ __attribute__((aligned(4096))) __launch_bounds__(256, 1) void mega_kernel(const void* x, const void* wr, const void* wi, const void* wg, const void* wu, const void* wd, void* out, void* ws) {
  asm volatile(
    "s_load_dwordx16 s[4:19], s[0:1], 0\n"
    "s_getpc_b64 s[0:1]\n"
    "LM_start:\n"
    "s_add_u32 s34, s0, mega_unit_table@rel32@lo+4\n"
    "s_addc_u32 s35, s1, mega_unit_table@rel32@hi+12\n"
    "s_lshl_b32 s36, s2, 2\n"
    "s_load_dword s38, s[34:35], s36\n"
    "v_and_b32 v1, 63, v0\n"
    "v_lshrrev_b32 v6, 6, v0\n"
    "v_and_b32 v2, 15, v0\n"
    "v_readfirstlane_b32 s32, v6\n"
    "v_lshrrev_b32 v3, 4, v1\n"
    "v_lshlrev_b32 v4, 4, v1\n"
    "s_and_b32 s47, s2, 7\n"
    "s_lshr_b32 s48, s2, 3\n"
    "s_lshl_b32 s68, s48, 2\n"
    "s_add_u32 s68, s68, s32\n"
    "s_lshl_b32 s68, s68, 10\n"
    "s_min_u32 s68, s68, LM_end-LM_start-1024\n"
    "s_add_u32 s0, s0, s68\n"
    "s_addc_u32 s1, s1, 0\n"
    "global_load_dwordx4 v[252:255], v4, s[0:1]\n"
    "s_mov_b32 s39, 0x10000\n"
    "s_mov_b32 s44, 0x80008000\n"
    "s_mov_b32 s45, 0xffbfffbf\n"
    "s_mov_b32 s22, 0x2cb2000\n"
    "s_mov_b32 s23, 0x20000\n"
    "s_mul_i32 s49, s47, 0x59000\n"
    "v_lshrrev_b32 v213, 3, v1\n"
    "v_mul_u32_u24 v213, 0x58f80, v213\n"
    "v_add_u32 v213, v213, v4\n"
    "v_mov_b32 v215, 0x7ffffff0\n"
    "v_cmp_gt_u32 vcc, 64, v0\n"
    "s_nop 1\n"
    "v_cndmask_b32 v215, v215, v213, vcc\n"
    "s_and_b32 s51, s32, 1\n"
    "s_lshl_b32 s50, s51, 1\n"
    "v_bfe_u32 v212, v1, 3, 2\n"
    "v_lshrrev_b32 v211, 5, v1\n"
    "v_lshl_add_u32 v212, v212, 5, v211\n"
    "v_add_u32 v212, s50, v212\n"
    "v_mul_u32_u24 v210, 0x480, v212\n"
    "s_lshl_b32 s50, s51, 2\n"
    "v_add_u32 v212, s50, v3\n"
    "v_and_b32 v211, 7, v1\n"
    "v_xor_b32 v212, v212, v211\n"
    "v_lshl_add_u32 v210, v212, 4, v210\n"
    "s_lshr_b32 s56, s32, 1\n"
    "s_lshl_b32 s57, s56, 7\n"
    "s_lshl_b32 s58, s51, 10\n"
    "s_add_u32 s58, s58, 0x8000\n"
    "s_lshl_b32 s56, s56, 11\n"
    "s_add_u32 s56, s56, s58\n"
    "s_mov_b32 s54, 0x240000\n"
    "s_mov_b32 s55, 0x20000\n"
    "s_waitcnt lgkmcnt(0)\n"
    "s_and_b32 s33, s38, 0x7f\n"
    "s_mov_b64 s[20:21], s[18:19]\n"
    "s_load_dword s37, s[18:19], 0\n"
    "s_and_b32 s50, s48, 7\n"
    "s_lshr_b32 s50, 0xa8, s50\n"
    "s_bitcmp1_b32 s50, 0\n"
    "s_cbranch_scc1 LM_B_1\n"
    "s_mov_b64 s[52:53], s[4:5]\n"
    "s_lshr_b32 s50, s33, 3\n"
    "s_lshl_b32 s50, s50, 5\n"
    "s_and_b32 s51, s33, 7\n"
    "s_add_u32 s50, s50, s51\n"
    "s_mul_i32 s50, s50, 0x1200\n"
    "s_add_u32 s57, s57, s50\n"
    "s_add_u32 m0, s56, 0\n"
    "s_nop 0\n"
    "buffer_load_dwordx4 v210, s[52:55], s57 offen offset:0 lds\n"
    "s_add_u32 m0, s56, 0xf00\n"
    "s_nop 0\n"
    "buffer_load_dwordx4 v210, s[52:55], s57 offen offset:256 lds\n"
    "s_add_u32 m0, s56, 0x1e00\n"
    "s_nop 0\n"
    "buffer_load_dwordx4 v210, s[52:55], s57 offen offset:512 lds\n"
    "s_add_u32 m0, s56, 0x2d00\n"
    "s_nop 0\n"
    "buffer_load_dwordx4 v210, s[52:55], s57 offen offset:768 lds\n"
    "s_add_u32 m0, s58, 0x3c00\n"
    "s_nop 0\n"
    "buffer_load_dwordx4 v210, s[52:55], s50 offen offset:1024 lds\n"
    "s_bitcmp1_b32 s48, 0\n"
    "s_cbranch_scc1 LM_B_1\n"
    "s_and_b32 s55, s33, 15\n"
    "s_lshr_b32 s56, s33, 4\n"
    "s_lshl_b32 s57, s56, 2\n"
    "s_add_u32 s57, s57, s32\n"
    "s_lshl_b32 s57, s57, 4\n"
    "s_add_u32 s57, s57, s55\n"
    "s_mul_i32 s57, s57, 0x908\n"
    "s_mov_b64 s[60:61], s[6:7]\n"
    "s_mov_b32 s62, 0x121000\n"
    "s_mov_b32 s63, 0x20000\n"
    "s_mov_b64 s[64:65], s[8:9]\n"
    "s_mov_b32 s66, 0x121000\n"
    "s_mov_b32 s67, 0x20000\n"
    "s_mul_i32 s58, s32, 0x1800\n"
    "v_cmp_gt_u32 vcc, 17, v1\n"
    "v_mov_b32 v213, 0x7ffffff0\n"
    "s_nop 0\n"
    "v_cndmask_b32 v213, v213, v4, vcc\n"
    "s_add_u32 m0, s58, 0x10000\n"
    "s_nop 0\n"
    "buffer_load_dwordx4 v4, s[60:63], s57 offen offset:0 lds\n"
    "buffer_load_dwordx4 v4, s[60:63], s57 offen offset:1024 lds\n"
    "buffer_load_dwordx4 v213, s[60:63], s57 offen offset:2048 lds\n"
    "s_add_u32 m0, s58, 0x10c00\n"
    "s_nop 0\n"
    "buffer_load_dwordx4 v4, s[64:67], s57 offen offset:0 lds\n"
    "buffer_load_dwordx4 v4, s[64:67], s57 offen offset:1024 lds\n"
    "buffer_load_dwordx4 v213, s[64:67], s57 offen offset:2048 lds\n"
    "v_lshrrev_b32 v212, 2, v1\n"
    "v_lshlrev_b32 v212, 14, v212\n"
    "v_and_b32 v211, 3, v1\n"
    "v_lshl_add_u32 v212, v211, 4, v212\n"
    "s_bfe_u32 s57, s33, 0x10002\n"
    "s_lshl_b32 s57, s57, 12\n"
    "s_lshr_b32 s58, s33, 3\n"
    "s_lshl_b32 s58, s58, 8\n"
    "s_add_u32 s57, s57, s58\n"
    "s_and_b32 s58, s33, 3\n"
    "s_lshl_b32 s58, s58, 6\n"
    "s_add_u32 s57, s57, s58\n"
    "s_lshr_b32 s58, s32, 1\n"
    "s_lshl_b32 s58, s58, 18\n"
    "s_add_u32 s57, s57, s58\n"
    "s_and_b32 s58, s32, 1\n"
    "s_lshl_b32 s58, s58, 13\n"
    "s_add_u32 s57, s57, s58\n"
    "s_add_u32 s59, s57, 0x2e0000\n"
    "s_add_u32 s60, s57, 0x360000\n"
    "s_add_u32 s61, s57, 0x3e0000\n"
    "s_add_u32 s62, s57, 0x460000\n"
    "s_add_u32 s63, s57, 0x4e0000\n"
    "s_lshr_b32 s50, s32, 1\n"
    "v_lshl_add_u32 v213, v3, 1, s50\n"
    "v_lshrrev_b32 v211, 1, v2\n"
    "v_xor_b32 v213, v213, v211\n"
    "v_lshlrev_b32 v213, 4, v213\n"
    "s_and_b32 s51, s32, 1\n"
    "s_lshl_b32 s51, s51, 3\n"
    "v_lshl_add_u32 v210, v2, 7, v213\n"
    "v_add_u32 v210, s51, v210\n"
    "v_lshrrev_b32 v209, 1, v2\n"
    "v_cvt_f32_u32 v209, v209\n"
    "v_and_b32 v208, 1, v2\n"
    "v_cvt_f32_u32 v207, v208\n"
    "v_mul_f32 v207, 0x3e800000, v207\n"
    "v_cvt_f32_u32 v206, v3\n"
    "v_mul_lo_u32 v211, v208, s44\n"
    "v_xor_b32 v205, s44, v211\n"
    "v_mul_f32 v204, 0x3ce38e39, v209\n"
    "v_add_f32 v203, 0, v204\n"
    "v_fma_f32 v213, v203, v206, v207\n"
    "v_cos_f32 v8, v213\n"
    "v_fmamk_f32 v9, v203, 0x40800000, v213\n"
    "v_cos_f32 v9, v9\n"
    "v_fmamk_f32 v10, v203, 0x41000000, v213\n"
    "v_cos_f32 v10, v10\n"
    "v_fmamk_f32 v11, v203, 0x41400000, v213\n"
    "v_cos_f32 v11, v11\n"
    "v_fmamk_f32 v12, v203, 0x41800000, v213\n"
    "v_cos_f32 v12, v12\n"
    "v_fmamk_f32 v13, v203, 0x41a00000, v213\n"
    "v_cos_f32 v13, v13\n"
    "v_fmamk_f32 v14, v203, 0x41c00000, v213\n"
    "v_cos_f32 v14, v14\n"
    "v_fmamk_f32 v15, v203, 0x41e00000, v213\n"
    "v_cos_f32 v15, v15\n"
    "v_fmamk_f32 v16, v203, 0x42000000, v213\n"
    "v_cos_f32 v16, v16\n"
    "v_cvt_pk_bf16_f32 v20, v8, v9\n"
    "v_cvt_pk_bf16_f32 v21, v10, v11\n"
    "v_cvt_pk_bf16_f32 v22, v12, v13\n"
    "v_cvt_pk_bf16_f32 v23, v14, v15\n"
    "v_cvt_pk_bf16_f32 v24, v16, 0\n"
    "v_mov_b32 v25, 0\n"
    "v_mov_b32 v26, 0\n"
    "v_mov_b32 v27, 0\n"
    "v_add_f32 v208, 0x3e638e39, v204\n"
    "v_fma_f32 v202, v208, v206, v207\n"
    "v_cos_f32 v28, v202\n"
    "v_fmamk_f32 v29, v208, 0x40800000, v202\n"
    "v_cos_f32 v29, v29\n"
    "v_fmamk_f32 v30, v208, 0x41000000, v202\n"
    "v_cos_f32 v30, v30\n"
    "v_fmamk_f32 v31, v208, 0x41400000, v202\n"
    "v_cos_f32 v31, v31\n"
    "v_fmamk_f32 v32, v208, 0x41800000, v202\n"
    "v_cos_f32 v32, v32\n"
    "v_fmamk_f32 v33, v208, 0x41a00000, v202\n"
    "v_cos_f32 v33, v33\n"
    "v_fmamk_f32 v34, v208, 0x41c00000, v202\n"
    "v_cos_f32 v34, v34\n"
    "v_fmamk_f32 v35, v208, 0x41e00000, v202\n"
    "v_cos_f32 v35, v35\n"
    "v_fmamk_f32 v36, v208, 0x42000000, v202\n"
    "v_cos_f32 v36, v36\n"
    "v_cvt_pk_bf16_f32 v8, v28, v29\n"
    "v_cvt_pk_bf16_f32 v9, v30, v31\n"
    "v_cvt_pk_bf16_f32 v10, v32, v33\n"
    "v_cvt_pk_bf16_f32 v11, v34, v35\n"
    "v_cvt_pk_bf16_f32 v12, v36, 0\n"
    "v_mov_b32 v13, 0\n"
    "v_mov_b32 v14, 0\n"
    "v_mov_b32 v15, 0\n"
    "v_add_f32 v213, 0x3ee38e39, v204\n"
    "v_fma_f32 v203, v213, v206, v207\n"
    "v_cos_f32 v40, v203\n"
    "v_fmamk_f32 v41, v213, 0x40800000, v203\n"
    "v_cos_f32 v41, v41\n"
    "v_fmamk_f32 v42, v213, 0x41000000, v203\n"
    "v_cos_f32 v42, v42\n"
    "v_fmamk_f32 v43, v213, 0x41400000, v203\n"
    "v_cos_f32 v43, v43\n"
    "v_fmamk_f32 v44, v213, 0x41800000, v203\n"
    "v_cos_f32 v44, v44\n"
    "v_fmamk_f32 v45, v213, 0x41a00000, v203\n"
    "v_cos_f32 v45, v45\n"
    "v_fmamk_f32 v46, v213, 0x41c00000, v203\n"
    "v_cos_f32 v46, v46\n"
    "v_fmamk_f32 v47, v213, 0x41e00000, v203\n"
    "v_cos_f32 v47, v47\n"
    "v_fmamk_f32 v48, v213, 0x42000000, v203\n"
    "v_cos_f32 v48, v48\n"
    "v_cvt_pk_bf16_f32 v16, v40, v41\n"
    "v_cvt_pk_bf16_f32 v17, v42, v43\n"
    "v_cvt_pk_bf16_f32 v18, v44, v45\n"
    "v_cvt_pk_bf16_f32 v19, v46, v47\n"
    "v_cvt_pk_bf16_f32 v28, v48, 0\n"
    "v_mov_b32 v29, 0\n"
    "v_mov_b32 v30, 0\n"
    "v_mov_b32 v31, 0\n"
    "v_cvt_f32_u32 v208, s32\n"
    "v_mul_f32 v208, 0x3ae38e39, v208\n"
    "v_fmamk_f32 v208, v209, 0x3d800000, v208\n"
    "v_mul_f32 v213, 4.0, v206\n"
    "v_add_f32 v204, 0, v208\n"
    "v_fma_f32 v32, v204, v213, v207\n"
    "v_fmamk_f32 v33, v204, 0x3f800000, v32\n"
    "v_fmamk_f32 v34, v204, 0x40000000, v32\n"
    "v_fmamk_f32 v35, v204, 0x40400000, v32\n"
    "v_sin_f32 v36, v32\n"
    "v_cos_f32 v40, v32\n"
    "v_sin_f32 v37, v33\n"
    "v_cvt_pk_bf16_f32 v44, v40, v36\n"
    "v_xor_b32 v48, v44, v211\n"
    "v_cos_f32 v41, v33\n"
    "v_sin_f32 v38, v34\n"
    "v_cvt_pk_bf16_f32 v45, v41, v37\n"
    "v_xor_b32 v49, v45, v205\n"
    "v_cos_f32 v42, v34\n"
    "v_sin_f32 v39, v35\n"
    "v_cvt_pk_bf16_f32 v46, v42, v38\n"
    "v_xor_b32 v50, v46, v211\n"
    "v_cos_f32 v43, v35\n"
    "v_add_f32 v209, 0x3be38e39, v208\n"
    "v_fma_f32 v52, v209, v213, v207\n"
    "v_cvt_pk_bf16_f32 v47, v43, v39\n"
    "v_xor_b32 v51, v47, v205\n"
    "v_fmamk_f32 v53, v209, 0x3f800000, v52\n"
    "v_fmamk_f32 v54, v209, 0x40000000, v52\n"
    "v_fmamk_f32 v55, v209, 0x40400000, v52\n"
    "v_sin_f32 v56, v52\n"
    "v_cos_f32 v60, v52\n"
    "v_sin_f32 v57, v53\n"
    "v_cvt_pk_bf16_f32 v64, v60, v56\n"
    "v_xor_b32 v32, v64, v211\n"
    "v_cos_f32 v61, v53\n"
    "v_sin_f32 v58, v54\n"
    "v_cvt_pk_bf16_f32 v65, v61, v57\n"
    "v_xor_b32 v33, v65, v205\n"
    "v_cos_f32 v62, v54\n"
    "v_sin_f32 v59, v55\n"
    "v_cvt_pk_bf16_f32 v66, v62, v58\n"
    "v_xor_b32 v34, v66, v211\n"
    "v_cos_f32 v63, v55\n"
    "v_add_f32 v209, 0x3c638e39, v208\n"
    "v_fma_f32 v36, v209, v213, v207\n"
    "v_cvt_pk_bf16_f32 v67, v63, v59\n"
    "v_xor_b32 v35, v67, v205\n"
    "v_fmamk_f32 v37, v209, 0x3f800000, v36\n"
    "v_fmamk_f32 v38, v209, 0x40000000, v36\n"
    "v_fmamk_f32 v39, v209, 0x40400000, v36\n"
    "v_sin_f32 v40, v36\n"
    "v_cos_f32 v68, v36\n"
    "v_sin_f32 v41, v37\n"
    "v_cvt_pk_bf16_f32 v72, v68, v40\n"
    "v_xor_b32 v52, v72, v211\n"
    "v_cos_f32 v69, v37\n"
    "v_sin_f32 v42, v38\n"
    "v_cvt_pk_bf16_f32 v73, v69, v41\n"
    "v_xor_b32 v53, v73, v205\n"
    "v_cos_f32 v70, v38\n"
    "v_sin_f32 v43, v39\n"
    "v_cvt_pk_bf16_f32 v74, v70, v42\n"
    "v_xor_b32 v54, v74, v211\n"
    "v_cos_f32 v71, v39\n"
    "v_add_f32 v209, 0x3caaaaab, v208\n"
    "v_fma_f32 v56, v209, v213, v207\n"
    "v_cvt_pk_bf16_f32 v75, v71, v43\n"
    "v_xor_b32 v55, v75, v205\n"
    "v_fmamk_f32 v57, v209, 0x3f800000, v56\n"
    "v_fmamk_f32 v58, v209, 0x40000000, v56\n"
    "v_fmamk_f32 v59, v209, 0x40400000, v56\n"
    "v_sin_f32 v60, v56\n"
    "v_cos_f32 v76, v56\n"
    "v_sin_f32 v61, v57\n"
    "v_cvt_pk_bf16_f32 v80, v76, v60\n"
    "v_xor_b32 v36, v80, v211\n"
    "v_cos_f32 v77, v57\n"
    "v_sin_f32 v62, v58\n"
    "v_cvt_pk_bf16_f32 v81, v77, v61\n"
    "v_xor_b32 v37, v81, v205\n"
    "v_cos_f32 v78, v58\n"
    "v_sin_f32 v63, v59\n"
    "v_cvt_pk_bf16_f32 v82, v78, v62\n"
    "v_xor_b32 v38, v82, v211\n"
    "v_cos_f32 v79, v59\n"
    "v_add_f32 v209, 0x3ce38e39, v208\n"
    "v_fma_f32 v40, v209, v213, v207\n"
    "v_cvt_pk_bf16_f32 v83, v79, v63\n"
    "v_xor_b32 v39, v83, v205\n"
    "v_fmamk_f32 v41, v209, 0x3f800000, v40\n"
    "v_fmamk_f32 v42, v209, 0x40000000, v40\n"
    "v_fmamk_f32 v43, v209, 0x40400000, v40\n"
    "v_sin_f32 v68, v40\n"
    "v_cos_f32 v84, v40\n"
    "v_sin_f32 v69, v41\n"
    "v_cvt_pk_bf16_f32 v88, v84, v68\n"
    "v_xor_b32 v56, v88, v211\n"
    "v_cos_f32 v85, v41\n"
    "v_sin_f32 v70, v42\n"
    "v_cvt_pk_bf16_f32 v89, v85, v69\n"
    "v_xor_b32 v57, v89, v205\n"
    "v_cos_f32 v86, v42\n"
    "v_sin_f32 v71, v43\n"
    "v_cvt_pk_bf16_f32 v90, v86, v70\n"
    "v_xor_b32 v58, v90, v211\n"
    "v_cos_f32 v87, v43\n"
    "v_mov_b32 v61, 0\n"
    "v_mov_b32 v62, 0\n"
    "v_cvt_pk_bf16_f32 v91, v87, v71\n"
    "v_xor_b32 v59, v91, v205\n"
    "v_mov_b32 v63, 0\n"
    "v_mov_b32 v77, 0\n"
    "v_mov_b32 v78, 0\n"
    "v_mov_b32 v79, 0\n"
    "v_mov_b32 v93, 0\n"
    "v_mov_b32 v94, 0\n"
    "v_mov_b32 v95, 0\n"
    "v_mov_b32 v41, 0\n"
    "v_mov_b32 v42, 0\n"
    "v_mov_b32 v43, 0\n"
    "s_lshl_b32 s58, s32, 10\n"
    "v_lshlrev_b32 v96, 7, v3\n"
    "v_lshl_add_u32 v96, v2, 2, v96\n"
    "v_add_u32 v96, s58, v96\n"
    "v_add_u32 v97, 0x1000, v96\n"
    "v_add_u32 v98, 0x2000, v96\n"
    "v_add_u32 v99, 0x3000, v96\n"
    "v_add_u32 v100, 0x4000, v96\n"
    "s_lshl_b32 s67, s33, 7\n"
    "s_add_u32 s67, s67, 0x1000\n"
    "s_cmp_eq_u32 s32, 0\n"
    "s_cselect_b32 s64, 0x42100000, 0\n"
    "s_lshl_b32 s58, s32, 8\n"
    "v_lshlrev_b32 v213, 11, v3\n"
    "v_lshl_add_u32 v213, v2, 4, v213\n"
    "v_add_u32 v213, s58, v213\n"
    "s_lshl_b32 s65, s32, 7\n"
    "v_lshl_add_u32 v211, v2, 3, s65\n"
    "s_lshl_b32 s66, s32, 10\n"
    "v_add_u32 v209, s66, v4\n"
    "s_waitcnt lgkmcnt(0)\n"
    "s_mul_i32 s40, s37, 0x9e3779b1\n"
    "s_add_u32 s40, s40, 0x7f4a7c15\n"
    "s_mul_i32 s41, s37, 0x85ebca6b\n"
    "s_xor_b32 s41, s41, 0xc2b2ae35\n"
    "v_mov_b32 v218, s40\n"
    "v_mov_b32 v219, s41\n"
    "v_mov_b32 v220, s40\n"
    "v_mov_b32 v221, s41\n"
    "s_mov_b64 exec, 0\n"
    "s_branch LS_first\n"
    "LS_back:\n"
    "s_mov_b64 exec, -1\n"
    "s_waitcnt vmcnt(6)\n"
    "s_barrier\n"
    "ds_read_b64 v[104:105], v210 offset:32768\n"
    "ds_read_b64 v[106:107], v210 offset:34816\n"
    "ds_read_b64 v[108:109], v210 offset:36864\n"
    "ds_read_b64 v[110:111], v210 offset:38912\n"
    "ds_read_b64 v[112:113], v210 offset:40960\n"
    "ds_read_b64 v[114:115], v210 offset:43008\n"
    "ds_read_b64 v[116:117], v210 offset:45056\n"
    "ds_read_b64 v[118:119], v210 offset:47104\n"
    "ds_read_b64 v[120:121], v210 offset:49152\n"
    "s_waitcnt lgkmcnt(8)\n"
    "v_lshlrev_b32 v124, 16, v104\n"
    "v_and_b32 v125, 0xffff0000, v104\n"
    "v_lshlrev_b32 v126, 16, v105\n"
    "v_and_b32 v127, 0xffff0000, v105\n"
    "s_waitcnt lgkmcnt(7)\n"
    "v_lshlrev_b32 v128, 16, v106\n"
    "v_and_b32 v129, 0xffff0000, v106\n"
    "v_lshlrev_b32 v130, 16, v107\n"
    "v_and_b32 v131, 0xffff0000, v107\n"
    "s_waitcnt lgkmcnt(6)\n"
    "v_lshlrev_b32 v132, 16, v108\n"
    "v_and_b32 v133, 0xffff0000, v108\n"
    "v_lshlrev_b32 v134, 16, v109\n"
    "v_and_b32 v135, 0xffff0000, v109\n"
    "s_waitcnt lgkmcnt(5)\n"
    "v_lshlrev_b32 v136, 16, v110\n"
    "v_and_b32 v137, 0xffff0000, v110\n"
    "v_lshlrev_b32 v138, 16, v111\n"
    "v_and_b32 v139, 0xffff0000, v111\n"
    "s_waitcnt lgkmcnt(4)\n"
    "v_lshlrev_b32 v140, 16, v112\n"
    "v_and_b32 v141, 0xffff0000, v112\n"
    "v_lshlrev_b32 v142, 16, v113\n"
    "v_and_b32 v143, 0xffff0000, v113\n"
    "s_waitcnt lgkmcnt(3)\n"
    "v_lshlrev_b32 v144, 16, v114\n"
    "v_and_b32 v145, 0xffff0000, v114\n"
    "v_lshlrev_b32 v146, 16, v115\n"
    "v_and_b32 v147, 0xffff0000, v115\n"
    "s_waitcnt lgkmcnt(2)\n"
    "v_lshlrev_b32 v148, 16, v116\n"
    "v_and_b32 v149, 0xffff0000, v116\n"
    "v_lshlrev_b32 v150, 16, v117\n"
    "v_and_b32 v151, 0xffff0000, v117\n"
    "s_waitcnt lgkmcnt(1)\n"
    "v_lshlrev_b32 v152, 16, v118\n"
    "v_and_b32 v153, 0xffff0000, v118\n"
    "v_lshlrev_b32 v154, 16, v119\n"
    "v_and_b32 v155, 0xffff0000, v119\n"
    "s_waitcnt lgkmcnt(0)\n"
    "v_lshlrev_b32 v156, 16, v120\n"
    "v_and_b32 v157, 0xffff0000, v120\n"
    "v_lshlrev_b32 v158, 16, v121\n"
    "v_and_b32 v159, 0xffff0000, v121\n"
    "v_pk_add_f32 v[206:207], v[124:125], v[126:127]\n"
    "v_pk_add_f32 v[206:207], v[206:207], v[128:129]\n"
    "v_pk_add_f32 v[206:207], v[206:207], v[130:131]\n"
    "v_pk_add_f32 v[206:207], v[206:207], v[132:133]\n"
    "v_pk_add_f32 v[206:207], v[206:207], v[134:135]\n"
    "v_pk_add_f32 v[204:205], v[136:137], v[138:139]\n"
    "v_pk_add_f32 v[204:205], v[204:205], v[140:141]\n"
    "v_pk_add_f32 v[204:205], v[204:205], v[142:143]\n"
    "v_pk_add_f32 v[204:205], v[204:205], v[144:145]\n"
    "v_pk_add_f32 v[204:205], v[204:205], v[146:147]\n"
    "v_pk_add_f32 v[202:203], v[148:149], v[150:151]\n"
    "v_pk_add_f32 v[202:203], v[202:203], v[152:153]\n"
    "v_pk_add_f32 v[202:203], v[202:203], v[154:155]\n"
    "v_pk_add_f32 v[202:203], v[202:203], v[156:157]\n"
    "v_pk_add_f32 v[202:203], v[202:203], v[158:159]\n"
    "v_pk_add_f32 v[206:207], v[206:207], v[204:205]\n"
    "v_pk_add_f32 v[206:207], v[206:207], v[202:203]\n"
    "v_add_f32 v210, v206, v207\n"
    "v_mov_b32 v208, v210\n"
    "s_nop 1\n"
    "v_permlane16_swap_b32 v210, v208\n"
    "v_add_f32 v210, v210, v208\n"
    "v_mov_b32 v208, v210\n"
    "s_nop 1\n"
    "v_permlane32_swap_b32 v210, v208\n"
    "v_add_f32 v210, v210, v208\n"
    "v_mul_f32 v200, 0x3be38e39, v210\n"
    "v_pk_add_f32 v[124:125], v[124:125], v[200:201] op_sel_hi:[1,0] neg_lo:[0,1] neg_hi:[0,1]\n"
    "v_pk_add_f32 v[126:127], v[126:127], v[200:201] op_sel_hi:[1,0] neg_lo:[0,1] neg_hi:[0,1]\n"
    "v_pk_add_f32 v[128:129], v[128:129], v[200:201] op_sel_hi:[1,0] neg_lo:[0,1] neg_hi:[0,1]\n"
    "v_pk_add_f32 v[130:131], v[130:131], v[200:201] op_sel_hi:[1,0] neg_lo:[0,1] neg_hi:[0,1]\n"
    "v_pk_add_f32 v[132:133], v[132:133], v[200:201] op_sel_hi:[1,0] neg_lo:[0,1] neg_hi:[0,1]\n"
    "v_pk_add_f32 v[134:135], v[134:135], v[200:201] op_sel_hi:[1,0] neg_lo:[0,1] neg_hi:[0,1]\n"
    "v_pk_add_f32 v[136:137], v[136:137], v[200:201] op_sel_hi:[1,0] neg_lo:[0,1] neg_hi:[0,1]\n"
    "v_pk_add_f32 v[138:139], v[138:139], v[200:201] op_sel_hi:[1,0] neg_lo:[0,1] neg_hi:[0,1]\n"
    "v_pk_add_f32 v[140:141], v[140:141], v[200:201] op_sel_hi:[1,0] neg_lo:[0,1] neg_hi:[0,1]\n"
    "v_pk_add_f32 v[142:143], v[142:143], v[200:201] op_sel_hi:[1,0] neg_lo:[0,1] neg_hi:[0,1]\n"
    "v_pk_add_f32 v[144:145], v[144:145], v[200:201] op_sel_hi:[1,0] neg_lo:[0,1] neg_hi:[0,1]\n"
    "v_pk_add_f32 v[146:147], v[146:147], v[200:201] op_sel_hi:[1,0] neg_lo:[0,1] neg_hi:[0,1]\n"
    "v_pk_add_f32 v[148:149], v[148:149], v[200:201] op_sel_hi:[1,0] neg_lo:[0,1] neg_hi:[0,1]\n"
    "v_pk_add_f32 v[150:151], v[150:151], v[200:201] op_sel_hi:[1,0] neg_lo:[0,1] neg_hi:[0,1]\n"
    "v_pk_add_f32 v[152:153], v[152:153], v[200:201] op_sel_hi:[1,0] neg_lo:[0,1] neg_hi:[0,1]\n"
    "v_pk_add_f32 v[154:155], v[154:155], v[200:201] op_sel_hi:[1,0] neg_lo:[0,1] neg_hi:[0,1]\n"
    "v_pk_add_f32 v[156:157], v[156:157], v[200:201] op_sel_hi:[1,0] neg_lo:[0,1] neg_hi:[0,1]\n"
    "v_pk_add_f32 v[158:159], v[158:159], v[200:201] op_sel_hi:[1,0] neg_lo:[0,1] neg_hi:[0,1]\n"
    "v_cvt_pk_bf16_f32 v68, v124, v128\n"
    "v_cvt_pk_bf16_f32 v69, v132, v136\n"
    "v_cvt_pk_bf16_f32 v70, v140, v144\n"
    "v_cvt_pk_bf16_f32 v71, v148, v152\n"
    "v_cvt_pk_bf16_f32 v60, v156, 0\n"
    "v_cvt_pk_bf16_f32 v84, v125, v129\n"
    "v_mfma_f32_16x16x32_bf16 v[104:107], v[20:23], v[68:71], 0\n"
    "v_cvt_pk_bf16_f32 v85, v133, v137\n"
    "v_cvt_pk_bf16_f32 v86, v141, v145\n"
    "v_mfma_f32_16x16x32_bf16 v[104:107], v[24:27], v[60:63], v[104:107]\n"
    "v_cvt_pk_bf16_f32 v87, v149, v153\n"
    "v_cvt_pk_bf16_f32 v76, v157, 0\n"
    "v_mfma_f32_16x16x32_bf16 v[108:111], v[8:11], v[68:71], 0\n"
    "v_cvt_pk_bf16_f32 v112, v126, v130\n"
    "v_cvt_pk_bf16_f32 v113, v134, v138\n"
    "v_mfma_f32_16x16x32_bf16 v[116:119], v[20:23], v[84:87], 0\n"
    "v_cvt_pk_bf16_f32 v114, v142, v146\n"
    "v_cvt_pk_bf16_f32 v115, v150, v154\n"
    "v_mfma_f32_16x16x32_bf16 v[116:119], v[24:27], v[76:79], v[116:119]\n"
    "v_cvt_pk_bf16_f32 v92, v158, 0\n"
    "v_cvt_pk_bf16_f32 v120, v127, v131\n"
    "v_mfma_f32_16x16x32_bf16 v[160:163], v[20:23], v[112:115], 0\n"
    "v_cvt_pk_bf16_f32 v121, v135, v139\n"
    "v_cvt_pk_bf16_f32 v122, v143, v147\n"
    "v_mfma_f32_16x16x32_bf16 v[160:163], v[24:27], v[92:95], v[160:163]\n"
    "v_cvt_pk_bf16_f32 v123, v151, v155\n"
    "v_cvt_pk_bf16_f32 v40, v159, 0\n"
    "v_mfma_f32_16x16x32_bf16 v[108:111], v[12:15], v[60:63], v[108:111]\n"
    "v_cvt_pk_bf16_f32 v164, v104, v105\n"
    "v_cvt_pk_bf16_f32 v168, v106, v107\n"
    "v_mfma_f32_16x16x32_bf16 v[172:175], v[20:23], v[120:123], 0\n"
    "v_cvt_pk_bf16_f32 v165, v116, v117\n"
    "v_cvt_pk_bf16_f32 v169, v118, v119\n"
    "v_mfma_f32_16x16x32_bf16 v[172:175], v[24:27], v[40:43], v[172:175]\n"
    "v_cvt_pk_bf16_f32 v166, v160, v161\n"
    "v_cvt_pk_bf16_f32 v170, v162, v163\n"
    "v_mfma_f32_16x16x32_bf16 v[176:179], v[8:11], v[84:87], 0\n"
    "v_cvt_pk_bf16_f32 v180, v108, v109\n"
    "v_cvt_pk_bf16_f32 v184, v110, v111\n"
    "v_mfma_f32_16x16x32_bf16 v[176:179], v[12:15], v[76:79], v[176:179]\n"
    "v_mul_f32 v188, v124, v124\n"
    "s_nop 0\n"
    "v_cvt_pk_bf16_f32 v167, v172, v173\n"
    "ds_write_b128 v213, v[164:167] offset:0\n"
    "v_mfma_f32_16x16x32_bf16 v[20:23], v[8:11], v[112:115], 0\n"
    "v_cvt_pk_bf16_f32 v171, v174, v175\n"
    "v_fmac_f32 v188, v128, v128\n"
    "v_mfma_f32_16x16x32_bf16 v[20:23], v[12:15], v[92:95], v[20:23]\n"
    "v_cvt_pk_bf16_f32 v181, v176, v177\n"
    "v_cvt_pk_bf16_f32 v185, v178, v179\n"
    "v_mfma_f32_16x16x32_bf16 v[24:27], v[8:11], v[120:123], 0\n"
    "v_fmac_f32 v188, v132, v132\n"
    "v_fmac_f32 v188, v136, v136\n"
    "ds_write_b128 v213, v[168:171] offset:1024\n"
    "v_mfma_f32_16x16x32_bf16 v[24:27], v[12:15], v[40:43], v[24:27]\n"
    "s_nop 0\n"
    "v_cvt_pk_bf16_f32 v182, v20, v21\n"
    "v_cvt_pk_bf16_f32 v186, v22, v23\n"
    "v_mfma_f32_16x16x32_bf16 v[104:107], v[16:19], v[68:71], 0\n"
    "v_fmac_f32 v188, v140, v140\n"
    "v_fmac_f32 v188, v144, v144\n"
    "v_mfma_f32_16x16x32_bf16 v[104:107], v[28:31], v[60:63], v[104:107]\n"
    "v_fmac_f32 v188, v148, v148\n"
    "v_cvt_pk_bf16_f32 v183, v24, v25\n"
    "ds_write_b128 v213, v[180:183] offset:8192\n"
    "v_mfma_f32_16x16x32_bf16 v[8:11], v[16:19], v[84:87], 0\n"
    "v_cvt_pk_bf16_f32 v187, v26, v27\n"
    "v_fmac_f32 v188, v152, v152\n"
    "v_mfma_f32_16x16x32_bf16 v[8:11], v[28:31], v[76:79], v[8:11]\n"
    "s_nop 0\n"
    "v_cvt_pk_bf16_f32 v12, v104, v105\n"
    "v_cvt_pk_bf16_f32 v20, v106, v107\n"
    "v_mfma_f32_16x16x32_bf16 v[68:71], v[16:19], v[112:115], 0\n"
    "v_fmac_f32 v188, v156, v156\n"
    "v_mul_f32 v189, v125, v125\n"
    "ds_write_b128 v213, v[184:187] offset:9216\n"
    "v_mfma_f32_16x16x32_bf16 v[68:71], v[28:31], v[92:95], v[68:71]\n"
    "v_cvt_pk_bf16_f32 v13, v8, v9\n"
    "v_cvt_pk_bf16_f32 v21, v10, v11\n"
    "v_mfma_f32_16x16x32_bf16 v[24:27], v[16:19], v[120:123], 0\n"
    "v_fmac_f32 v189, v129, v129\n"
    "v_fmac_f32 v189, v133, v133\n"
    "v_mfma_f32_16x16x32_bf16 v[24:27], v[28:31], v[40:43], v[24:27]\n"
    "v_fmac_f32 v189, v137, v137\n"
    "s_nop 0\n"
    "v_cvt_pk_bf16_f32 v14, v68, v69\n"
    "v_cvt_pk_bf16_f32 v22, v70, v71\n"
    "v_fmac_f32 v189, v141, v141\n"
    "v_fmac_f32 v189, v145, v145\n"
    "v_fmac_f32 v189, v149, v149\n"
    "v_fmac_f32 v189, v153, v153\n"
    "v_fmac_f32 v189, v157, v157\n"
    "v_mul_f32 v190, v126, v126\n"
    "v_cvt_pk_bf16_f32 v15, v24, v25\n"
    "ds_write_b128 v213, v[12:15] offset:16384\n"
    "v_cvt_pk_bf16_f32 v23, v26, v27\n"
    "v_fmac_f32 v190, v130, v130\n"
    "v_fmac_f32 v190, v134, v134\n"
    "v_fmac_f32 v190, v138, v138\n"
    "v_fmac_f32 v190, v142, v142\n"
    "v_fmac_f32 v190, v146, v146\n"
    "v_fmac_f32 v190, v150, v150\n"
    "v_fmac_f32 v190, v154, v154\n"
    "v_fmac_f32 v190, v158, v158\n"
    "v_mul_f32 v191, v127, v127\n"
    "v_fmac_f32 v191, v131, v131\n"
    "v_fmac_f32 v191, v135, v135\n"
    "ds_write_b128 v213, v[20:23] offset:17408\n"
    "v_fmac_f32 v191, v139, v139\n"
    "v_fmac_f32 v191, v143, v143\n"
    "v_fmac_f32 v191, v147, v147\n"
    "v_fmac_f32 v191, v151, v151\n"
    "v_fmac_f32 v191, v155, v155\n"
    "v_fmac_f32 v191, v159, v159\n"
    "v_add_f32 v188, v188, v189\n"
    "v_add_f32 v190, v190, v191\n"
    "v_add_f32 v210, v188, v190\n"
    "v_mov_b32 v208, v210\n"
    "s_nop 1\n"
    "v_permlane16_swap_b32 v210, v208\n"
    "v_add_f32 v210, v210, v208\n"
    "v_mov_b32 v208, v210\n"
    "s_nop 1\n"
    "v_permlane32_swap_b32 v210, v208\n"
    "v_add_f32 v210, v210, v208\n"
    "v_mov_b32 v201, v210\n"
    "ds_write_b64 v211, v[200:201] offset:24576\n"
    "s_waitcnt vmcnt(0) lgkmcnt(0)\n"
    "s_barrier\n"
    "v_lshlrev_b32 v213, 3, v2\n"
    "v_mov_b32 v207, 0x3727c5ac\n"
    "v_lshlrev_b32 v206, 3, v1\n"
    "buffer_store_dwordx4 v[218:221], v215, s[20:23], s67 offen sc1\n"
    "ds_read_b64 v[204:205], v213 offset:24576\n"
    "ds_read_b64 v[202:203], v213 offset:24704\n"
    "ds_read_b64 v[198:199], v213 offset:24832\n"
    "ds_read_b64 v[196:197], v213 offset:24960\n"
    "ds_read_b32 v210, v206 offset:24576\n"
    "ds_read_b128 v[8:11], v209 offset:0\n"
    "ds_read_b128 v[12:15], v209 offset:4096\n"
    "ds_read_b128 v[16:19], v209 offset:8192\n"
    "ds_read_b128 v[20:23], v209 offset:12288\n"
    "ds_read_b128 v[24:27], v209 offset:16384\n"
    "s_waitcnt lgkmcnt(8)\n"
    "v_add_f32 v211, v204, v202\n"
    "v_add_f32 v208, v205, v203\n"
    "s_waitcnt lgkmcnt(6)\n"
    "v_add_f32 v201, v198, v196\n"
    "v_add_f32 v211, v211, v201\n"
    "v_mul_f32 v211, 0x3e800000, v211\n"
    "v_add_f32 v213, v199, v197\n"
    "s_waitcnt lgkmcnt(4)\n"
    "v_mfma_f32_16x16x32_bf16 v[28:31], v[44:47], v[8:11], 0\n"
    "v_add_f32 v208, v208, v213\n"
    "v_sub_f32 v40, v204, v211\n"
    "s_waitcnt lgkmcnt(3)\n"
    "v_mfma_f32_16x16x32_bf16 v[60:63], v[64:67], v[12:15], 0\n"
    "v_sub_f32 v41, v202, v211\n"
    "v_sub_f32 v42, v198, v211\n"
    "v_mfma_f32_16x16x32_bf16 v[68:71], v[32:35], v[12:15], 0\n"
    "v_sub_f32 v43, v196, v211\n"
    "v_mul_f32 v206, v40, v40\n"
    "s_waitcnt lgkmcnt(2)\n"
    "v_mfma_f32_16x16x32_bf16 v[76:79], v[72:75], v[16:19], 0\n"
    "v_fmac_f32 v206, v41, v41\n"
    "v_fmac_f32 v206, v42, v42\n"
    "v_mfma_f32_16x16x32_bf16 v[84:87], v[52:55], v[16:19], 0\n"
    "v_fmac_f32 v206, v43, v43\n"
    "v_fmamk_f32 v208, v206, 0x43100000, v208\n"
    "s_waitcnt lgkmcnt(1)\n"
    "v_mfma_f32_16x16x32_bf16 v[64:67], v[80:83], v[20:23], 0\n"
    "v_fmamk_f32 v208, v208, 0x3ae38e39, v207\n"
    "v_rsq_f32 v204, v208\n"
    "v_mfma_f32_16x16x32_bf16 v[12:15], v[36:39], v[20:23], 0\n"
    "v_sub_f32 v210, v210, v211\n"
    "v_mul_f32 v210, s64, v210\n"
    "s_waitcnt lgkmcnt(0)\n"
    "v_mfma_f32_16x16x32_bf16 v[32:35], v[88:91], v[24:27], 0\n"
    "v_cvt_pk_bf16_f32 v72, v210, 0\n"
    "v_cvt_pk_bf16_f32 v73, v210, 0\n"
    "v_mfma_f32_16x16x32_bf16 v[16:19], v[56:59], v[24:27], 0\n"
    "v_cvt_pk_bf16_f32 v74, v210, 0\n"
    "v_cvt_pk_bf16_f32 v75, v210, 0\n"
    "v_mfma_f32_16x16x32_bf16 v[40:43], v[48:51], v[8:11], 0\n"
    "v_mul_f32 v60, v204, v60\n"
    "v_mul_f32 v61, v204, v61\n"
    "v_mfma_f32_16x16x32_bf16 v[28:31], v[44:47], v[72:75], v[28:31]\n"
    "v_mul_f32 v62, v204, v62\n"
    "v_mul_f32 v63, v204, v63\n"
    "v_mfma_f32_16x16x32_bf16 v[40:43], v[48:51], v[72:75], v[40:43]\n"
    "v_cvt_pk_bf16_f32 v206, v60, v61\n"
    "v_cvt_pk_bf16_f32 v207, v62, v63\n"
    "ds_write2_b32 v97, v206, v207 offset0:0 offset1:16\n"
    "v_mul_f32 v68, v204, v68\n"
    "v_mul_f32 v69, v204, v69\n"
    "v_mul_f32 v70, v204, v70\n"
    "v_mul_f32 v71, v204, v71\n"
    "v_cvt_pk_bf16_f32 v210, v68, v69\n"
    "LS_first:\n"
    "s_cbranch_execz LS_back\n"
    "v_cvt_pk_bf16_f32 v211, v70, v71\n"
    "ds_write2_b32 v97, v210, v211 offset0:128 offset1:144\n"
    "v_mul_f32 v76, v204, v76\n"
    "v_mul_f32 v77, v204, v77\n"
    "v_mul_f32 v78, v204, v78\n"
    "v_mul_f32 v79, v204, v79\n"
    "v_cvt_pk_bf16_f32 v206, v76, v77\n"
    "ds_read_b128 v[8:11], v209 offset:4096\n"
    "v_cvt_pk_bf16_f32 v207, v78, v79\n"
    "v_mul_f32 v84, v204, v84\n"
    "v_mul_f32 v85, v204, v85\n"
    "ds_write2_b32 v98, v206, v207 offset0:0 offset1:16\n"
    "v_mul_f32 v86, v204, v86\n"
    "v_mul_f32 v87, v204, v87\n"
    "v_cvt_pk_bf16_f32 v210, v84, v85\n"
    "v_cvt_pk_bf16_f32 v211, v86, v87\n"
    "v_mul_f32 v64, v204, v64\n"
    "ds_write2_b32 v98, v210, v211 offset0:128 offset1:144\n"
    "v_mul_f32 v65, v204, v65\n"
    "v_mul_f32 v66, v204, v66\n"
    "v_mul_f32 v67, v204, v67\n"
    "v_cvt_pk_bf16_f32 v202, v64, v65\n"
    "v_cvt_pk_bf16_f32 v203, v66, v67\n"
    "ds_read_b128 v[20:23], v209 offset:8192\n"
    "v_mul_f32 v12, v204, v12\n"
    "v_mul_f32 v13, v204, v13\n"
    "v_mul_f32 v14, v204, v14\n"
    "ds_write2_b32 v99, v202, v203 offset0:0 offset1:16\n"
    "v_mul_f32 v15, v204, v15\n"
    "v_cvt_pk_bf16_f32 v210, v12, v13\n"
    "v_cvt_pk_bf16_f32 v211, v14, v15\n"
    "v_mul_f32 v32, v204, v32\n"
    "v_mul_f32 v33, v204, v33\n"
    "ds_write2_b32 v99, v210, v211 offset0:128 offset1:144\n"
    "v_mul_f32 v34, v204, v34\n"
    "v_mul_f32 v35, v204, v35\n"
    "v_cvt_pk_bf16_f32 v206, v32, v33\n"
    "v_cvt_pk_bf16_f32 v207, v34, v35\n"
    "v_mul_f32 v16, v204, v16\n"
    "ds_read_b128 v[24:27], v209 offset:12288\n"
    "s_waitcnt lgkmcnt(6)\n"
    "buffer_store_dwordx4 v[8:11], v212, s[20:23], s60 offen sc1\n"
    "v_mul_f32 v17, v204, v17\n"
    "v_mul_f32 v18, v204, v18\n"
    "ds_write2_b32 v100, v206, v207 offset0:0 offset1:16\n"
    "v_mul_f32 v19, v204, v19\n"
    "v_cvt_pk_bf16_f32 v210, v16, v17\n"
    "v_cvt_pk_bf16_f32 v211, v18, v19\n"
    "v_mul_f32 v28, v204, v28\n"
    "v_mul_f32 v29, v204, v29\n"
    "ds_write2_b32 v100, v210, v211 offset0:128 offset1:144\n"
    "v_mul_f32 v30, v204, v30\n"
    "v_mul_f32 v31, v204, v31\n"
    "v_cvt_pk_bf16_f32 v202, v28, v29\n"
    "v_cvt_pk_bf16_f32 v203, v30, v31\n"
    "v_mul_f32 v40, v204, v40\n"
    "ds_read_b128 v[8:11], v209 offset:16384\n"
    "s_waitcnt lgkmcnt(6)\n"
    "buffer_store_dwordx4 v[20:23], v212, s[20:23], s61 offen sc1\n"
    "v_mul_f32 v41, v204, v41\n"
    "v_mul_f32 v42, v204, v42\n"
    "ds_write2_b32 v96, v202, v203 offset0:0 offset1:16\n"
    "v_mul_f32 v43, v204, v43\n"
    "v_cvt_pk_bf16_f32 v210, v40, v41\n"
    "v_cvt_pk_bf16_f32 v211, v42, v43\n"
    "ds_write2_b32 v96, v210, v211 offset0:128 offset1:144\n"
    "ds_read_b128 v[12:15], v209 offset:0\n"
    "s_waitcnt lgkmcnt(6)\n"
    "buffer_store_dwordx4 v[24:27], v212, s[20:23], s62 offen sc1\n"
    "s_waitcnt lgkmcnt(3)\n"
    "buffer_store_dwordx4 v[8:11], v212, s[20:23], s63 offen sc1\n"
    "s_waitcnt lgkmcnt(0)\n"
    "buffer_store_dwordx4 v[12:15], v212, s[20:23], s59 offen sc1\n"
    "s_bfe_u32 s52, s33, 0x20002\n"
    "s_lshl_b32 s52, s52, 11\n"
    "s_lshr_b32 s53, s33, 4\n"
    "s_lshl_b32 s53, s53, 8\n"
    "s_add_u32 s52, s52, s53\n"
    "s_and_b32 s53, s33, 3\n"
    "s_lshl_b32 s53, s53, 6\n"
    "s_add_u32 s52, s52, s53\n"
    "s_add_u32 s52, s52, 0x7e0000\n"
    "v_lshrrev_b32 v213, 2, v0\n"
    "v_and_b32 v211, 3, v0\n"
    "v_lshlrev_b32 v212, 13, v213\n"
    "v_lshl_add_u32 v212, v211, 4, v212\n"
    "v_cmp_gt_u32 vcc, 33, v213\n"
    "v_mov_b32 v210, 0x7ffffff0\n"
    "v_mul_u32_u24 v209, 0x242, v211\n"
    "v_cndmask_b32 v210, v210, v212, vcc\n"
    "v_lshl_add_u32 v209, v213, 1, v209\n"
    "v_add_u32 v209, 0x10000, v209\n"
    "ds_read_u16 v8, v209 offset:0\n"
    "s_add_u32 s53, s52, 0x80000\n"
    "ds_read_u16 v12, v209 offset:3072\n"
    "s_add_u32 s54, s52, 0x100000\n"
    "ds_read_u16 v9, v209 offset:6144\n"
    "s_add_u32 s55, s52, 0x180000\n"
    "ds_read_u16 v13, v209 offset:9216\n"
    "s_add_u32 s56, s52, 0x200000\n"
    "ds_read_u16 v10, v209 offset:12288\n"
    "ds_read_u16 v14, v209 offset:15360\n"
    "ds_read_u16 v11, v209 offset:18432\n"
    "ds_read_u16 v15, v209 offset:21504\n"
    "ds_read_u16 v16, v209 offset:128\n"
    "ds_read_u16 v20, v209 offset:3200\n"
    "ds_read_u16 v17, v209 offset:6272\n"
    "ds_read_u16 v21, v209 offset:9344\n"
    "ds_read_u16 v18, v209 offset:12416\n"
    "ds_read_u16 v22, v209 offset:15488\n"
    "ds_read_u16 v19, v209 offset:18560\n"
    "ds_read_u16 v23, v209 offset:21632\n"
    "ds_read_u16 v24, v209 offset:256\n"
    "ds_read_u16 v28, v209 offset:3328\n"
    "s_waitcnt lgkmcnt(15)\n"
    "v_lshl_or_b32 v8, v12, 16, v8\n"
    "ds_read_u16 v25, v209 offset:6400\n"
    "ds_read_u16 v29, v209 offset:9472\n"
    "s_waitcnt lgkmcnt(15)\n"
    "v_lshl_or_b32 v9, v13, 16, v9\n"
    "ds_read_u16 v26, v209 offset:12544\n"
    "ds_read_u16 v30, v209 offset:15616\n"
    "s_waitcnt lgkmcnt(15)\n"
    "v_lshl_or_b32 v10, v14, 16, v10\n"
    "ds_read_u16 v27, v209 offset:18688\n"
    "ds_read_u16 v31, v209 offset:21760\n"
    "s_waitcnt lgkmcnt(15)\n"
    "v_lshl_or_b32 v11, v15, 16, v11\n"
    "buffer_store_dwordx4 v[8:11], v212, s[20:23], s52 offen sc1\n"
    "ds_read_u16 v32, v209 offset:384\n"
    "s_waitcnt lgkmcnt(15)\n"
    "v_lshl_or_b32 v16, v20, 16, v16\n"
    "ds_read_u16 v36, v209 offset:3456\n"
    "ds_read_u16 v33, v209 offset:6528\n"
    "s_waitcnt lgkmcnt(15)\n"
    "v_lshl_or_b32 v17, v21, 16, v17\n"
    "ds_read_u16 v37, v209 offset:9600\n"
    "ds_read_u16 v34, v209 offset:12672\n"
    "s_waitcnt lgkmcnt(15)\n"
    "v_lshl_or_b32 v18, v22, 16, v18\n"
    "ds_read_u16 v38, v209 offset:15744\n"
    "ds_read_u16 v35, v209 offset:18816\n"
    "s_waitcnt lgkmcnt(15)\n"
    "v_lshl_or_b32 v19, v23, 16, v19\n"
    "buffer_store_dwordx4 v[16:19], v212, s[20:23], s53 offen sc1\n"
    "ds_read_u16 v39, v209 offset:21888\n"
    "ds_read_u16 v8, v209 offset:512\n"
    "s_waitcnt lgkmcnt(15)\n"
    "v_lshl_or_b32 v24, v28, 16, v24\n"
    "ds_read_u16 v12, v209 offset:3584\n"
    "ds_read_u16 v9, v209 offset:6656\n"
    "s_waitcnt lgkmcnt(15)\n"
    "v_lshl_or_b32 v25, v29, 16, v25\n"
    "ds_read_u16 v13, v209 offset:9728\n"
    "ds_read_u16 v10, v209 offset:12800\n"
    "s_waitcnt lgkmcnt(15)\n"
    "v_lshl_or_b32 v26, v30, 16, v26\n"
    "ds_read_u16 v14, v209 offset:15872\n"
    "ds_read_u16 v11, v209 offset:18944\n"
    "s_waitcnt lgkmcnt(15)\n"
    "v_lshl_or_b32 v27, v31, 16, v27\n"
    "buffer_store_dwordx4 v[24:27], v212, s[20:23], s54 offen sc1\n"
    "ds_read_u16 v15, v209 offset:22016\n"
    "s_waitcnt lgkmcnt(14)\n"
    "v_lshl_or_b32 v32, v36, 16, v32\n"
    "s_waitcnt lgkmcnt(12)\n"
    "v_lshl_or_b32 v33, v37, 16, v33\n"
    "s_waitcnt lgkmcnt(10)\n"
    "v_lshl_or_b32 v34, v38, 16, v34\n"
    "s_waitcnt lgkmcnt(8)\n"
    "v_lshl_or_b32 v35, v39, 16, v35\n"
    "buffer_store_dwordx4 v[32:35], v212, s[20:23], s55 offen sc1\n"
    "s_waitcnt lgkmcnt(6)\n"
    "v_lshl_or_b32 v8, v12, 16, v8\n"
    "s_waitcnt lgkmcnt(4)\n"
    "v_lshl_or_b32 v9, v13, 16, v9\n"
    "s_waitcnt lgkmcnt(2)\n"
    "v_lshl_or_b32 v10, v14, 16, v10\n"
    "s_waitcnt lgkmcnt(0)\n"
    "v_lshl_or_b32 v11, v15, 16, v11\n"
    "buffer_store_dwordx4 v[8:11], v210, s[20:23], s56 offen sc1\n"
    "s_call_b64 s[0:1], LI2_entry\n"
    "s_add_u32 s67, s67, 0x4000\n"
    "s_add_u32 s68, s67, 0x4000\n"
    "s_waitcnt vmcnt(5)\n"
    "s_barrier\n"
    "buffer_store_dwordx4 v[218:221], v215, s[20:23], s67 offen sc1\n"
    "s_call_b64 s[0:1], LS_entry\n"
    "s_waitcnt vmcnt(1)\n"
    "s_barrier\n"
    "buffer_store_dwordx4 v[218:221], v215, s[20:23], s68 offen sc1\n"
    "s_and_b32 s50, s48, 6\n"
    "s_cbranch_scc1 LM_A1_7\n"
    "s_lshr_b32 s43, s48, 3\n"
    "s_lshl_b32 s43, s43, 3\n"
    "s_add_u32 s43, s43, s47\n"
    "s_add_u32 s43, s43, 0x80\n"
    "s_branch LM_P2_4\n"
    "LM_B_1:\n"
    "v_min_u32 v213, 59, v1\n"
    "v_mul_u32_u24 v212, 0x199a, v213\n"
    "v_lshrrev_b32 v212, 16, v212\n"
    "v_mul_u32_u24 v214, 0x2400, v212\n"
    "v_mul_u32_u24 v212, 10, v212\n"
    "v_sub_u32 v213, v213, v212\n"
    "v_and_b32 v212, 1, v213\n"
    "v_lshl_add_u32 v214, v212, 7, v214\n"
    "v_lshrrev_b32 v213, 1, v213\n"
    "v_mad_u32_u24 v212, v213, 9, s47\n"
    "s_lshr_b32 s60, s47, 1\n"
    "s_mul_i32 s60, s60, 9\n"
    "s_add_u32 s60, s60, 8\n"
    "v_mov_b32 v211, s60\n"
    "v_cmp_gt_u32 vcc, 4, v213\n"
    "s_nop 1\n"
    "v_cndmask_b32 v212, v211, v212, vcc\n"
    "v_lshl_add_u32 v214, v212, 8, v214\n"
    "s_mov_b32 s60, 0\n"
    "s_lshr_b32 s61, s48, 3\n"
    "s_mul_i32 s61, s61, 3\n"
    "s_bfe_u32 s62, s48, 0x20001\n"
    "s_max_u32 s62, s62, 1\n"
    "s_add_u32 s61, s61, s62\n"
    "s_sub_u32 s61, s61, 1\n"
    "s_lshl_b32 s61, s61, 1\n"
    "s_lshr_b32 s62, s32, 1\n"
    "s_add_u32 s61, s61, s62\n"
    "s_mul_i32 s61, s61, 0xd800\n"
    "s_add_u32 s60, s60, s61\n"
    "s_bitcmp1_b32 s32, 0\n"
    "s_cselect_b64 s[98:99], s[12:13], s[10:11]\n"
    "s_add_u32 s98, s98, s60\n"
    "s_addc_u32 s99, s99, 0\n"
    "s_mov_b32 exec_lo, 0x3fffffff\n"
    "s_mov_b32 exec_hi, 0\n"
    "global_load_dword v255, v214, s[98:99]\n"
    "s_mov_b64 exec, -1\n"
    "s_call_b64 s[0:1], LI2_entry\n"
    "s_mov_b32 s43, s33\n"
    "s_and_b32 s50, s48, 6\n"
    "s_cbranch_scc1 LM_bx_8\n"
    "v_mov_b32 v8, s45\n"
    "v_mov_b32 v9, s45\n"
    "v_mov_b32 v10, s45\n"
    "v_mov_b32 v11, s45\n"
    "s_mul_i32 s66, s33, 0x4800\n"
    "s_mul_i32 s67, s32, 0x1200\n"
    "s_add_u32 s66, s66, s67\n"
    "s_add_u32 s66, s66, 0xa22000\n"
    "buffer_store_dwordx4 v[8:11], v4, s[20:23], s66 offen offset:0 sc1\n"
    "buffer_store_dwordx4 v[8:11], v4, s[20:23], s66 offen offset:1024 sc1\n"
    "buffer_store_dwordx4 v[8:11], v4, s[20:23], s66 offen offset:2048 sc1\n"
    "buffer_store_dwordx4 v[8:11], v4, s[20:23], s66 offen offset:3072 sc1\n"
    "buffer_store_dwordx4 v[8:11], v4, s[20:23], s66 offen offset:3584 sc1\n"
    "LM_bx_8:\n"
    "s_and_b32 s59, s48, 6\n"
    "s_cbranch_scc0 LM_sb_9\n"
    "s_call_b64 s[0:1], LGS_entry\n"
    "s_waitcnt vmcnt(0)\n"
    "s_waitcnt lgkmcnt(0)\n"
    "s_mul_i32 s40, s37, 0x9e3779b1\n"
    "s_add_u32 s40, s40, 0x7f4a7c15\n"
    "s_mul_i32 s41, s37, 0x85ebca6b\n"
    "s_xor_b32 s41, s41, 0xc2b2ae35\n"
    "v_mov_b32 v218, s40\n"
    "v_mov_b32 v219, s41\n"
    "v_mov_b32 v220, s40\n"
    "v_mov_b32 v221, s41\n"
    "s_mov_b32 exec_lo, 0xc0000000\n"
    "s_mov_b32 exec_hi, 0xfffffff\n"
    "global_load_dword v255, v214, s[98:99]\n"
    "s_mov_b64 exec, -1\n"
    "v_add_u32 v214, 0x144000, v214\n"
    "global_load_dword v255, v214, s[98:99]\n"
    "v_add_u32 v214, 0x144000, v214\n"
    "s_call_b64 s[0:1], LS_entry\n"
    "s_branch LM_P2_4\n"
    "LM_sb_9:\n"
    "v_mov_b32 v8, s45\n"
    "v_mov_b32 v9, s45\n"
    "v_mov_b32 v10, s45\n"
    "v_mov_b32 v11, s45\n"
    "s_mul_i32 s66, s33, 0x5000\n"
    "s_mul_i32 s67, s32, 0x1400\n"
    "s_add_u32 s66, s66, s67\n"
    "s_add_u32 s67, s66, 0x561000\n"
    "s_add_u32 s66, s66, 0x560000\n"
    "buffer_store_dwordx4 v[8:11], v4, s[20:23], s66 offen offset:0 sc1\n"
    "buffer_store_dwordx4 v[8:11], v4, s[20:23], s66 offen offset:1024 sc1\n"
    "buffer_store_dwordx4 v[8:11], v4, s[20:23], s66 offen offset:2048 sc1\n"
    "buffer_store_dwordx4 v[8:11], v4, s[20:23], s66 offen offset:3072 sc1\n"
    "buffer_store_dwordx4 v[8:11], v4, s[20:23], s67 offen offset:0 sc1\n"
    "s_call_b64 s[0:1], LGS_entry\n"
    "s_waitcnt vmcnt(0)\n"
    "s_waitcnt lgkmcnt(0)\n"
    "s_mul_i32 s40, s37, 0x9e3779b1\n"
    "s_add_u32 s40, s40, 0x7f4a7c15\n"
    "s_mul_i32 s41, s37, 0x85ebca6b\n"
    "s_xor_b32 s41, s41, 0xc2b2ae35\n"
    "v_mov_b32 v218, s40\n"
    "v_mov_b32 v219, s41\n"
    "v_mov_b32 v220, s40\n"
    "v_mov_b32 v221, s41\n"
    "s_lshl_b32 s68, s33, 7\n"
    "s_add_u32 s68, s68, 0xd000\n"
    "s_barrier\n"
    "buffer_store_dwordx4 v[218:221], v215, s[20:23], s68 offen sc1\n"
    "s_call_b64 s[0:1], LS_entry\n"
    "LM_P2_4:\n"
    "v_lshrrev_b32 v211, 2, v2\n"
    "v_lshl_add_u32 v211, v3, 3, v211\n"
    "v_mul_u32_u24 v210, 0x5000, v211\n"
    "v_and_b32 v211, 3, v2\n"
    "v_lshl_add_u32 v210, v211, 4, v210\n"
    "s_mul_i32 s57, s32, 0xa0000\n"
    "s_lshl_b32 s58, s43, 7\n"
    "s_add_u32 s57, s57, s58\n"
    "s_add_u32 s57, s57, 0x560000\n"
    "s_add_u32 s58, s57, 0x14000\n"
    "s_mul_i32 s64, s32, 0x1000\n"
    "s_add_u32 s64, s64, 0x5000\n"
    "v_min_u32 v209, 31, v1\n"
    "v_lshl_add_u32 v209, v209, 7, s49\n"
    "s_lshl_b32 s63, s43, 7\n"
    "s_add_u32 s63, s63, 0x11000\n"
    "s_lshl_b32 s59, s43, 2\n"
    "s_add_u32 s59, s59, s32\n"
    "s_mul_i32 s59, s59, 0x1000\n"
    "s_add_u32 s59, s59, 0x7c\n"
    "v_min_u32 v212, 15, v1\n"
    "v_lshl_add_u32 v212, v212, 8, s59\n"
    "v_min_u32 v212, 0x50fff8, v212\n"
    "s_lshl_b32 s50, s32, 4\n"
    "v_add_u32 v211, s50, v2\n"
    "v_cvt_f32_u32 v211, v211\n"
    "v_lshlrev_b32 v208, 2, v3\n"
    "v_cvt_f32_u32 v207, v208\n"
    "v_cvt_f32_u32 v206, v2\n"
    "v_and_b32 v205, 1, v2\n"
    "v_mul_lo_u32 v205, v205, s44\n"
    "v_mul_f32 v204, 0x3c800000, v211\n"
    "v_mul_f32 v203, v204, v207\n"
    "v_mov_b32 v8, v203\n"
    "v_fmamk_f32 v9, v204, 0x3f800000, v203\n"
    "v_fmamk_f32 v10, v204, 0x40000000, v203\n"
    "v_fmamk_f32 v11, v204, 0x40400000, v203\n"
    "v_fmamk_f32 v12, v204, 0x41800000, v203\n"
    "v_fmamk_f32 v13, v204, 0x41880000, v203\n"
    "v_fmamk_f32 v14, v204, 0x41900000, v203\n"
    "v_fmamk_f32 v15, v204, 0x41980000, v203\n"
    "v_cos_f32 v200, v8\n"
    "v_sin_f32 v201, v8\n"
    "v_cos_f32 v198, v9\n"
    "v_cvt_pk_bf16_f32 v16, v200, v201\n"
    "v_cvt_pk_bf16_f32 v20, -v201, v200\n"
    "v_xor_b32 v24, v205, v16\n"
    "v_xor_b32 v28, v205, v20\n"
    "v_sin_f32 v199, v9\n"
    "v_cos_f32 v196, v10\n"
    "v_cvt_pk_bf16_f32 v17, v198, v199\n"
    "v_cvt_pk_bf16_f32 v21, -v199, v198\n"
    "v_xor_b32 v25, v205, v17\n"
    "v_xor_b32 v29, v205, v21\n"
    "v_sin_f32 v197, v10\n"
    "v_cos_f32 v202, v11\n"
    "v_cvt_pk_bf16_f32 v18, v196, v197\n"
    "v_cvt_pk_bf16_f32 v22, -v197, v196\n"
    "v_xor_b32 v26, v205, v18\n"
    "v_xor_b32 v30, v205, v22\n"
    "v_sin_f32 v203, v11\n"
    "v_cos_f32 v200, v12\n"
    "v_cvt_pk_bf16_f32 v19, v202, v203\n"
    "v_cvt_pk_bf16_f32 v23, -v203, v202\n"
    "v_xor_b32 v27, v205, v19\n"
    "v_xor_b32 v31, v205, v23\n"
    "v_sin_f32 v201, v12\n"
    "v_cos_f32 v198, v13\n"
    "v_cvt_pk_bf16_f32 v32, v200, v201\n"
    "v_cvt_pk_bf16_f32 v36, -v201, v200\n"
    "v_xor_b32 v40, v205, v32\n"
    "v_xor_b32 v44, v205, v36\n"
    "v_sin_f32 v199, v13\n"
    "v_cos_f32 v196, v14\n"
    "v_cvt_pk_bf16_f32 v33, v198, v199\n"
    "v_cvt_pk_bf16_f32 v37, -v199, v198\n"
    "v_xor_b32 v41, v205, v33\n"
    "v_xor_b32 v45, v205, v37\n"
    "v_sin_f32 v197, v14\n"
    "v_cos_f32 v202, v15\n"
    "v_cvt_pk_bf16_f32 v34, v196, v197\n"
    "v_cvt_pk_bf16_f32 v38, -v197, v196\n"
    "v_xor_b32 v42, v205, v34\n"
    "v_xor_b32 v46, v205, v38\n"
    "v_sin_f32 v203, v15\n"
    "v_mul_f32 v204, 0x3a000000, v211\n"
    "v_mul_f32 v201, v204, v207\n"
    "v_cvt_pk_bf16_f32 v35, v202, v203\n"
    "v_cvt_pk_bf16_f32 v39, -v203, v202\n"
    "v_xor_b32 v43, v205, v35\n"
    "v_xor_b32 v47, v205, v39\n"
    "v_mov_b32 v48, v201\n"
    "v_fmamk_f32 v49, v204, 0x3f800000, v201\n"
    "v_fmamk_f32 v50, v204, 0x40000000, v201\n"
    "v_fmamk_f32 v51, v204, 0x40400000, v201\n"
    "v_fmamk_f32 v52, v204, 0x41800000, v201\n"
    "v_fmamk_f32 v53, v204, 0x41880000, v201\n"
    "v_fmamk_f32 v54, v204, 0x41900000, v201\n"
    "v_fmamk_f32 v55, v204, 0x41980000, v201\n"
    "v_cos_f32 v8, v48\n"
    "v_sin_f32 v56, v48\n"
    "v_cos_f32 v9, v49\n"
    "v_sin_f32 v57, v49\n"
    "v_cos_f32 v10, v50\n"
    "v_sin_f32 v58, v50\n"
    "v_cos_f32 v11, v51\n"
    "v_sin_f32 v59, v51\n"
    "v_cos_f32 v12, v52\n"
    "v_sin_f32 v60, v52\n"
    "v_cos_f32 v13, v53\n"
    "v_sin_f32 v61, v53\n"
    "v_cos_f32 v14, v54\n"
    "v_sin_f32 v62, v54\n"
    "v_cos_f32 v15, v55\n"
    "v_sin_f32 v63, v55\n"
    "v_mul_f32 v211, 0x3d000000, v206\n"
    "v_mul_f32 v204, v211, v207\n"
    "v_mov_b32 v64, v204\n"
    "v_fmamk_f32 v65, v211, 0x3f800000, v204\n"
    "v_fmamk_f32 v66, v211, 0x40000000, v204\n"
    "v_fmamk_f32 v67, v211, 0x40400000, v204\n"
    "v_cos_f32 v202, v64\n"
    "v_sin_f32 v203, v64\n"
    "v_cos_f32 v200, v65\n"
    "v_cvt_pk_bf16_f32 v68, v202, v203\n"
    "v_cvt_pk_bf16_f32 v72, -v203, v202\n"
    "v_xor_b32 v48, v205, v68\n"
    "v_mov_b32 v52, v68\n"
    "v_mov_b32 v76, v48\n"
    "v_xor_b32 v80, v205, v72\n"
    "v_mov_b32 v84, v72\n"
    "v_mov_b32 v88, v80\n"
    "v_sin_f32 v201, v65\n"
    "v_cos_f32 v198, v66\n"
    "v_cvt_pk_bf16_f32 v69, v200, v201\n"
    "v_cvt_pk_bf16_f32 v73, -v201, v200\n"
    "v_xor_b32 v49, v205, v69\n"
    "v_xor_b32 v53, s44, v69\n"
    "v_xor_b32 v77, s44, v49\n"
    "v_xor_b32 v81, v205, v73\n"
    "v_xor_b32 v85, s44, v73\n"
    "v_xor_b32 v89, s44, v81\n"
    "v_sin_f32 v199, v66\n"
    "v_cos_f32 v202, v67\n"
    "v_cvt_pk_bf16_f32 v70, v198, v199\n"
    "v_cvt_pk_bf16_f32 v74, -v199, v198\n"
    "v_xor_b32 v50, v205, v70\n"
    "v_mov_b32 v54, v70\n"
    "v_mov_b32 v78, v50\n"
    "v_xor_b32 v82, v205, v74\n"
    "v_mov_b32 v86, v74\n"
    "v_mov_b32 v90, v82\n"
    "v_sin_f32 v203, v67\n"
    "v_add_u32 v211, s50, v208\n"
    "v_cvt_f32_u32 v211, v211\n"
    "v_cvt_pk_bf16_f32 v71, v202, v203\n"
    "v_cvt_pk_bf16_f32 v75, -v203, v202\n"
    "v_xor_b32 v51, v205, v71\n"
    "v_xor_b32 v55, s44, v71\n"
    "v_xor_b32 v79, s44, v51\n"
    "v_xor_b32 v83, v205, v75\n"
    "v_xor_b32 v87, s44, v75\n"
    "v_xor_b32 v91, s44, v83\n"
    "v_add_f32 v207, 0, v206\n"
    "v_mul_f32 v204, 0x3a000000, v207\n"
    "v_mul_f32 v208, v204, v211\n"
    "v_mov_b32 v64, v208\n"
    "v_fmamk_f32 v65, v204, 0x3f800000, v208\n"
    "v_fmamk_f32 v66, v204, 0x40000000, v208\n"
    "v_fmamk_f32 v67, v204, 0x40400000, v208\n"
    "v_cos_f32 v92, v64\n"
    "v_sin_f32 v100, v64\n"
    "v_cos_f32 v93, v65\n"
    "v_sin_f32 v101, v65\n"
    "v_cos_f32 v94, v66\n"
    "v_sin_f32 v102, v66\n"
    "v_cos_f32 v95, v67\n"
    "v_sin_f32 v103, v67\n"
    "v_add_f32 v207, 0x41800000, v206\n"
    "v_mul_f32 v205, 0x3a000000, v207\n"
    "v_mul_f32 v203, v205, v211\n"
    "v_mov_b32 v108, v203\n"
    "v_fmamk_f32 v109, v205, 0x3f800000, v203\n"
    "v_fmamk_f32 v110, v205, 0x40000000, v203\n"
    "v_fmamk_f32 v111, v205, 0x40400000, v203\n"
    "v_cos_f32 v96, v108\n"
    "v_sin_f32 v104, v108\n"
    "v_cos_f32 v97, v109\n"
    "v_sin_f32 v105, v109\n"
    "v_cos_f32 v98, v110\n"
    "v_sin_f32 v106, v110\n"
    "v_cos_f32 v99, v111\n"
    "v_sin_f32 v107, v111\n"
    "s_lshl_b32 s51, s32, 12\n"
    "s_lshl_b32 s52, s32, 11\n"
    "v_add_u32 v211, s52, v4\n"
    "s_lshl_b32 s53, s43, 14\n"
    "s_add_u32 s53, s53, s51\n"
    "s_add_u32 s53, s53, 0x2e0000\n"
    "s_lshr_b32 s54, s43, 4\n"
    "s_lshl_b32 s54, s54, 1\n"
    "s_and_b32 s55, s43, 15\n"
    "s_mul_i32 s56, s55, 36\n"
    "s_add_u32 s54, s54, s56\n"
    "s_sub_u32 s56, 0x240, s54\n"
    "s_cmp_lt_u32 s55, 8\n"
    "s_cselect_b32 s54, s54, s56\n"
    "s_cselect_b32 s56, 1, -1\n"
    "s_lshl_b32 s56, s56, 13\n"
    "s_lshl_b32 s54, s54, 13\n"
    "s_add_u32 s54, s54, s52\n"
    "s_add_u32 s54, s54, 0x7e0000\n"
    "s_add_u32 s55, s54, s56\n"
    "s_bitcmp0_b32 s48, 0\n"
    "s_cbranch_scc1 LM_bm_11\n"
    "s_and_b32 s59, s48, 6\n"
    "s_cbranch_scc0 LM_sc_12\n"
    "s_waitcnt vmcnt(0)\n"
    "global_load_dword v255, v214, s[98:99]\n"
    "v_add_u32 v214, 0x144000, v214\n"
    "v_mov_b32 v64, s45\n"
    "v_mov_b32 v65, s45\n"
    "v_mov_b32 v66, s45\n"
    "v_mov_b32 v67, s45\n"
    "s_mul_i32 s66, s33, 0x5000\n"
    "s_mul_i32 s67, s32, 0x1400\n"
    "s_add_u32 s66, s66, s67\n"
    "s_add_u32 s67, s66, 0x561000\n"
    "s_add_u32 s66, s66, 0x560000\n"
    "buffer_store_dwordx4 v[64:67], v4, s[20:23], s66 offen offset:0 sc1\n"
    "buffer_store_dwordx4 v[64:67], v4, s[20:23], s66 offen offset:1024 sc1\n"
    "buffer_store_dwordx4 v[64:67], v4, s[20:23], s66 offen offset:2048 sc1\n"
    "buffer_store_dwordx4 v[64:67], v4, s[20:23], s66 offen offset:3072 sc1\n"
    "buffer_store_dwordx4 v[64:67], v4, s[20:23], s67 offen offset:0 sc1\n"
    "s_waitcnt vmcnt(0)\n"
    "global_load_dword v255, v214, s[98:99]\n"
    "s_waitcnt vmcnt(0)\n"
    "s_lshl_b32 s68, s33, 7\n"
    "s_add_u32 s68, s68, 0xd000\n"
    "s_barrier\n"
    "buffer_load_dwordx2 v[206:207], v209, s[20:23], s64 offen sc1\n"
    "buffer_store_dwordx4 v[218:221], v215, s[20:23], s68 offen sc1\n"
    "s_waitcnt vmcnt(1)\n"
    "s_branch LM_ck_10\n"
    "s_branch LM_bm_11\n"
    "LM_sc_12:\n"
    "s_mov_b32 s68, 6\n"
    "s_call_b64 s[0:1], LW_entry\n"
    "LM_bm_11:\n"
    "buffer_load_dwordx2 v[206:207], v209, s[20:23], s64 offen sc1\n"
    "s_waitcnt vmcnt(0)\n"
    "LM_ck_10:\n"
    "v_xor_b32 v204, s40, v206\n"
    "v_xor_b32 v205, s41, v207\n"
    "v_or_b32 v204, v204, v205\n"
    "v_cmp_ne_u32 vcc, 0, v204\n"
    "s_nop 1\n"
    "s_cbranch_vccz LM_f1_13\n"
    "s_sub_u32 s64, s64, 0x4000\n"
    "s_mov_b64 exec, 1\n"
    "buffer_load_dwordx2 v[206:207], v209, s[20:23], s64 offen sc1\n"
    "LM_chk_14:\n"
    "s_waitcnt vmcnt(0)\n"
    "v_xor_b32 v202, s40, v206\n"
    "v_xor_b32 v203, s41, v207\n"
    "v_or_b32 v202, v202, v203\n"
    "v_cmp_ne_u32 vcc, 0, v202\n"
    "s_nop 1\n"
    "s_cbranch_vccz LM_ok_15\n"
    "s_sub_u32 s39, s39, 1\n"
    "s_cbranch_scc1 LM_ok_15\n"
    "s_sleep 2\n"
    "buffer_load_dwordx2 v[206:207], v209, s[20:23], s64 offen sc1\n"
    "s_waitcnt vmcnt(0)\n"
    "s_branch LM_chk_14\n"
    "LM_ok_15:\n"
    "s_max_i32 s39, s39, 0\n"
    "s_mov_b64 exec, -1\n"
    "s_add_u32 s64, s64, 0x4000\n"
    "buffer_load_dwordx2 v[206:207], v209, s[20:23], s64 offen sc1\n"
    "LM_chk_16:\n"
    "s_waitcnt vmcnt(0)\n"
    "v_xor_b32 v204, s40, v206\n"
    "v_xor_b32 v205, s41, v207\n"
    "v_or_b32 v204, v204, v205\n"
    "v_cmp_ne_u32 vcc, 0, v204\n"
    "s_nop 1\n"
    "s_cbranch_vccz LM_ok_17\n"
    "s_sub_u32 s39, s39, 1\n"
    "s_cbranch_scc1 LM_ok_17\n"
    "s_mov_b64 exec, vcc\n"
    "buffer_load_dwordx2 v[206:207], v209, s[20:23], s64 offen sc1\n"
    "s_waitcnt vmcnt(0)\n"
    "s_branch LM_chk_16\n"
    "LM_ok_17:\n"
    "s_max_i32 s39, s39, 0\n"
    "s_mov_b64 exec, -1\n"
    "LM_f1_13:\n"
    "s_barrier\n"
    "s_mov_b32 m0, s51\n"
    "s_nop 0\n"
    "buffer_load_dwordx4 v4, s[20:23], s53 offen offset:0 sc1 lds\n"
    "buffer_load_dwordx4 v4, s[20:23], s53 offen offset:1024 sc1 lds\n"
    "buffer_load_dwordx4 v4, s[20:23], s53 offen offset:2048 sc1 lds\n"
    "buffer_load_dwordx4 v4, s[20:23], s53 offen offset:3072 sc1 lds\n"
    "s_add_u32 s64, s64, 0x4000\n"
    "buffer_load_dwordx2 v[206:207], v209, s[20:23], s64 offen sc1\n"
    "LM_chk_18:\n"
    "s_waitcnt vmcnt(0)\n"
    "v_xor_b32 v204, s40, v206\n"
    "v_xor_b32 v205, s41, v207\n"
    "v_or_b32 v204, v204, v205\n"
    "v_cmp_ne_u32 vcc, 0, v204\n"
    "s_nop 1\n"
    "s_cbranch_vccz LM_ok_19\n"
    "s_sub_u32 s39, s39, 1\n"
    "s_cbranch_scc1 LM_ok_19\n"
    "s_mov_b64 exec, vcc\n"
    "buffer_load_dwordx2 v[206:207], v209, s[20:23], s64 offen sc1\n"
    "s_waitcnt vmcnt(0)\n"
    "s_branch LM_chk_18\n"
    "LM_ok_19:\n"
    "s_max_i32 s39, s39, 0\n"
    "s_mov_b64 exec, -1\n"
    "s_barrier\n"
    "s_add_u32 s64, s64, 0x4000\n"
    "buffer_load_dwordx4 v[64:67], v4, s[20:23], s54 offen offset:0 sc1\n"
    "ds_read_b128 v[108:111], v4 offset:0\n"
    "buffer_load_dwordx4 v[112:115], v4, s[20:23], s54 offen offset:1024 sc1\n"
    "ds_read_b128 v[116:119], v4 offset:1024\n"
    "buffer_load_dwordx4 v[120:123], v4, s[20:23], s55 offen offset:0 sc1\n"
    "ds_read_b128 v[124:127], v4 offset:2048\n"
    "buffer_load_dwordx4 v[128:131], v4, s[20:23], s55 offen offset:1024 sc1\n"
    "ds_read_b128 v[132:135], v4 offset:3072\n"
    "buffer_load_dwordx2 v[206:207], v209, s[20:23], s64 offen sc1\n"
    "ds_read_b128 v[136:139], v4 offset:4096\n"
    "global_load_dwordx2 v[212:213], v212, s[14:15]\n"
    "ds_read_b128 v[140:143], v4 offset:5120\n"
    "ds_read_b128 v[144:147], v4 offset:6144\n"
    "ds_read_b128 v[148:151], v4 offset:7168\n"
    "ds_read_b128 v[152:155], v4 offset:8192\n"
    "s_waitcnt lgkmcnt(8)\n"
    "v_mfma_f32_16x16x32_bf16 v[156:159], v[108:111], v[16:19], 0\n"
    "ds_read_b128 v[160:163], v4 offset:9216\n"
    "v_mfma_f32_16x16x32_bf16 v[164:167], v[108:111], v[20:23], 0\n"
    "ds_read_b128 v[168:171], v4 offset:10240\n"
    "s_waitcnt lgkmcnt(9)\n"
    "v_mfma_f32_16x16x32_bf16 v[156:159], v[116:119], v[32:35], v[156:159]\n"
    "ds_read_b128 v[172:175], v4 offset:11264\n"
    "v_mfma_f32_16x16x32_bf16 v[164:167], v[116:119], v[36:39], v[164:167]\n"
    "ds_read_b128 v[176:179], v4 offset:12288\n"
    "s_waitcnt lgkmcnt(10)\n"
    "v_mfma_f32_16x16x32_bf16 v[156:159], v[124:127], v[24:27], v[156:159]\n"
    "ds_read_b128 v[180:183], v4 offset:13312\n"
    "v_mfma_f32_16x16x32_bf16 v[164:167], v[124:127], v[28:31], v[164:167]\n"
    "ds_read_b128 v[184:187], v4 offset:14336\n"
    "s_waitcnt lgkmcnt(11)\n"
    "v_mfma_f32_16x16x32_bf16 v[156:159], v[132:135], v[40:43], v[156:159]\n"
    "ds_read_b128 v[188:191], v4 offset:15360\n"
    "v_mfma_f32_16x16x32_bf16 v[164:167], v[132:135], v[44:47], v[164:167]\n"
    "s_waitcnt lgkmcnt(11)\n"
    "v_mfma_f32_16x16x32_bf16 v[108:111], v[136:139], v[16:19], 0\n"
    "s_nop 1\n"
    "v_mul_f32 v208, v156, v8\n"
    "v_mfma_f32_16x16x32_bf16 v[192:195], v[136:139], v[20:23], 0\n"
    "v_mul_f32 v205, v157, v9\n"
    "s_nop 0\n"
    "v_mul_f32 v204, v164, v8\n"
    "s_waitcnt lgkmcnt(10)\n"
    "v_mfma_f32_16x16x32_bf16 v[108:111], v[140:143], v[32:35], v[108:111]\n"
    "v_fmac_f32 v208, v164, v56\n"
    "v_fma_f32 v204, -v156, v56, v204\n"
    "v_mfma_f32_16x16x32_bf16 v[192:195], v[140:143], v[36:39], v[192:195]\n"
    "v_cvt_pk_bf16_f32 v116, v208, v204\n"
    "v_mul_f32 v203, v165, v9\n"
    "s_waitcnt lgkmcnt(9)\n"
    "v_mfma_f32_16x16x32_bf16 v[108:111], v[144:147], v[24:27], v[108:111]\n"
    "v_fmac_f32 v205, v165, v57\n"
    "v_fma_f32 v203, -v157, v57, v203\n"
    "v_mfma_f32_16x16x32_bf16 v[192:195], v[144:147], v[28:31], v[192:195]\n"
    "v_cvt_pk_bf16_f32 v117, v205, v203\n"
    "v_mul_f32 v202, v158, v10\n"
    "s_waitcnt lgkmcnt(8)\n"
    "v_mfma_f32_16x16x32_bf16 v[108:111], v[148:151], v[40:43], v[108:111]\n"
    "v_mul_f32 v201, v166, v10\n"
    "v_fmac_f32 v202, v166, v58\n"
    "v_mfma_f32_16x16x32_bf16 v[192:195], v[148:151], v[44:47], v[192:195]\n"
    "v_fma_f32 v201, -v158, v58, v201\n"
    "v_cvt_pk_bf16_f32 v118, v202, v201\n"
    "s_waitcnt lgkmcnt(7)\n"
    "v_mfma_f32_16x16x32_bf16 v[124:127], v[152:155], v[16:19], 0\n"
    "v_mul_f32 v208, v159, v11\n"
    "v_mul_f32 v204, v167, v11\n"
    "v_mfma_f32_16x16x32_bf16 v[132:135], v[152:155], v[20:23], 0\n"
    "v_fmac_f32 v208, v167, v59\n"
    "v_fma_f32 v204, -v159, v59, v204\n"
    "s_waitcnt lgkmcnt(6)\n"
    "v_mfma_f32_16x16x32_bf16 v[124:127], v[160:163], v[32:35], v[124:127]\n"
    "v_cvt_pk_bf16_f32 v119, v208, v204\n"
    "v_mul_f32 v205, v108, v12\n"
    "v_mfma_f32_16x16x32_bf16 v[132:135], v[160:163], v[36:39], v[132:135]\n"
    "v_mul_f32 v203, v192, v12\n"
    "v_fmac_f32 v205, v192, v60\n"
    "s_waitcnt lgkmcnt(5)\n"
    "v_mfma_f32_16x16x32_bf16 v[124:127], v[168:171], v[24:27], v[124:127]\n"
    "v_fma_f32 v203, -v108, v60, v203\n"
    "v_cvt_pk_bf16_f32 v136, v205, v203\n"
    "v_mfma_f32_16x16x32_bf16 v[132:135], v[168:171], v[28:31], v[132:135]\n"
    "v_mul_f32 v202, v109, v13\n"
    "v_mul_f32 v201, v193, v13\n"
    "s_waitcnt lgkmcnt(4)\n"
    "v_mfma_f32_16x16x32_bf16 v[124:127], v[172:175], v[40:43], v[124:127]\n"
    "v_fmac_f32 v202, v193, v61\n"
    "LS_0:\n"
    "s_cbranch_execz LS_4\n"
    "v_fma_f32 v201, -v109, v61, v201\n"
    "v_mfma_f32_16x16x32_bf16 v[132:135], v[172:175], v[44:47], v[132:135]\n"
    "v_cvt_pk_bf16_f32 v137, v202, v201\n"
    "v_mul_f32 v208, v110, v14\n"
    "s_waitcnt lgkmcnt(3)\n"
    "v_mfma_f32_16x16x32_bf16 v[140:143], v[176:179], v[16:19], 0\n"
    "v_mul_f32 v204, v194, v14\n"
    "v_fmac_f32 v208, v194, v62\n"
    "v_mfma_f32_16x16x32_bf16 v[144:147], v[176:179], v[20:23], 0\n"
    "v_fma_f32 v204, -v110, v62, v204\n"
    "v_cvt_pk_bf16_f32 v138, v208, v204\n"
    "s_waitcnt lgkmcnt(2)\n"
    "v_mfma_f32_16x16x32_bf16 v[140:143], v[180:183], v[32:35], v[140:143]\n"
    "v_mul_f32 v205, v111, v15\n"
    "v_mul_f32 v203, v195, v15\n"
    "v_mfma_f32_16x16x32_bf16 v[144:147], v[180:183], v[36:39], v[144:147]\n"
    "v_fmac_f32 v205, v195, v63\n"
    "v_fma_f32 v203, -v111, v63, v203\n"
    "s_waitcnt lgkmcnt(1)\n"
    "v_mfma_f32_16x16x32_bf16 v[140:143], v[184:187], v[24:27], v[140:143]\n"
    "v_cvt_pk_bf16_f32 v139, v205, v203\n"
    "v_mul_f32 v202, v124, v8\n"
    "v_mfma_f32_16x16x32_bf16 v[144:147], v[184:187], v[28:31], v[144:147]\n"
    "v_mul_f32 v201, v132, v8\n"
    "v_fmac_f32 v202, v132, v56\n"
    "s_waitcnt lgkmcnt(0)\n"
    "v_mfma_f32_16x16x32_bf16 v[140:143], v[188:191], v[40:43], v[140:143]\n"
    "v_fma_f32 v201, -v124, v56, v201\n"
    "v_cvt_pk_bf16_f32 v148, v202, v201\n"
    "v_mfma_f32_16x16x32_bf16 v[144:147], v[188:191], v[44:47], v[144:147]\n"
    "v_mul_f32 v208, v125, v9\n"
    "v_mul_f32 v204, v133, v9\n"
    "v_mfma_f32_16x16x32_bf16 v[108:111], v[68:71], v[116:119], 0\n"
    "v_fmac_f32 v208, v133, v57\n"
    "v_fma_f32 v204, -v125, v57, v204\n"
    "v_mfma_f32_16x16x32_bf16 v[108:111], v[48:51], v[136:139], v[108:111]\n"
    "v_cvt_pk_bf16_f32 v149, v208, v204\n"
    "v_mul_f32 v205, v126, v10\n"
    "v_mfma_f32_16x16x32_bf16 v[152:155], v[72:75], v[116:119], 0\n"
    "v_mul_f32 v203, v134, v10\n"
    "v_fmac_f32 v205, v134, v58\n"
    "v_mfma_f32_16x16x32_bf16 v[152:155], v[80:83], v[136:139], v[152:155]\n"
    "v_fma_f32 v203, -v126, v58, v203\n"
    "v_cvt_pk_bf16_f32 v150, v205, v203\n"
    "v_mfma_f32_16x16x32_bf16 v[156:159], v[52:55], v[116:119], 0\n"
    "v_mul_f32 v202, v127, v11\n"
    "v_mul_f32 v201, v135, v11\n"
    "v_mfma_f32_16x16x32_bf16 v[156:159], v[76:79], v[136:139], v[156:159]\n"
    "v_fmac_f32 v202, v135, v59\n"
    "v_fma_f32 v201, -v127, v59, v201\n"
    "v_mfma_f32_16x16x32_bf16 v[160:163], v[84:87], v[116:119], 0\n"
    "v_cvt_pk_bf16_f32 v151, v202, v201\n"
    "v_mul_f32 v208, v140, v12\n"
    "v_mfma_f32_16x16x32_bf16 v[160:163], v[88:91], v[136:139], v[160:163]\n"
    "v_mul_f32 v204, v144, v12\n"
    "v_fmac_f32 v208, v144, v60\n"
    "v_mfma_f32_16x16x32_bf16 v[164:167], v[68:71], v[148:151], 0\n"
    "v_fma_f32 v204, -v140, v60, v204\n"
    "v_cvt_pk_bf16_f32 v168, v208, v204\n"
    "v_mfma_f32_16x16x32_bf16 v[172:175], v[72:75], v[148:151], 0\n"
    "v_mul_f32 v205, v141, v13\n"
    "v_mul_f32 v203, v145, v13\n"
    "v_mfma_f32_16x16x32_bf16 v[116:119], v[52:55], v[148:151], 0\n"
    "v_fmac_f32 v205, v145, v61\n"
    "v_fma_f32 v203, -v141, v61, v203\n"
    "v_mfma_f32_16x16x32_bf16 v[124:127], v[84:87], v[148:151], 0\n"
    "v_cvt_pk_bf16_f32 v169, v205, v203\n"
    "v_mul_f32 v202, v142, v14\n"
    "v_mul_f32 v201, v146, v14\n"
    "v_fmac_f32 v202, v146, v62\n"
    "v_fma_f32 v201, -v142, v62, v201\n"
    "v_cvt_pk_bf16_f32 v170, v202, v201\n"
    "v_mul_f32 v208, v143, v15\n"
    "v_mul_f32 v204, v147, v15\n"
    "v_fmac_f32 v208, v147, v63\n"
    "v_fma_f32 v204, -v143, v63, v204\n"
    "v_cvt_pk_bf16_f32 v171, v208, v204\n"
    "s_nop 1\n"
    "v_mfma_f32_16x16x32_bf16 v[164:167], v[48:51], v[168:171], v[164:167]\n"
    "v_mfma_f32_16x16x32_bf16 v[172:175], v[80:83], v[168:171], v[172:175]\n"
    "s_waitcnt vmcnt(5)\n"
    "v_lshlrev_b32 v205, 16, v64\n"
    "v_and_b32 v64, 0xffff0000, v64\n"
    "v_mfma_f32_16x16x32_bf16 v[116:119], v[76:79], v[168:171], v[116:119]\n"
    "v_mul_f32 v203, v108, v205\n"
    "v_mul_f32 v202, v152, v205\n"
    "v_mfma_f32_16x16x32_bf16 v[124:127], v[88:91], v[168:171], v[124:127]\n"
    "v_fma_f32 v203, -v152, v64, v203\n"
    "v_fmac_f32 v202, v108, v64\n"
    "v_cvt_pk_bf16_f32 v8, v202, v203\n"
    "v_lshlrev_b32 v208, 16, v65\n"
    "v_and_b32 v65, 0xffff0000, v65\n"
    "v_mul_f32 v204, v109, v208\n"
    "v_mul_f32 v201, v153, v208\n"
    "v_fma_f32 v204, -v153, v65, v204\n"
    "v_fmac_f32 v201, v109, v65\n"
    "v_cvt_pk_bf16_f32 v9, v201, v204\n"
    "v_lshlrev_b32 v205, 16, v66\n"
    "v_and_b32 v66, 0xffff0000, v66\n"
    "v_mul_f32 v200, v110, v205\n"
    "v_mul_f32 v199, v154, v205\n"
    "v_fma_f32 v200, -v154, v66, v200\n"
    "v_fmac_f32 v199, v110, v66\n"
    "v_cvt_pk_bf16_f32 v10, v199, v200\n"
    "v_lshlrev_b32 v203, 16, v67\n"
    "v_and_b32 v67, 0xffff0000, v67\n"
    "v_mul_f32 v208, v111, v203\n"
    "v_mul_f32 v202, v155, v203\n"
    "v_fma_f32 v208, -v155, v67, v208\n"
    "v_fmac_f32 v202, v111, v67\n"
    "v_cvt_pk_bf16_f32 v11, v202, v208\n"
    "s_waitcnt vmcnt(4)\n"
    "v_lshlrev_b32 v204, 16, v112\n"
    "v_and_b32 v112, 0xffff0000, v112\n"
    "v_mfma_f32_16x16x32_bf16 v[12:15], v[8:11], v[72:75], 0\n"
    "v_mul_f32 v205, v156, v204\n"
    "v_mul_f32 v201, v160, v204\n"
    "v_mfma_f32_16x16x32_bf16 v[56:59], v[8:11], v[68:71], 0\n"
    "v_fma_f32 v205, -v160, v112, v205\n"
    "v_fmac_f32 v201, v156, v112\n"
    "v_mfma_f32_16x16x32_bf16 v[60:63], v[8:11], v[84:87], 0\n"
    "v_cvt_pk_bf16_f32 v132, v201, v205\n"
    "v_lshlrev_b32 v203, 16, v113\n"
    "v_mfma_f32_16x16x32_bf16 v[64:67], v[8:11], v[52:55], 0\n"
    "v_and_b32 v113, 0xffff0000, v113\n"
    "v_mul_f32 v208, v157, v203\n"
    "v_mul_f32 v202, v161, v203\n"
    "v_fma_f32 v208, -v161, v113, v208\n"
    "v_fmac_f32 v202, v157, v113\n"
    "v_cvt_pk_bf16_f32 v133, v202, v208\n"
    "v_lshlrev_b32 v204, 16, v114\n"
    "v_and_b32 v114, 0xffff0000, v114\n"
    "v_mul_f32 v200, v158, v204\n"
    "v_mul_f32 v205, v162, v204\n"
    "v_fma_f32 v200, -v162, v114, v200\n"
    "v_fmac_f32 v205, v158, v114\n"
    "v_cvt_pk_bf16_f32 v134, v205, v200\n"
    "v_lshlrev_b32 v201, 16, v115\n"
    "v_and_b32 v115, 0xffff0000, v115\n"
    "v_mul_f32 v203, v159, v201\n"
    "v_mul_f32 v199, v163, v201\n"
    "v_fma_f32 v203, -v163, v115, v203\n"
    "v_fmac_f32 v199, v159, v115\n"
    "v_cvt_pk_bf16_f32 v135, v199, v203\n"
    "s_waitcnt vmcnt(3)\n"
    "v_lshlrev_b32 v208, 16, v120\n"
    "v_and_b32 v120, 0xffff0000, v120\n"
    "v_mfma_f32_16x16x32_bf16 v[12:15], v[132:135], v[80:83], v[12:15]\n"
    "v_mul_f32 v204, v164, v208\n"
    "v_mul_f32 v205, v172, v208\n"
    "v_mfma_f32_16x16x32_bf16 v[56:59], v[132:135], v[48:51], v[56:59]\n"
    "v_fma_f32 v204, -v172, v120, v204\n"
    "v_fmac_f32 v205, v164, v120\n"
    "v_mfma_f32_16x16x32_bf16 v[60:63], v[132:135], v[88:91], v[60:63]\n"
    "v_cvt_pk_bf16_f32 v8, v205, v204\n"
    "s_nop 0\n"
    "v_mul_f32 v202, v12, v92\n"
    "v_mfma_f32_16x16x32_bf16 v[64:67], v[132:135], v[76:79], v[64:67]\n"
    "v_mul_f32 v203, v13, v93\n"
    "v_mul_f32 v201, v56, v92\n"
    "v_fma_f32 v202, -v56, v100, v202\n"
    "v_fmac_f32 v201, v12, v100\n"
    "v_cvt_pk_bf16_f32 v108, v201, v202\n"
    "v_mul_f32 v208, v57, v93\n"
    "v_fma_f32 v203, -v57, v101, v203\n"
    "v_fmac_f32 v208, v13, v101\n"
    "v_cvt_pk_bf16_f32 v109, v208, v203\n"
    "v_mul_f32 v205, v14, v94\n"
    "v_mul_f32 v204, v58, v94\n"
    "v_fma_f32 v205, -v58, v102, v205\n"
    "v_fmac_f32 v204, v14, v102\n"
    "v_cvt_pk_bf16_f32 v110, v204, v205\n"
    "v_mul_f32 v200, v15, v95\n"
    "v_mul_f32 v199, v59, v95\n"
    "v_fma_f32 v200, -v59, v103, v200\n"
    "v_fmac_f32 v199, v15, v103\n"
    "v_cvt_pk_bf16_f32 v111, v199, v200\n"
    "ds_write_b128 v211, v[108:111] offset:16384\n"
    "v_mul_f32 v208, v60, v96\n"
    "v_mul_f32 v203, v64, v96\n"
    "v_fma_f32 v208, -v64, v104, v208\n"
    "v_fmac_f32 v203, v60, v104\n"
    "v_cvt_pk_bf16_f32 v112, v203, v208\n"
    "v_mul_f32 v205, v61, v97\n"
    "v_mul_f32 v204, v65, v97\n"
    "v_fma_f32 v205, -v65, v105, v205\n"
    "v_fmac_f32 v204, v61, v105\n"
    "v_cvt_pk_bf16_f32 v113, v204, v205\n"
    "v_mul_f32 v202, v62, v98\n"
    "v_mul_f32 v201, v66, v98\n"
    "v_fma_f32 v202, -v66, v106, v202\n"
    "v_fmac_f32 v201, v62, v106\n"
    "v_cvt_pk_bf16_f32 v114, v201, v202\n"
    "v_mul_f32 v200, v63, v99\n"
    "v_mul_f32 v208, v67, v99\n"
    "v_fma_f32 v200, -v67, v107, v200\n"
    "v_fmac_f32 v208, v63, v107\n"
    "v_cvt_pk_bf16_f32 v115, v208, v200\n"
    "ds_write_b128 v211, v[112:115] offset:17408\n"
    "v_lshlrev_b32 v205, 16, v121\n"
    "v_and_b32 v121, 0xffff0000, v121\n"
    "v_mul_f32 v204, v165, v205\n"
    "v_mul_f32 v203, v173, v205\n"
    "v_fma_f32 v204, -v173, v121, v204\n"
    "v_fmac_f32 v203, v165, v121\n"
    "v_cvt_pk_bf16_f32 v9, v203, v204\n"
    "v_lshlrev_b32 v202, 16, v122\n"
    "v_and_b32 v122, 0xffff0000, v122\n"
    "v_mul_f32 v201, v166, v202\n"
    "v_mul_f32 v208, v174, v202\n"
    "v_fma_f32 v201, -v174, v122, v201\n"
    "v_fmac_f32 v208, v166, v122\n"
    "v_cvt_pk_bf16_f32 v10, v208, v201\n"
    "v_lshlrev_b32 v200, 16, v123\n"
    "v_and_b32 v123, 0xffff0000, v123\n"
    "v_mul_f32 v205, v167, v200\n"
    "v_mul_f32 v199, v175, v200\n"
    "v_fma_f32 v205, -v175, v123, v205\n"
    "v_fmac_f32 v199, v167, v123\n"
    "v_cvt_pk_bf16_f32 v11, v199, v205\n"
    "s_waitcnt vmcnt(2)\n"
    "v_lshlrev_b32 v204, 16, v128\n"
    "v_and_b32 v128, 0xffff0000, v128\n"
    "v_mfma_f32_16x16x32_bf16 v[12:15], v[8:11], v[72:75], 0\n"
    "v_mul_f32 v203, v116, v204\n"
    "v_mul_f32 v208, v124, v204\n"
    "v_mfma_f32_16x16x32_bf16 v[56:59], v[8:11], v[68:71], 0\n"
    "v_fma_f32 v203, -v124, v128, v203\n"
    "v_fmac_f32 v208, v116, v128\n"
    "v_mfma_f32_16x16x32_bf16 v[60:63], v[8:11], v[84:87], 0\n"
    "v_cvt_pk_bf16_f32 v64, v208, v203\n"
    "v_lshlrev_b32 v202, 16, v129\n"
    "v_mfma_f32_16x16x32_bf16 v[108:111], v[8:11], v[52:55], 0\n"
    "v_and_b32 v129, 0xffff0000, v129\n"
    "v_mul_f32 v205, v117, v202\n"
    "v_mul_f32 v201, v125, v202\n"
    "v_fma_f32 v205, -v125, v129, v205\n"
    "v_fmac_f32 v201, v117, v129\n"
    "v_cvt_pk_bf16_f32 v65, v201, v205\n"
    "v_lshlrev_b32 v204, 16, v130\n"
    "v_and_b32 v130, 0xffff0000, v130\n"
    "v_mul_f32 v200, v118, v204\n"
    "v_mul_f32 v208, v126, v204\n"
    "v_fma_f32 v200, -v126, v130, v200\n"
    "v_fmac_f32 v208, v118, v130\n"
    "v_cvt_pk_bf16_f32 v66, v208, v200\n"
    "v_lshlrev_b32 v203, 16, v131\n"
    "v_and_b32 v131, 0xffff0000, v131\n"
    "v_mul_f32 v202, v119, v203\n"
    "v_mul_f32 v199, v127, v203\n"
    "v_fma_f32 v202, -v127, v131, v202\n"
    "v_fmac_f32 v199, v119, v131\n"
    "v_cvt_pk_bf16_f32 v67, v199, v202\n"
    "s_nop 1\n"
    "v_mfma_f32_16x16x32_bf16 v[12:15], v[64:67], v[80:83], v[12:15]\n"
    "v_mfma_f32_16x16x32_bf16 v[56:59], v[64:67], v[48:51], v[56:59]\n"
    "v_mfma_f32_16x16x32_bf16 v[60:63], v[64:67], v[88:91], v[60:63]\n"
    "s_nop 1\n"
    "v_mul_f32 v205, v12, v92\n"
    "v_mfma_f32_16x16x32_bf16 v[108:111], v[64:67], v[76:79], v[108:111]\n"
    "v_mul_f32 v208, v13, v93\n"
    "s_nop 0\n"
    "v_mul_f32 v204, v56, v92\n"
    "v_fma_f32 v205, -v56, v100, v205\n"
    "v_fmac_f32 v204, v12, v100\n"
    "v_cvt_pk_bf16_f32 v8, v204, v205\n"
    "v_mul_f32 v203, v57, v93\n"
    "v_fma_f32 v208, -v57, v101, v208\n"
    "v_fmac_f32 v203, v13, v101\n"
    "v_cvt_pk_bf16_f32 v9, v203, v208\n"
    "v_mul_f32 v202, v14, v94\n"
    "v_mul_f32 v201, v58, v94\n"
    "v_fma_f32 v202, -v58, v102, v202\n"
    "v_fmac_f32 v201, v14, v102\n"
    "v_cvt_pk_bf16_f32 v10, v201, v202\n"
    "v_mul_f32 v200, v15, v95\n"
    "v_mul_f32 v199, v59, v95\n"
    "v_fma_f32 v200, -v59, v103, v200\n"
    "v_fmac_f32 v199, v15, v103\n"
    "v_cvt_pk_bf16_f32 v11, v199, v200\n"
    "ds_write_b128 v211, v[8:11] offset:24576\n"
    "v_mul_f32 v208, v60, v96\n"
    "v_mul_f32 v205, v108, v96\n"
    "v_fma_f32 v208, -v108, v104, v208\n"
    "v_fmac_f32 v205, v60, v104\n"
    "v_cvt_pk_bf16_f32 v48, v205, v208\n"
    "v_mul_f32 v204, v61, v97\n"
    "v_mul_f32 v203, v109, v97\n"
    "v_fma_f32 v204, -v109, v105, v204\n"
    "v_fmac_f32 v203, v61, v105\n"
    "v_cvt_pk_bf16_f32 v49, v203, v204\n"
    "v_mul_f32 v202, v62, v98\n"
    "v_mul_f32 v201, v110, v98\n"
    "v_fma_f32 v202, -v110, v106, v202\n"
    "v_fmac_f32 v201, v62, v106\n"
    "v_cvt_pk_bf16_f32 v50, v201, v202\n"
    "v_mul_f32 v200, v63, v99\n"
    "v_mul_f32 v208, v111, v99\n"
    "v_fma_f32 v200, -v111, v107, v200\n"
    "v_fmac_f32 v208, v63, v107\n"
    "v_cvt_pk_bf16_f32 v51, v208, v200\n"
    "ds_write_b128 v211, v[48:51] offset:25600\n"
    "LM_chk_20:\n"
    "s_waitcnt vmcnt(1)\n"
    "v_xor_b32 v204, s40, v206\n"
    "v_xor_b32 v205, s41, v207\n"
    "v_or_b32 v204, v204, v205\n"
    "v_cmp_ne_u32 vcc, 0, v204\n"
    "s_nop 1\n"
    "s_cbranch_vccz LM_ok_21\n"
    "s_sub_u32 s39, s39, 1\n"
    "s_cbranch_scc1 LM_ok_21\n"
    "s_mov_b64 exec, vcc\n"
    "buffer_load_dwordx2 v[206:207], v209, s[20:23], s64 offen sc1\n"
    "s_waitcnt vmcnt(0)\n"
    "s_branch LM_chk_20\n"
    "LM_ok_21:\n"
    "s_max_i32 s39, s39, 0\n"
    "s_mov_b64 exec, -1\n"
    "s_waitcnt lgkmcnt(0)\n"
    "s_barrier\n"
    "buffer_store_dwordx4 v[218:221], v215, s[20:23], s63 offen sc1\n"
    "ds_read_b128 v[8:11], v4 offset:16384\n"
    "ds_read_b128 v[12:15], v4 offset:18432\n"
    "ds_read_b128 v[48:51], v4 offset:20480\n"
    "ds_read_b128 v[52:55], v4 offset:22528\n"
    "ds_read_b128 v[56:59], v4 offset:24576\n"
    "ds_read_b128 v[60:63], v4 offset:26624\n"
    "ds_read_b128 v[64:67], v4 offset:28672\n"
    "ds_read_b128 v[68:71], v4 offset:30720\n"
    "ds_read_b128 v[72:75], v4 offset:17408\n"
    "s_waitcnt lgkmcnt(8)\n"
    "v_mfma_f32_16x16x32_bf16 v[76:79], v[20:23], v[8:11], 0\n"
    "ds_read_b128 v[80:83], v4 offset:19456\n"
    "s_waitcnt lgkmcnt(8)\n"
    "v_mfma_f32_16x16x32_bf16 v[76:79], v[36:39], v[12:15], v[76:79]\n"
    "ds_read_b128 v[84:87], v4 offset:21504\n"
    "s_waitcnt lgkmcnt(8)\n"
    "v_mfma_f32_16x16x32_bf16 v[76:79], v[28:31], v[48:51], v[76:79]\n"
    "ds_read_b128 v[88:91], v4 offset:23552\n"
    "s_waitcnt lgkmcnt(8)\n"
    "v_mfma_f32_16x16x32_bf16 v[76:79], v[44:47], v[52:55], v[76:79]\n"
    "ds_read_b128 v[92:95], v4 offset:25600\n"
    "v_mfma_f32_16x16x32_bf16 v[96:99], v[16:19], v[8:11], 0\n"
    "ds_read_b128 v[100:103], v4 offset:27648\n"
    "v_mfma_f32_16x16x32_bf16 v[96:99], v[32:35], v[12:15], v[96:99]\n"
    "ds_read_b128 v[104:107], v4 offset:29696\n"
    "v_mfma_f32_16x16x32_bf16 v[96:99], v[24:27], v[48:51], v[96:99]\n"
    "ds_read_b128 v[108:111], v4 offset:31744\n"
    "v_mfma_f32_16x16x32_bf16 v[96:99], v[40:43], v[52:55], v[96:99]\n"
    "s_waitcnt lgkmcnt(11)\n"
    "v_mfma_f32_16x16x32_bf16 v[112:115], v[20:23], v[56:59], 0\n"
    "s_waitcnt lgkmcnt(10)\n"
    "v_mfma_f32_16x16x32_bf16 v[112:115], v[36:39], v[60:63], v[112:115]\n"
    "s_nop 1\n"
    "v_cvt_pk_bf16_f32 v116, v76, v96\n"
    "s_waitcnt lgkmcnt(9)\n"
    "v_mfma_f32_16x16x32_bf16 v[112:115], v[28:31], v[64:67], v[112:115]\n"
    "v_cvt_pk_bf16_f32 v117, v77, v97\n"
    "v_cvt_pk_bf16_f32 v118, v78, v98\n"
    "s_waitcnt lgkmcnt(8)\n"
    "v_mfma_f32_16x16x32_bf16 v[112:115], v[44:47], v[68:71], v[112:115]\n"
    "v_cvt_pk_bf16_f32 v119, v79, v99\n"
    "buffer_store_dwordx4 v[116:119], v210, s[20:23], s57 offen offset:0 sc1\n"
    "v_mfma_f32_16x16x32_bf16 v[8:11], v[16:19], v[56:59], 0\n"
    "v_mfma_f32_16x16x32_bf16 v[8:11], v[32:35], v[60:63], v[8:11]\n"
    "v_mfma_f32_16x16x32_bf16 v[8:11], v[24:27], v[64:67], v[8:11]\n"
    "v_mfma_f32_16x16x32_bf16 v[8:11], v[40:43], v[68:71], v[8:11]\n"
    "s_waitcnt lgkmcnt(7)\n"
    "v_mfma_f32_16x16x32_bf16 v[12:15], v[20:23], v[72:75], 0\n"
    "s_waitcnt lgkmcnt(6)\n"
    "v_mfma_f32_16x16x32_bf16 v[12:15], v[36:39], v[80:83], v[12:15]\n"
    "s_nop 1\n"
    "v_cvt_pk_bf16_f32 v48, v112, v8\n"
    "s_waitcnt lgkmcnt(5)\n"
    "v_mfma_f32_16x16x32_bf16 v[12:15], v[28:31], v[84:87], v[12:15]\n"
    "v_cvt_pk_bf16_f32 v49, v113, v9\n"
    "v_cvt_pk_bf16_f32 v50, v114, v10\n"
    "s_waitcnt lgkmcnt(4)\n"
    "v_mfma_f32_16x16x32_bf16 v[12:15], v[44:47], v[88:91], v[12:15]\n"
    "v_cvt_pk_bf16_f32 v51, v115, v11\n"
    "buffer_store_dwordx4 v[48:51], v210, s[20:23], s57 offen offset:64 sc1\n"
    "v_mfma_f32_16x16x32_bf16 v[52:55], v[16:19], v[72:75], 0\n"
    "v_mfma_f32_16x16x32_bf16 v[52:55], v[32:35], v[80:83], v[52:55]\n"
    "v_mfma_f32_16x16x32_bf16 v[52:55], v[24:27], v[84:87], v[52:55]\n"
    "v_mfma_f32_16x16x32_bf16 v[52:55], v[40:43], v[88:91], v[52:55]\n"
    "s_waitcnt lgkmcnt(3)\n"
    "v_mfma_f32_16x16x32_bf16 v[56:59], v[20:23], v[92:95], 0\n"
    "s_waitcnt lgkmcnt(2)\n"
    "v_mfma_f32_16x16x32_bf16 v[56:59], v[36:39], v[100:103], v[56:59]\n"
    "s_nop 1\n"
    "v_cvt_pk_bf16_f32 v60, v12, v52\n"
    "s_waitcnt lgkmcnt(1)\n"
    "v_mfma_f32_16x16x32_bf16 v[56:59], v[28:31], v[104:107], v[56:59]\n"
    "v_cvt_pk_bf16_f32 v61, v13, v53\n"
    "v_cvt_pk_bf16_f32 v62, v14, v54\n"
    "s_waitcnt lgkmcnt(0)\n"
    "v_mfma_f32_16x16x32_bf16 v[56:59], v[44:47], v[108:111], v[56:59]\n"
    "v_cvt_pk_bf16_f32 v63, v15, v55\n"
    "buffer_store_dwordx4 v[60:63], v210, s[20:23], s58 offen offset:0 sc1\n"
    "v_mfma_f32_16x16x32_bf16 v[8:11], v[16:19], v[92:95], 0\n"
    "v_mfma_f32_16x16x32_bf16 v[8:11], v[32:35], v[100:103], v[8:11]\n"
    "v_mfma_f32_16x16x32_bf16 v[8:11], v[24:27], v[104:107], v[8:11]\n"
    "v_mfma_f32_16x16x32_bf16 v[8:11], v[40:43], v[108:111], v[8:11]\n"
    "s_nop 7\n"
    "v_cvt_pk_bf16_f32 v20, v56, v8\n"
    "v_cvt_pk_bf16_f32 v21, v57, v9\n"
    "v_cvt_pk_bf16_f32 v22, v58, v10\n"
    "v_cvt_pk_bf16_f32 v23, v59, v11\n"
    "buffer_store_dwordx4 v[20:23], v210, s[20:23], s58 offen offset:64 sc1\n"
    "s_and_b32 s50, s48, 7\n"
    "s_cmp_eq_u32 s50, 1\n"
    "s_cbranch_scc1 LM_P3_5\n"
    "s_bitcmp1_b32 s48, 0\n"
    "s_cbranch_scc1 LM_ag_22\n"
    "s_call_b64 s[0:1], LGS_entry\n"
    "LM_ag_22:\n"
    "s_barrier\n"
    "s_mov_b32 s54, 0\n"
    "s_mov_b32 s68, 9\n"
    "s_call_b64 s[0:1], LW_entry\n"
    "s_branch LM_J_6\n"
    "LW_entry:\n"
    "s_min_u32 s68, s68, s46\n"
    "s_cmp_eq_u32 s68, 0\n"
    "s_cbranch_scc1 LW_ret\n"
    "s_sub_u32 s46, s46, s68\n"
    "s_mov_b32 s66, s68\n"
    "s_mov_b32 s67, 2\n"
    "s_cmp_eq_u32 s3, 2\n"
    "s_cbranch_scc1 LW_d0\n"
    "s_cmp_eq_u32 s3, 3\n"
    "s_cbranch_scc1 LW_d1\n"
    "s_cmp_eq_u32 s3, 4\n"
    "s_cbranch_scc1 LW_d2\n"
    "s_cmp_eq_u32 s3, 5\n"
    "s_cbranch_scc1 LW_d3\n"
    "s_cmp_eq_u32 s3, 6\n"
    "s_cbranch_scc1 LW_d4\n"
    "s_cmp_eq_u32 s3, 7\n"
    "s_cbranch_scc1 LW_d5\n"
    "s_cmp_eq_u32 s3, 8\n"
    "s_cbranch_scc1 LW_d6\n"
    "s_cmp_eq_u32 s3, 0\n"
    "s_cbranch_scc1 LW_d7\n"
    "s_cmp_eq_u32 s3, 1\n"
    "s_cbranch_scc1 LW_d8\n"
    "LW_s0:\n"
    "s_waitcnt vmcnt(8) lgkmcnt(0)\n"
    "s_cmp_lg_u32 s66, 1\n"
    "s_cbranch_scc1 LW_b0\n"
    "s_waitcnt vmcnt(0)\n"
    "LW_b0:\n"
    "s_barrier\n"
    "LW_d0:\n"
    "s_cmp_eq_u32 s68, 0\n"
    "s_cbranch_scc1 LW_r0\n"
    "s_sub_u32 s68, s68, 1\n"
    "s_add_u32 m0, s42, 65536\n"
    "s_mov_b32 s69, 1179648\n"
    "buffer_load_dwordx4 v5, s[24:27], s69 offen offset:0 lds\n"
    "buffer_load_dwordx4 v5, s[28:31], s69 offen offset:2048 lds\n"
    "buffer_load_dwordx4 v217, s[24:27], s69 offen offset:1024 lds\n"
    "buffer_load_dwordx4 v217, s[28:31], s69 offen offset:3072 lds\n"
    "s_add_u32 m0, s42, 81920\n"
    "s_mov_b32 s69, 1216512\n"
    "buffer_load_dwordx4 v5, s[24:27], s69 offen offset:0 lds\n"
    "buffer_load_dwordx4 v5, s[28:31], s69 offen offset:2048 lds\n"
    "buffer_load_dwordx4 v217, s[24:27], s69 offen offset:1024 lds\n"
    "buffer_load_dwordx4 v217, s[28:31], s69 offen offset:3072 lds\n"
    "LW_r0:\n"
    "s_cmp_eq_u32 s67, 0\n"
    "s_cbranch_scc1 LW_q0\n"
    "s_sub_u32 s67, s67, 1\n"
    "s_cmp_eq_u32 s67, 0\n"
    "s_cbranch_scc1 LW_s1\n"
    "s_branch LW_d1\n"
    "LW_q0:\n"
    "ds_read_b64_tr_b16 a[0:1], v6 offset:0\n"
    "ds_read_b64_tr_b16 a[2:3], v6 offset:1024\n"
    "ds_read_b64_tr_b16 a[4:5], v6 offset:2048\n"
    "ds_read_b64_tr_b16 a[6:7], v6 offset:3072\n"
    "ds_read_b64_tr_b16 a[8:9], v7 offset:0\n"
    "ds_read_b64_tr_b16 a[10:11], v7 offset:1024\n"
    "ds_read_b64_tr_b16 a[12:13], v7 offset:2048\n"
    "ds_read_b64_tr_b16 a[14:15], v7 offset:3072\n"
    "ds_read_b64_tr_b16 a[16:17], v6 offset:16384\n"
    "ds_read_b64_tr_b16 a[18:19], v6 offset:17408\n"
    "ds_read_b64_tr_b16 a[20:21], v6 offset:18432\n"
    "ds_read_b64_tr_b16 a[22:23], v6 offset:19456\n"
    "ds_read_b64_tr_b16 a[24:25], v7 offset:16384\n"
    "ds_read_b64_tr_b16 a[26:27], v7 offset:17408\n"
    "ds_read_b64_tr_b16 a[28:29], v7 offset:18432\n"
    "ds_read_b64_tr_b16 a[30:31], v7 offset:19456\n"
    "s_mov_b32 s3, 1\n"
    "s_sub_u32 s66, s66, 1\n"
    "s_cmp_eq_u32 s66, 0\n"
    "s_cbranch_scc1 LW_done\n"
    "LW_s1:\n"
    "s_waitcnt vmcnt(8) lgkmcnt(0)\n"
    "s_cmp_lg_u32 s66, 1\n"
    "s_cbranch_scc1 LW_b1\n"
    "s_waitcnt vmcnt(0)\n"
    "LW_b1:\n"
    "s_barrier\n"
    "LW_d1:\n"
    "s_cmp_eq_u32 s68, 0\n"
    "s_cbranch_scc1 LW_r1\n"
    "s_sub_u32 s68, s68, 1\n"
    "s_add_u32 m0, s42, 0\n"
    "s_mov_b32 s69, 1253376\n"
    "buffer_load_dwordx4 v5, s[24:27], s69 offen offset:0 lds\n"
    "buffer_load_dwordx4 v5, s[28:31], s69 offen offset:2048 lds\n"
    "buffer_load_dwordx4 v217, s[24:27], s69 offen offset:1024 lds\n"
    "buffer_load_dwordx4 v217, s[28:31], s69 offen offset:3072 lds\n"
    "s_add_u32 m0, s42, 16384\n"
    "s_mov_b32 s69, 1290240\n"
    "buffer_load_dwordx4 v5, s[24:27], s69 offen offset:0 lds\n"
    "buffer_load_dwordx4 v5, s[28:31], s69 offen offset:2048 lds\n"
    "buffer_load_dwordx4 v217, s[24:27], s69 offen offset:1024 lds\n"
    "buffer_load_dwordx4 v217, s[28:31], s69 offen offset:3072 lds\n"
    "LW_r1:\n"
    "s_cmp_eq_u32 s67, 0\n"
    "s_cbranch_scc1 LW_q1\n"
    "s_sub_u32 s67, s67, 1\n"
    "s_cmp_eq_u32 s67, 0\n"
    "s_cbranch_scc1 LW_s2\n"
    "s_branch LW_d2\n"
    "LW_q1:\n"
    "ds_read_b64_tr_b16 a[32:33], v6 offset:32768\n"
    "ds_read_b64_tr_b16 a[34:35], v6 offset:33792\n"
    "ds_read_b64_tr_b16 a[36:37], v6 offset:34816\n"
    "ds_read_b64_tr_b16 a[38:39], v6 offset:35840\n"
    "ds_read_b64_tr_b16 a[40:41], v7 offset:32768\n"
    "ds_read_b64_tr_b16 a[42:43], v7 offset:33792\n"
    "ds_read_b64_tr_b16 a[44:45], v7 offset:34816\n"
    "ds_read_b64_tr_b16 a[46:47], v7 offset:35840\n"
    "ds_read_b64_tr_b16 a[48:49], v222 offset:0\n"
    "ds_read_b64_tr_b16 a[50:51], v222 offset:1024\n"
    "ds_read_b64_tr_b16 a[52:53], v222 offset:2048\n"
    "ds_read_b64_tr_b16 a[54:55], v222 offset:3072\n"
    "ds_read_b64_tr_b16 a[56:57], v223 offset:0\n"
    "ds_read_b64_tr_b16 a[58:59], v223 offset:1024\n"
    "ds_read_b64_tr_b16 a[60:61], v223 offset:2048\n"
    "ds_read_b64_tr_b16 a[62:63], v223 offset:3072\n"
    "s_mov_b32 s3, 2\n"
    "s_sub_u32 s66, s66, 1\n"
    "s_cmp_eq_u32 s66, 0\n"
    "s_cbranch_scc1 LW_done\n"
    "LW_s2:\n"
    "s_waitcnt vmcnt(8) lgkmcnt(0)\n"
    "s_cmp_lg_u32 s66, 1\n"
    "s_cbranch_scc1 LW_b2\n"
    "s_waitcnt vmcnt(0)\n"
    "LW_b2:\n"
    "s_barrier\n"
    "LW_d2:\n"
    "s_cmp_eq_u32 s68, 0\n"
    "s_cbranch_scc1 LW_r2\n"
    "s_sub_u32 s68, s68, 1\n"
    "s_add_u32 m0, s42, 32768\n"
    "s_mov_b32 s69, 2359296\n"
    "buffer_load_dwordx4 v5, s[24:27], s69 offen offset:0 lds\n"
    "buffer_load_dwordx4 v5, s[28:31], s69 offen offset:2048 lds\n"
    "buffer_load_dwordx4 v217, s[24:27], s69 offen offset:1024 lds\n"
    "buffer_load_dwordx4 v217, s[28:31], s69 offen offset:3072 lds\n"
    "s_add_u32 m0, s42, 49152\n"
    "s_mov_b32 s69, 2396160\n"
    "buffer_load_dwordx4 v5, s[24:27], s69 offen offset:0 lds\n"
    "buffer_load_dwordx4 v5, s[28:31], s69 offen offset:2048 lds\n"
    "buffer_load_dwordx4 v217, s[24:27], s69 offen offset:1024 lds\n"
    "buffer_load_dwordx4 v217, s[28:31], s69 offen offset:3072 lds\n"
    "LW_r2:\n"
    "s_cmp_eq_u32 s67, 0\n"
    "s_cbranch_scc1 LW_q2\n"
    "s_sub_u32 s67, s67, 1\n"
    "s_cmp_eq_u32 s67, 0\n"
    "s_cbranch_scc1 LW_s3\n"
    "s_branch LW_d3\n"
    "LW_q2:\n"
    "ds_read_b64_tr_b16 a[64:65], v222 offset:16384\n"
    "ds_read_b64_tr_b16 a[66:67], v222 offset:17408\n"
    "ds_read_b64_tr_b16 a[68:69], v222 offset:18432\n"
    "ds_read_b64_tr_b16 a[70:71], v222 offset:19456\n"
    "ds_read_b64_tr_b16 a[72:73], v223 offset:16384\n"
    "ds_read_b64_tr_b16 a[74:75], v223 offset:17408\n"
    "ds_read_b64_tr_b16 a[76:77], v223 offset:18432\n"
    "ds_read_b64_tr_b16 a[78:79], v223 offset:19456\n"
    "ds_read_b64_tr_b16 a[80:81], v222 offset:32768\n"
    "ds_read_b64_tr_b16 a[82:83], v222 offset:33792\n"
    "ds_read_b64_tr_b16 a[84:85], v222 offset:34816\n"
    "ds_read_b64_tr_b16 a[86:87], v222 offset:35840\n"
    "ds_read_b64_tr_b16 a[88:89], v223 offset:32768\n"
    "ds_read_b64_tr_b16 a[90:91], v223 offset:33792\n"
    "ds_read_b64_tr_b16 a[92:93], v223 offset:34816\n"
    "ds_read_b64_tr_b16 a[94:95], v223 offset:35840\n"
    "s_mov_b32 s3, 3\n"
    "s_sub_u32 s66, s66, 1\n"
    "s_cmp_eq_u32 s66, 0\n"
    "s_cbranch_scc1 LW_done\n"
    "LW_s3:\n"
    "s_waitcnt vmcnt(8) lgkmcnt(0)\n"
    "s_cmp_lg_u32 s66, 1\n"
    "s_cbranch_scc1 LW_b3\n"
    "s_waitcnt vmcnt(0)\n"
    "LW_b3:\n"
    "s_barrier\n"
    "LW_d3:\n"
    "s_cmp_eq_u32 s68, 0\n"
    "s_cbranch_scc1 LW_r3\n"
    "s_sub_u32 s68, s68, 1\n"
    "s_add_u32 m0, s42, 65536\n"
    "s_mov_b32 s69, 2433024\n"
    "buffer_load_dwordx4 v5, s[24:27], s69 offen offset:0 lds\n"
    "buffer_load_dwordx4 v5, s[28:31], s69 offen offset:2048 lds\n"
    "buffer_load_dwordx4 v217, s[24:27], s69 offen offset:1024 lds\n"
    "buffer_load_dwordx4 v217, s[28:31], s69 offen offset:3072 lds\n"
    "s_add_u32 m0, s42, 81920\n"
    "s_mov_b32 s69, 2469888\n"
    "buffer_load_dwordx4 v5, s[24:27], s69 offen offset:0 lds\n"
    "buffer_load_dwordx4 v5, s[28:31], s69 offen offset:2048 lds\n"
    "buffer_load_dwordx4 v217, s[24:27], s69 offen offset:1024 lds\n"
    "buffer_load_dwordx4 v217, s[28:31], s69 offen offset:3072 lds\n"
    "LW_r3:\n"
    "s_cmp_eq_u32 s67, 0\n"
    "s_cbranch_scc1 LW_q3\n"
    "s_sub_u32 s67, s67, 1\n"
    "s_cmp_eq_u32 s67, 0\n"
    "s_cbranch_scc1 LW_s4\n"
    "s_branch LW_d4\n"
    "LW_q3:\n"
    "ds_read_b64_tr_b16 a[96:97], v6 offset:0\n"
    "ds_read_b64_tr_b16 a[98:99], v6 offset:1024\n"
    "ds_read_b64_tr_b16 a[100:101], v6 offset:2048\n"
    "ds_read_b64_tr_b16 a[102:103], v6 offset:3072\n"
    "ds_read_b64_tr_b16 a[104:105], v7 offset:0\n"
    "ds_read_b64_tr_b16 a[106:107], v7 offset:1024\n"
    "ds_read_b64_tr_b16 a[108:109], v7 offset:2048\n"
    "ds_read_b64_tr_b16 a[110:111], v7 offset:3072\n"
    "ds_read_b64_tr_b16 a[112:113], v6 offset:16384\n"
    "ds_read_b64_tr_b16 a[114:115], v6 offset:17408\n"
    "ds_read_b64_tr_b16 a[116:117], v6 offset:18432\n"
    "ds_read_b64_tr_b16 a[118:119], v6 offset:19456\n"
    "ds_read_b64_tr_b16 a[120:121], v7 offset:16384\n"
    "ds_read_b64_tr_b16 a[122:123], v7 offset:17408\n"
    "ds_read_b64_tr_b16 a[124:125], v7 offset:18432\n"
    "ds_read_b64_tr_b16 a[126:127], v7 offset:19456\n"
    "s_mov_b32 s3, 4\n"
    "s_sub_u32 s66, s66, 1\n"
    "s_cmp_eq_u32 s66, 0\n"
    "s_cbranch_scc1 LW_done\n"
    "LW_s4:\n"
    "s_waitcnt vmcnt(8) lgkmcnt(0)\n"
    "s_cmp_lg_u32 s66, 1\n"
    "s_cbranch_scc1 LW_b4\n"
    "s_waitcnt vmcnt(0)\n"
    "LW_b4:\n"
    "s_barrier\n"
    "LW_d4:\n"
    "s_cmp_eq_u32 s68, 0\n"
    "s_cbranch_scc1 LW_r4\n"
    "s_sub_u32 s68, s68, 1\n"
    "s_add_u32 m0, s42, 0\n"
    "s_mov_b32 s69, 3538944\n"
    "buffer_load_dwordx4 v5, s[24:27], s69 offen offset:0 lds\n"
    "buffer_load_dwordx4 v5, s[28:31], s69 offen offset:2048 lds\n"
    "buffer_load_dwordx4 v217, s[24:27], s69 offen offset:1024 lds\n"
    "buffer_load_dwordx4 v217, s[28:31], s69 offen offset:3072 lds\n"
    "s_add_u32 m0, s42, 16384\n"
    "s_mov_b32 s69, 3575808\n"
    "buffer_load_dwordx4 v5, s[24:27], s69 offen offset:0 lds\n"
    "buffer_load_dwordx4 v5, s[28:31], s69 offen offset:2048 lds\n"
    "buffer_load_dwordx4 v217, s[24:27], s69 offen offset:1024 lds\n"
    "buffer_load_dwordx4 v217, s[28:31], s69 offen offset:3072 lds\n"
    "LW_r4:\n"
    "s_cmp_eq_u32 s67, 0\n"
    "s_cbranch_scc1 LW_q4\n"
    "s_sub_u32 s67, s67, 1\n"
    "s_cmp_eq_u32 s67, 0\n"
    "s_cbranch_scc1 LW_s5\n"
    "s_branch LW_d5\n"
    "LW_q4:\n"
    "ds_read_b64_tr_b16 a[128:129], v6 offset:32768\n"
    "ds_read_b64_tr_b16 a[130:131], v6 offset:33792\n"
    "ds_read_b64_tr_b16 a[132:133], v6 offset:34816\n"
    "ds_read_b64_tr_b16 a[134:135], v6 offset:35840\n"
    "ds_read_b64_tr_b16 a[136:137], v7 offset:32768\n"
    "ds_read_b64_tr_b16 a[138:139], v7 offset:33792\n"
    "ds_read_b64_tr_b16 a[140:141], v7 offset:34816\n"
    "ds_read_b64_tr_b16 a[142:143], v7 offset:35840\n"
    "ds_read_b64_tr_b16 a[144:145], v222 offset:0\n"
    "ds_read_b64_tr_b16 a[146:147], v222 offset:1024\n"
    "ds_read_b64_tr_b16 a[148:149], v222 offset:2048\n"
    "ds_read_b64_tr_b16 a[150:151], v222 offset:3072\n"
    "ds_read_b64_tr_b16 a[152:153], v223 offset:0\n"
    "ds_read_b64_tr_b16 a[154:155], v223 offset:1024\n"
    "ds_read_b64_tr_b16 a[156:157], v223 offset:2048\n"
    "ds_read_b64_tr_b16 a[158:159], v223 offset:3072\n"
    "LS_1:\n"
    "s_cbranch_execz LS_5\n"
    "s_mov_b32 s3, 5\n"
    "s_sub_u32 s66, s66, 1\n"
    "s_cmp_eq_u32 s66, 0\n"
    "s_cbranch_scc1 LW_done\n"
    "LW_s5:\n"
    "s_waitcnt vmcnt(8) lgkmcnt(0)\n"
    "s_cmp_lg_u32 s66, 1\n"
    "s_cbranch_scc1 LW_b5\n"
    "s_waitcnt vmcnt(0)\n"
    "LW_b5:\n"
    "s_barrier\n"
    "LW_d5:\n"
    "s_cmp_eq_u32 s68, 0\n"
    "s_cbranch_scc1 LW_r5\n"
    "s_sub_u32 s68, s68, 1\n"
    "s_add_u32 m0, s42, 32768\n"
    "s_mov_b32 s69, 3612672\n"
    "buffer_load_dwordx4 v5, s[24:27], s69 offen offset:0 lds\n"
    "buffer_load_dwordx4 v5, s[28:31], s69 offen offset:2048 lds\n"
    "buffer_load_dwordx4 v217, s[24:27], s69 offen offset:1024 lds\n"
    "buffer_load_dwordx4 v217, s[28:31], s69 offen offset:3072 lds\n"
    "s_add_u32 m0, s42, 49152\n"
    "s_mov_b32 s69, 3649536\n"
    "buffer_load_dwordx4 v5, s[24:27], s69 offen offset:0 lds\n"
    "buffer_load_dwordx4 v5, s[28:31], s69 offen offset:2048 lds\n"
    "buffer_load_dwordx4 v217, s[24:27], s69 offen offset:1024 lds\n"
    "buffer_load_dwordx4 v217, s[28:31], s69 offen offset:3072 lds\n"
    "LW_r5:\n"
    "s_cmp_eq_u32 s67, 0\n"
    "s_cbranch_scc1 LW_q5\n"
    "s_sub_u32 s67, s67, 1\n"
    "s_cmp_eq_u32 s67, 0\n"
    "s_cbranch_scc1 LW_s6\n"
    "s_branch LW_d6\n"
    "LW_q5:\n"
    "ds_read_b64_tr_b16 a[160:161], v222 offset:16384\n"
    "ds_read_b64_tr_b16 a[162:163], v222 offset:17408\n"
    "ds_read_b64_tr_b16 a[164:165], v222 offset:18432\n"
    "ds_read_b64_tr_b16 a[166:167], v222 offset:19456\n"
    "ds_read_b64_tr_b16 a[168:169], v223 offset:16384\n"
    "ds_read_b64_tr_b16 a[170:171], v223 offset:17408\n"
    "ds_read_b64_tr_b16 a[172:173], v223 offset:18432\n"
    "ds_read_b64_tr_b16 a[174:175], v223 offset:19456\n"
    "ds_read_b64_tr_b16 a[176:177], v222 offset:32768\n"
    "ds_read_b64_tr_b16 a[178:179], v222 offset:33792\n"
    "ds_read_b64_tr_b16 a[180:181], v222 offset:34816\n"
    "ds_read_b64_tr_b16 a[182:183], v222 offset:35840\n"
    "ds_read_b64_tr_b16 a[184:185], v223 offset:32768\n"
    "ds_read_b64_tr_b16 a[186:187], v223 offset:33792\n"
    "ds_read_b64_tr_b16 a[188:189], v223 offset:34816\n"
    "ds_read_b64_tr_b16 a[190:191], v223 offset:35840\n"
    "s_mov_b32 s3, 6\n"
    "s_sub_u32 s66, s66, 1\n"
    "s_cmp_eq_u32 s66, 0\n"
    "s_cbranch_scc1 LW_done\n"
    "LW_s6:\n"
    "s_waitcnt vmcnt(8) lgkmcnt(0)\n"
    "s_cmp_lg_u32 s66, 1\n"
    "s_cbranch_scc1 LW_b6\n"
    "s_waitcnt vmcnt(0)\n"
    "LW_b6:\n"
    "s_barrier\n"
    "LW_d6:\n"
    "s_cmp_eq_u32 s68, 0\n"
    "s_cbranch_scc1 LW_r6\n"
    "s_sub_u32 s68, s68, 1\n"
    "s_add_u32 m0, s42, 65536\n"
    "s_mov_b32 s69, 4718592\n"
    "s_cmp_lt_u32 s32, 2\n"
    "s_cselect_b32 s69, s69, 4460544\n"
    "buffer_load_dwordx4 v5, s[24:27], s69 offen offset:0 lds\n"
    "buffer_load_dwordx4 v5, s[28:31], s69 offen offset:2048 lds\n"
    "s_sub_u32 s69, s69, 294912\n"
    "buffer_load_dwordx4 v217, s[24:27], s69 offen offset:1024 lds\n"
    "buffer_load_dwordx4 v217, s[28:31], s69 offen offset:3072 lds\n"
    "s_add_u32 m0, s42, 81920\n"
    "s_mov_b32 s69, 4792320\n"
    "s_cmp_lt_u32 s32, 2\n"
    "s_cselect_b32 s69, s69, 4534272\n"
    "buffer_load_dwordx4 v5, s[24:27], s69 offen offset:0 lds\n"
    "buffer_load_dwordx4 v5, s[28:31], s69 offen offset:2048 lds\n"
    "s_sub_u32 s69, s69, 294912\n"
    "buffer_load_dwordx4 v217, s[24:27], s69 offen offset:1024 lds\n"
    "buffer_load_dwordx4 v217, s[28:31], s69 offen offset:3072 lds\n"
    "LW_r6:\n"
    "s_cmp_eq_u32 s67, 0\n"
    "s_cbranch_scc1 LW_q6\n"
    "s_sub_u32 s67, s67, 1\n"
    "s_cmp_eq_u32 s67, 0\n"
    "s_cbranch_scc1 LW_s7\n"
    "s_branch LW_d7\n"
    "LW_q6:\n"
    "ds_read_b64_tr_b16 a[192:193], v6 offset:0\n"
    "ds_read_b64_tr_b16 a[194:195], v6 offset:1024\n"
    "ds_read_b64_tr_b16 a[196:197], v6 offset:2048\n"
    "ds_read_b64_tr_b16 a[198:199], v6 offset:3072\n"
    "ds_read_b64_tr_b16 a[200:201], v7 offset:0\n"
    "ds_read_b64_tr_b16 a[202:203], v7 offset:1024\n"
    "ds_read_b64_tr_b16 a[204:205], v7 offset:2048\n"
    "ds_read_b64_tr_b16 a[206:207], v7 offset:3072\n"
    "ds_read_b64_tr_b16 a[208:209], v6 offset:16384\n"
    "ds_read_b64_tr_b16 a[210:211], v6 offset:17408\n"
    "ds_read_b64_tr_b16 a[212:213], v6 offset:18432\n"
    "ds_read_b64_tr_b16 a[214:215], v6 offset:19456\n"
    "ds_read_b64_tr_b16 a[216:217], v7 offset:16384\n"
    "ds_read_b64_tr_b16 a[218:219], v7 offset:17408\n"
    "ds_read_b64_tr_b16 a[220:221], v7 offset:18432\n"
    "ds_read_b64_tr_b16 a[222:223], v7 offset:19456\n"
    "s_mov_b32 s3, 7\n"
    "s_sub_u32 s66, s66, 1\n"
    "s_cmp_eq_u32 s66, 0\n"
    "s_cbranch_scc1 LW_done\n"
    "LW_s7:\n"
    "s_waitcnt vmcnt(8) lgkmcnt(0)\n"
    "s_cmp_lg_u32 s66, 1\n"
    "s_cbranch_scc1 LW_b7\n"
    "s_waitcnt vmcnt(0)\n"
    "LW_b7:\n"
    "s_barrier\n"
    "LW_d7:\n"
    "s_cmp_eq_u32 s68, 0\n"
    "s_cbranch_scc1 LW_r7\n"
    "s_sub_u32 s68, s68, 1\n"
    "s_add_u32 m0, s42, 0\n"
    "s_mov_b32 s69, 0\n"
    "buffer_load_dwordx4 v5, s[24:27], s69 offen offset:0 lds\n"
    "buffer_load_dwordx4 v5, s[28:31], s69 offen offset:2048 lds\n"
    "buffer_load_dwordx4 v217, s[24:27], s69 offen offset:1024 lds\n"
    "buffer_load_dwordx4 v217, s[28:31], s69 offen offset:3072 lds\n"
    "s_add_u32 m0, s42, 16384\n"
    "s_mov_b32 s69, 36864\n"
    "buffer_load_dwordx4 v5, s[24:27], s69 offen offset:0 lds\n"
    "buffer_load_dwordx4 v5, s[28:31], s69 offen offset:2048 lds\n"
    "buffer_load_dwordx4 v217, s[24:27], s69 offen offset:1024 lds\n"
    "buffer_load_dwordx4 v217, s[28:31], s69 offen offset:3072 lds\n"
    "LW_r7:\n"
    "s_cmp_eq_u32 s67, 0\n"
    "s_cbranch_scc1 LW_q7\n"
    "s_sub_u32 s67, s67, 1\n"
    "s_cmp_eq_u32 s67, 0\n"
    "s_cbranch_scc1 LW_s8\n"
    "s_branch LW_d8\n"
    "LW_q7:\n"
    "ds_read_b64_tr_b16 a[224:225], v6 offset:32768\n"
    "ds_read_b64_tr_b16 a[226:227], v6 offset:33792\n"
    "ds_read_b64_tr_b16 a[228:229], v6 offset:34816\n"
    "ds_read_b64_tr_b16 a[230:231], v6 offset:35840\n"
    "ds_read_b64_tr_b16 a[232:233], v7 offset:32768\n"
    "ds_read_b64_tr_b16 a[234:235], v7 offset:33792\n"
    "ds_read_b64_tr_b16 a[236:237], v7 offset:34816\n"
    "ds_read_b64_tr_b16 a[238:239], v7 offset:35840\n"
    "ds_read_b64_tr_b16 a[240:241], v222 offset:0\n"
    "ds_read_b64_tr_b16 a[242:243], v222 offset:1024\n"
    "ds_read_b64_tr_b16 a[244:245], v222 offset:2048\n"
    "ds_read_b64_tr_b16 a[246:247], v222 offset:3072\n"
    "ds_read_b64_tr_b16 a[248:249], v223 offset:0\n"
    "ds_read_b64_tr_b16 a[250:251], v223 offset:1024\n"
    "ds_read_b64_tr_b16 a[252:253], v223 offset:2048\n"
    "ds_read_b64_tr_b16 a[254:255], v223 offset:3072\n"
    "s_mov_b32 s3, 8\n"
    "s_sub_u32 s66, s66, 1\n"
    "s_cmp_eq_u32 s66, 0\n"
    "s_cbranch_scc1 LW_done\n"
    "LW_s8:\n"
    "s_waitcnt vmcnt(8) lgkmcnt(0)\n"
    "s_cmp_lg_u32 s66, 1\n"
    "s_cbranch_scc1 LW_b8\n"
    "s_waitcnt vmcnt(0)\n"
    "LW_b8:\n"
    "s_barrier\n"
    "LW_d8:\n"
    "s_cmp_eq_u32 s68, 0\n"
    "s_cbranch_scc1 LW_r8\n"
    "s_sub_u32 s68, s68, 1\n"
    "s_add_u32 m0, s42, 32768\n"
    "s_mov_b32 s69, 73728\n"
    "buffer_load_dwordx4 v5, s[24:27], s69 offen offset:0 lds\n"
    "buffer_load_dwordx4 v5, s[28:31], s69 offen offset:2048 lds\n"
    "buffer_load_dwordx4 v217, s[24:27], s69 offen offset:1024 lds\n"
    "buffer_load_dwordx4 v217, s[28:31], s69 offen offset:3072 lds\n"
    "s_add_u32 m0, s42, 49152\n"
    "s_mov_b32 s69, 110592\n"
    "buffer_load_dwordx4 v5, s[24:27], s69 offen offset:0 lds\n"
    "buffer_load_dwordx4 v5, s[28:31], s69 offen offset:2048 lds\n"
    "buffer_load_dwordx4 v217, s[24:27], s69 offen offset:1024 lds\n"
    "buffer_load_dwordx4 v217, s[28:31], s69 offen offset:3072 lds\n"
    "LW_r8:\n"
    "s_cmp_eq_u32 s67, 0\n"
    "s_cbranch_scc1 LW_q8\n"
    "s_sub_u32 s67, s67, 1\n"
    "s_cmp_eq_u32 s67, 0\n"
    "s_cbranch_scc1 LW_s0\n"
    "s_branch LW_d0\n"
    "LW_q8:\n"
    "ds_read_b64_tr_b16 v[224:225], v222 offset:16384\n"
    "ds_read_b64_tr_b16 v[226:227], v222 offset:17408\n"
    "ds_read_b64_tr_b16 v[228:229], v222 offset:18432\n"
    "ds_read_b64_tr_b16 v[230:231], v222 offset:19456\n"
    "ds_read_b64_tr_b16 v[232:233], v223 offset:16384\n"
    "ds_read_b64_tr_b16 v[234:235], v223 offset:17408\n"
    "ds_read_b64_tr_b16 v[236:237], v223 offset:18432\n"
    "ds_read_b64_tr_b16 v[238:239], v223 offset:19456\n"
    "ds_read_b64_tr_b16 v[240:241], v222 offset:32768\n"
    "ds_read_b64_tr_b16 v[242:243], v222 offset:33792\n"
    "ds_read_b64_tr_b16 v[244:245], v222 offset:34816\n"
    "ds_read_b64_tr_b16 v[246:247], v222 offset:35840\n"
    "ds_read_b64_tr_b16 v[248:249], v223 offset:32768\n"
    "ds_read_b64_tr_b16 v[250:251], v223 offset:33792\n"
    "ds_read_b64_tr_b16 v[252:253], v223 offset:34816\n"
    "ds_read_b64_tr_b16 v[254:255], v223 offset:35840\n"
    "s_mov_b32 s3, 0\n"
    "s_sub_u32 s66, s66, 1\n"
    "s_cmp_eq_u32 s66, 0\n"
    "s_cbranch_scc1 LW_done\n"
    "s_branch LW_s0\n"
    "LW_done:\n"
    "s_waitcnt lgkmcnt(0)\n"
    "s_barrier\n"
    "LW_ret:\n"
    "s_setpc_b64 s[0:1]\n"
    "LS_entry:\n"
    "s_mov_b64 exec, 0\n"
    "s_cmp_eq_u32 s32, 0\n"
    "s_cbranch_scc1 LS_0\n"
    "s_cmp_eq_u32 s32, 1\n"
    "s_cbranch_scc1 LS_1\n"
    "s_cmp_eq_u32 s32, 2\n"
    "s_cbranch_scc1 LS_2\n"
    "s_branch LS_3\n"
    "LS_ret:\n"
    "s_mov_b64 exec, -1\n"
    "s_setpc_b64 s[0:1]\n"
    "LI2_entry:\n"
    "s_mov_b64 s[24:25], s[10:11]\n"
    "s_mov_b32 s26, 0x510000\n"
    "s_mov_b32 s27, 0x20000\n"
    "s_sub_u32 s28, s12, 0x800\n"
    "s_subb_u32 s29, s13, 0\n"
    "s_mov_b32 s30, 0x510800\n"
    "s_mov_b32 s31, 0x20000\n"
    "s_bfe_u32 s34, s38, 0x60007\n"
    "s_bfe_u32 s35, s38, 0x3000d\n"
    "s_bfe_u32 s36, s38, 0x70012\n"
    "s_bitcmp1_b32 s38, 16\n"
    "s_cselect_b32 s46, 9, 0\n"
    "s_lshl_b32 s42, s32, 12\n"
    "s_add_u32 s42, s42, 0x10000\n"
    "s_mov_b32 s3, 0\n"
    "s_and_b32 s54, s32, 1\n"
    "s_lshl_b32 s54, s54, 3\n"
    "v_lshl_or_b32 v211, v3, 1, s54\n"
    "v_xor_b32 v211, v211, v2\n"
    "v_lshlrev_b32 v211, 4, v211\n"
    "s_mul_i32 s55, s32, 0x24000\n"
    "s_lshl_b32 s56, s34, 8\n"
    "s_add_u32 s55, s55, s56\n"
    "v_mul_u32_u24 v210, 0x2400, v3\n"
    "v_add3_u32 v5, v211, v210, s55\n"
    "v_add_u32 v217, 0x8fc00, v5\n"
    "v_bfe_u32 v209, v1, 2, 2\n"
    "v_and_b32 v208, 3, v1\n"
    "v_and_b32 v207, 1, v3\n"
    "v_lshlrev_b32 v207, 3, v207\n"
    "v_lshl_or_b32 v207, v209, 1, v207\n"
    "s_lshl_b32 s56, s32, 2\n"
    "v_lshrrev_b32 v206, 1, v208\n"
    "v_add_u32 v206, s56, v206\n"
    "v_xor_b32 v207, v207, v206\n"
    "v_lshlrev_b32 v207, 4, v207\n"
    "v_and_b32 v208, 1, v208\n"
    "v_lshl_add_u32 v207, v208, 3, v207\n"
    "v_lshl_add_u32 v209, v3, 4, v209\n"
    "v_lshl_add_u32 v207, v209, 8, v207\n"
    "v_add_u32 v6, 0x10000, v207\n"
    "v_xor_b32 v7, 32, v6\n"
    "v_add_u32 v222, 0xc000, v6\n"
    "v_add_u32 v223, 0xc000, v7\n"
    "s_setpc_b64 s[0:1]\n"
    "LGS_entry:\n"
    "s_lshr_b32 s10, s47, 1\n"
    "s_and_b32 s11, s47, 1\n"
    "s_lshl_b32 s11, s11, 3\n"
    "s_lshr_b32 s77, s48, 2\n"
    "s_add_u32 s11, s11, s77\n"
    "s_and_b32 s12, s48, 3\n"
    "s_lshl_b32 s77, s11, 2\n"
    "s_add_u32 s77, s77, s12\n"
    "s_lshl_b32 s77, s77, 2\n"
    "s_add_u32 s77, s77, s32\n"
    "s_lshl_b32 s13, s10, 8\n"
    "s_add_u32 s13, s13, s77\n"
    "s_mul_i32 s13, s13, 0x1c00\n"
    "s_add_u32 s13, s13, 0x13b2000\n"
    "s_add_u32 s78, s77, 0x100\n"
    "s_cmp_lt_u32 s78, 0x144\n"
    "s_cselect_b32 s6, s22, 0\n"
    "s_cselect_b32 s78, s78, s77\n"
    "s_lshl_b32 s84, s32, 13\n"
    "s_add_u32 s84, s84, 0x12000\n"
    "s_mov_b32 s18, 0\n"
    "s_mul_i32 s85, s77, 0x1c72\n"
    "s_lshr_b32 s85, s85, 16\n"
    "s_mul_i32 s81, s85, 9\n"
    "s_sub_u32 s81, s77, s81\n"
    "s_mul_i32 s79, s10, 36\n"
    "s_add_u32 s85, s85, s79\n"
    "s_mul_i32 s86, s85, 0x9000\n"
    "s_lshl_b32 s79, s81, 7\n"
    "s_add_u32 s86, s86, s79\n"
    "s_mul_i32 s85, s85, 0x2400\n"
    "s_add_u32 s85, s85, 0xea2000\n"
    "s_lshr_b32 s79, s81, 1\n"
    "s_mul_i32 s79, s79, 0x144000\n"
    "s_add_u32 s79, s79, s85\n"
    "s_lshl_b32 s80, s81, 1\n"
    "s_add_u32 s80, s80, 3\n"
    "s_sub_u32 s80, s80, s10\n"
    "s_and_b32 s80, s80, 3\n"
    "s_lshl_b32 s80, s80, 11\n"
    "s_add_u32 s80, s80, s79\n"
    "s_add_u32 s80, s80, 0x400\n"
    "s_add_u32 s88, s85, 0\n"
    "s_cmp_lt_u32 s81, 8\n"
    "s_cselect_b32 s88, s80, s88\n"
    "s_add_u32 s80, s80, 0x400\n"
    "s_add_u32 s89, s85, 0x144000\n"
    "s_cmp_lt_u32 s81, 8\n"
    "s_cselect_b32 s89, s80, s89\n"
    "s_lshl_b32 s80, s81, 1\n"
    "s_add_u32 s80, s80, 4\n"
    "s_sub_u32 s80, s80, s10\n"
    "s_and_b32 s80, s80, 3\n"
    "s_lshl_b32 s80, s80, 11\n"
    "s_add_u32 s80, s80, s79\n"
    "s_add_u32 s80, s80, 0x400\n"
    "s_add_u32 s90, s85, 0x288000\n"
    "s_cmp_lt_u32 s81, 8\n"
    "s_cselect_b32 s90, s80, s90\n"
    "s_add_u32 s80, s80, 0x400\n"
    "s_add_u32 s91, s85, 0x3cc000\n"
    "s_cmp_lt_u32 s81, 8\n"
    "s_cselect_b32 s91, s80, s91\n"
    "s_lshr_b32 s81, s81, 3\n"
    "s_lshl_b32 s81, s81, 0\n"
    "s_or_b32 s18, s18, s81\n"
    "s_mul_i32 s85, s78, 0x1c72\n"
    "s_lshr_b32 s85, s85, 16\n"
    "s_mul_i32 s81, s85, 9\n"
    "s_sub_u32 s81, s78, s81\n"
    "s_mul_i32 s79, s10, 36\n"
    "s_add_u32 s85, s85, s79\n"
    "s_mul_i32 s87, s85, 0x9000\n"
    "s_lshl_b32 s79, s81, 7\n"
    "s_add_u32 s87, s87, s79\n"
    "s_mul_i32 s85, s85, 0x2400\n"
    "s_add_u32 s85, s85, 0xea2000\n"
    "s_lshr_b32 s79, s81, 1\n"
    "s_mul_i32 s79, s79, 0x144000\n"
    "s_add_u32 s79, s79, s85\n"
    "s_lshl_b32 s80, s81, 1\n"
    "s_add_u32 s80, s80, 3\n"
    "s_sub_u32 s80, s80, s10\n"
    "s_and_b32 s80, s80, 3\n"
    "s_lshl_b32 s80, s80, 11\n"
    "s_add_u32 s80, s80, s79\n"
    "s_add_u32 s80, s80, 0x400\n"
    "s_add_u32 s92, s85, 0\n"
    "s_cmp_lt_u32 s81, 8\n"
    "s_cselect_b32 s92, s80, s92\n"
    "s_add_u32 s80, s80, 0x400\n"
    "s_add_u32 s93, s85, 0x144000\n"
    "s_cmp_lt_u32 s81, 8\n"
    "s_cselect_b32 s93, s80, s93\n"
    "s_lshl_b32 s80, s81, 1\n"
    "s_add_u32 s80, s80, 4\n"
    "s_sub_u32 s80, s80, s10\n"
    "s_and_b32 s80, s80, 3\n"
    "s_lshl_b32 s80, s80, 11\n"
    "s_add_u32 s80, s80, s79\n"
    "s_add_u32 s80, s80, 0x400\n"
    "s_add_u32 s94, s85, 0x288000\n"
    "s_cmp_lt_u32 s81, 8\n"
    "s_cselect_b32 s94, s80, s94\n"
    "s_add_u32 s80, s80, 0x400\n"
    "s_add_u32 s95, s85, 0x3cc000\n"
    "s_cmp_lt_u32 s81, 8\n"
    "s_cselect_b32 s95, s80, s95\n"
    "s_lshr_b32 s81, s81, 3\n"
    "s_lshl_b32 s81, s81, 1\n"
    "s_or_b32 s18, s18, s81\n"
    "s_lshl_b32 s85, s11, 3\n"
    "s_lshr_b32 s8, s38, 25\n"
    "s_mul_i32 s80, s12, 4\n"
    "s_add_u32 s80, s80, s10\n"
    "s_mul_i32 s80, s80, 0x51000\n"
    "s_mul_i32 s70, s32, 0x2400\n"
    "s_add_u32 s80, s80, s70\n"
    "s_add_u32 s80, s80, 0xea2000\n"
    "s_mul_i32 s83, s32, 0x1400\n"
    "s_lshl_b32 s72, s32, 11\n"
    "s_lshl_b32 s78, s32, 1\n"
    "s_add_u32 s7, s85, s78\n"
    "s_mul_i32 s82, s7, 0x90\n"
    "s_mul_i32 s78, s10, 36\n"
    "s_add_u32 s82, s82, s78\n"
    "s_lshl_b32 s82, s82, 10\n"
    "s_add_u32 s82, s82, 0x1ab2000\n"
    "s_add_u32 s19, s36, s35\n"
    "s_lshl_b32 s76, s34, 2\n"
    "s_add_u32 s76, s76, s32\n"
    "s_lshl_b32 s76, s76, 10\n"
    "s_add_u32 s76, s76, 0x1ab2000\n"
    "s_mul_i32 s78, s33, 0x24000\n"
    "s_add_u32 s74, s76, s78\n"
    "s_and_b32 s78, s32, 1\n"
    "s_mul_i32 s73, s78, 0x2400\n"
    "s_add_u32 s73, s73, 0xa22000\n"
    "s_lshr_b32 s78, s32, 1\n"
    "s_mul_i32 s78, s78, 7\n"
    "s_add_u32 s75, s78, s19\n"
    "s_and_b32 s78, s75, 0x7f\n"
    "s_mul_i32 s71, s78, 0x4800\n"
    "s_add_u32 s71, s71, s73\n"
    "s_setpc_b64 s[0:1]\n"
    "LM_A1_7:\n"
    "LM_P3_5:\n"
    "s_lshl_b32 s57, s32, 12\n"
    "s_mul_i32 s64, s33, 0x4800\n"
    "s_add_u32 s64, s64, 0xa22000\n"
    "s_lshl_b32 s61, s32, 9\n"
    "s_add_u32 s61, s61, s64\n"
    "s_add_u32 s61, s61, 0x4000\n"
    "s_lshl_b32 s62, s32, 10\n"
    "s_add_u32 s64, s64, s62\n"
    "s_lshr_b32 s50, s32, 1\n"
    "s_and_b32 s51, s32, 1\n"
    "s_lshl_b32 s51, s51, 3\n"
    "s_mul_i32 s53, s32, 0x1400\n"
    "s_add_u32 s53, s53, 0x11000\n"
    "s_lshl_b32 s52, s33, 7\n"
    "s_add_u32 s52, s52, 0x16000\n"
    "s_lshl_b32 s54, s32, 2\n"
    "s_add_u32 s55, s32, 16\n"
    "s_mul_i32 s65, s32, 0x1400\n"
    "s_add_u32 s65, s65, 0x8000\n"
    "s_lshl_b32 s56, s32, 8\n"
    "s_lshl_b32 s58, s32, 10\n"
    "s_lshl_b32 s59, s32, 7\n"
    "s_mul_i32 s60, s33, 0x5000\n"
    "s_add_u32 s60, s60, 0x560000\n"
    "s_lshr_b32 s63, s32, 1\n"
    "s_and_b32 s62, s32, 1\n"
    "s_lshl_b32 s63, s63, 11\n"
    "s_lshl_b32 s62, s62, 6\n"
    "s_add_u32 s63, s63, s62\n"
    "s_add_u32 s63, s63, s60\n"
    "s_add_u32 s63, s63, 0x4000\n"
    "s_add_u32 s60, s60, s57\n"
    "s_add_u32 m0, s42, 0\n"
    "s_mov_b32 s69, 3538944\n"
    "v_lshl_add_u32 v189, v3, 1, s50\n"
    "buffer_load_dwordx4 v5, s[24:27], s69 offen offset:0 lds\n"
    "v_lshrrev_b32 v188, 1, v2\n"
    "v_xor_b32 v189, v189, v188\n"
    "v_lshlrev_b32 v189, 4, v189\n"
    "v_lshl_add_u32 v187, v2, 7, v189\n"
    "v_add_u32 v187, s51, v187\n"
    "ds_read_b64 v[8:9], v187 offset:32768\n"
    "v_min_u32 v186, 7, v1\n"
    "ds_read_b64 v[10:11], v187 offset:34816\n"
    "v_lshl_add_u32 v186, v186, 7, s49\n"
    "ds_read_b64 v[12:13], v187 offset:36864\n"
    "buffer_load_dwordx4 v5, s[28:31], s69 offen offset:2048 lds\n"
    "ds_read_b64 v[14:15], v187 offset:38912\n"
    "v_cvt_f32_u32 v185, v2\n"
    "ds_read_b64 v[16:17], v187 offset:40960\n"
    "v_mul_f32 v185, 0x3ae38e39, v185\n"
    "ds_read_b64 v[18:19], v187 offset:43008\n"
    "v_cvt_f32_u32 v189, v3\n"
    "ds_read_b64 v[20:21], v187 offset:45056\n"
    "v_lshrrev_b32 v188, 1, v3\n"
    "ds_read_b64 v[22:23], v187 offset:47104\n"
    "v_mul_lo_u32 v184, v188, s44\n"
    "buffer_load_dwordx4 v217, s[24:27], s69 offen offset:1024 lds\n"
    "ds_read_b64 v[24:25], v187 offset:49152\n"
    "v_lshlrev_b32 v188, 31, v188\n"
    "v_xor_b32 v185, v188, v185\n"
    "v_mul_u32_u24 v183, 0x90, v3\n"
    "v_add_u32 v182, s54, v183\n"
    "v_add_u32 v181, s55, v183\n"
    "v_cvt_f32_u32 v182, v182\n"
    "v_cvt_f32_u32 v181, v181\n"
    "v_mul_f32 v182, v185, v182\n"
    "v_mul_f32 v181, v185, v181\n"
    "buffer_load_dwordx4 v217, s[28:31], s69 offen offset:3072 lds\n"
    "s_add_u32 m0, s42, 16384\n"
    "s_mov_b32 s69, 3575808\n"
    "v_cos_f32 v178, v182\n"
    "v_sin_f32 v179, v182\n"
    "v_fmamk_f32 v188, v185, 0x42100000, v182\n"
    "v_cos_f32 v176, v188\n"
    "v_cvt_pk_bf16_f32 v28, v178, -v179\n"
    "buffer_load_dwordx4 v5, s[24:27], s69 offen offset:0 lds\n"
    "v_cvt_pk_bf16_f32 v32, v179, v178\n"
    "v_xor_b32 v32, v184, v32\n"
    "v_sin_f32 v177, v188\n"
    "v_fmamk_f32 v187, v185, 0x42900000, v182\n"
    "v_cos_f32 v174, v187\n"
    "v_cvt_pk_bf16_f32 v29, v176, -v177\n"
    "v_cvt_pk_bf16_f32 v33, v177, v176\n"
    "v_xor_b32 v33, v184, v33\n"
    "buffer_load_dwordx4 v5, s[28:31], s69 offen offset:2048 lds\n"
    "v_sin_f32 v175, v187\n"
    "v_fmamk_f32 v183, v185, 0x42d80000, v182\n"
    "v_cos_f32 v172, v183\n"
    "v_cvt_pk_bf16_f32 v30, v174, -v175\n"
    "v_cvt_pk_bf16_f32 v34, v175, v174\n"
    "v_xor_b32 v34, v184, v34\n"
    "v_sin_f32 v173, v183\n"
    "buffer_load_dwordx4 v217, s[24:27], s69 offen offset:1024 lds\n"
    "v_fmamk_f32 v188, v185, 0x3f800000, v182\n"
    "v_cvt_pk_bf16_f32 v31, v172, -v173\n"
    "v_cvt_pk_bf16_f32 v35, v173, v172\n"
    "v_xor_b32 v35, v184, v35\n"
    "v_cos_f32 v178, v188\n"
    "v_sin_f32 v179, v188\n"
    "v_fmamk_f32 v187, v185, 0x42140000, v182\n"
    "v_cos_f32 v176, v187\n"
    "buffer_load_dwordx4 v217, s[28:31], s69 offen offset:3072 lds\n"
    "s_add_u32 m0, s42, 32768\n"
    "s_mov_b32 s69, 3612672\n"
    "v_cvt_pk_bf16_f32 v36, v178, -v179\n"
    "v_cvt_pk_bf16_f32 v40, v179, v178\n"
    "v_xor_b32 v40, v184, v40\n"
    "v_sin_f32 v177, v187\n"
    "v_fmamk_f32 v183, v185, 0x42920000, v182\n"
    "v_cos_f32 v174, v183\n"
    "buffer_load_dwordx4 v5, s[24:27], s69 offen offset:0 lds\n"
    "v_cvt_pk_bf16_f32 v37, v176, -v177\n"
    "v_cvt_pk_bf16_f32 v41, v177, v176\n"
    "v_xor_b32 v41, v184, v41\n"
    "v_sin_f32 v175, v183\n"
    "v_fmamk_f32 v188, v185, 0x42da0000, v182\n"
    "v_cos_f32 v172, v188\n"
    "v_cvt_pk_bf16_f32 v38, v174, -v175\n"
    "v_cvt_pk_bf16_f32 v42, v175, v174\n"
    "buffer_load_dwordx4 v5, s[28:31], s69 offen offset:2048 lds\n"
    "v_xor_b32 v42, v184, v42\n"
    "v_sin_f32 v173, v188\n"
    "v_fmamk_f32 v187, v185, 0x40000000, v182\n"
    "v_cos_f32 v178, v187\n"
    "v_cvt_pk_bf16_f32 v39, v172, -v173\n"
    "v_cvt_pk_bf16_f32 v43, v173, v172\n"
    "v_xor_b32 v43, v184, v43\n"
    "v_sin_f32 v179, v187\n"
    "buffer_load_dwordx4 v217, s[24:27], s69 offen offset:1024 lds\n"
    "v_fmamk_f32 v183, v185, 0x42180000, v182\n"
    "v_cvt_pk_bf16_f32 v44, v178, -v179\n"
    "v_cvt_pk_bf16_f32 v48, v179, v178\n"
    "v_xor_b32 v48, v184, v48\n"
    "v_cos_f32 v176, v183\n"
    "v_sin_f32 v177, v183\n"
    "v_fmamk_f32 v188, v185, 0x42940000, v182\n"
    "v_cos_f32 v174, v188\n"
    "buffer_load_dwordx4 v217, s[28:31], s69 offen offset:3072 lds\n"
    "s_add_u32 m0, s42, 49152\n"
    "s_mov_b32 s69, 3649536\n"
    "v_cvt_pk_bf16_f32 v45, v176, -v177\n"
    "v_cvt_pk_bf16_f32 v49, v177, v176\n"
    "v_xor_b32 v49, v184, v49\n"
    "v_sin_f32 v175, v188\n"
    "v_fmamk_f32 v187, v185, 0x42dc0000, v182\n"
    "v_cos_f32 v178, v187\n"
    "buffer_load_dwordx4 v5, s[24:27], s69 offen offset:0 lds\n"
    "v_cvt_pk_bf16_f32 v46, v174, -v175\n"
    "v_cvt_pk_bf16_f32 v50, v175, v174\n"
    "v_xor_b32 v50, v184, v50\n"
    "v_sin_f32 v179, v187\n"
    "v_fmamk_f32 v183, v185, 0x40400000, v182\n"
    "v_cos_f32 v172, v183\n"
    "v_cvt_pk_bf16_f32 v47, v178, -v179\n"
    "v_cvt_pk_bf16_f32 v51, v179, v178\n"
    "buffer_load_dwordx4 v5, s[28:31], s69 offen offset:2048 lds\n"
    "v_xor_b32 v51, v184, v51\n"
    "v_sin_f32 v173, v183\n"
    "v_fmamk_f32 v188, v185, 0x421c0000, v182\n"
    "v_cos_f32 v176, v188\n"
    "v_cvt_pk_bf16_f32 v52, v172, -v173\n"
    "v_cvt_pk_bf16_f32 v56, v173, v172\n"
    "v_xor_b32 v56, v184, v56\n"
    "v_sin_f32 v177, v188\n"
    "buffer_load_dwordx4 v217, s[24:27], s69 offen offset:1024 lds\n"
    "v_fmamk_f32 v187, v185, 0x42960000, v182\n"
    "v_cvt_pk_bf16_f32 v53, v176, -v177\n"
    "v_cvt_pk_bf16_f32 v57, v177, v176\n"
    "v_xor_b32 v57, v184, v57\n"
    "v_cos_f32 v178, v187\n"
    "v_sin_f32 v179, v187\n"
    "v_fmamk_f32 v183, v185, 0x42de0000, v182\n"
    "v_cos_f32 v174, v183\n"
    "buffer_load_dwordx4 v217, s[28:31], s69 offen offset:3072 lds\n"
    "s_add_u32 m0, s42, 65536\n"
    "s_mov_b32 s69, 4718592\n"
    "s_cmp_lt_u32 s32, 2\n"
    "s_cselect_b32 s69, s69, 4460544\n"
    "v_cvt_pk_bf16_f32 v54, v178, -v179\n"
    "v_cvt_pk_bf16_f32 v58, v179, v178\n"
    "v_xor_b32 v58, v184, v58\n"
    "v_sin_f32 v175, v183\n"
    "v_cos_f32 v176, v181\n"
    "buffer_load_dwordx4 v5, s[24:27], s69 offen offset:0 lds\n"
    "v_cvt_pk_bf16_f32 v55, v174, -v175\n"
    "v_cvt_pk_bf16_f32 v59, v175, v174\n"
    "v_xor_b32 v59, v184, v59\n"
    "v_sin_f32 v177, v181\n"
    "v_fmamk_f32 v188, v185, 0x42100000, v181\n"
    "v_cos_f32 v172, v188\n"
    "v_cvt_pk_bf16_f32 v60, v176, -v177\n"
    "v_cvt_pk_bf16_f32 v64, v177, v176\n"
    "buffer_load_dwordx4 v5, s[28:31], s69 offen offset:2048 lds\n"
    "s_sub_u32 s69, s69, 294912\n"
    "v_xor_b32 v64, v184, v64\n"
    "v_sin_f32 v173, v188\n"
    "v_fmamk_f32 v187, v185, 0x42900000, v181\n"
    "v_cos_f32 v182, v187\n"
    "v_cvt_pk_bf16_f32 v61, v172, -v173\n"
    "v_cvt_pk_bf16_f32 v65, v173, v172\n"
    "v_xor_b32 v65, v184, v65\n"
    "buffer_load_dwordx4 v217, s[24:27], s69 offen offset:1024 lds\n"
    "v_sin_f32 v183, v187\n"
    "v_fmamk_f32 v180, v185, 0x42d80000, v181\n"
    "v_cos_f32 v178, v180\n"
    "v_cvt_pk_bf16_f32 v62, v182, -v183\n"
    "v_cvt_pk_bf16_f32 v66, v183, v182\n"
    "v_xor_b32 v66, v184, v66\n"
    "v_sin_f32 v179, v180\n"
    "buffer_load_dwordx4 v217, s[28:31], s69 offen offset:3072 lds\n"
    "s_add_u32 m0, s42, 81920\n"
    "s_mov_b32 s69, 4792320\n"
    "s_cmp_lt_u32 s32, 2\n"
    "s_cselect_b32 s69, s69, 4534272\n"
    "v_cvt_pk_bf16_f32 v63, v178, -v179\n"
    "v_cvt_pk_bf16_f32 v67, v179, v178\n"
    "v_xor_b32 v67, v184, v67\n"
    "v_lshrrev_b32 v188, 2, v1\n"
    "v_lshlrev_b32 v188, 7, v188\n"
    "v_and_b32 v187, 3, v1\n"
    "buffer_load_dwordx4 v5, s[24:27], s69 offen offset:0 lds\n"
    "v_lshl_add_u32 v188, v187, 4, v188\n"
    "v_and_b32 v187, 32, v1\n"
    "v_lshlrev_b32 v187, 1, v187\n"
    "v_xor_b32 v185, v187, v4\n"
    "v_and_b32 v187, 1, v3\n"
    "v_lshlrev_b32 v187, 6, v187\n"
    "v_lshl_add_u32 v187, v2, 2, v187\n"
    "v_lshl_add_u32 v187, v3, 9, v187\n"
    "v_add_u32 v68, s65, v187\n"
    "v_xor_b32 v69, 64, v68\n"
    "buffer_load_dwordx4 v5, s[28:31], s69 offen offset:2048 lds\n"
    "s_sub_u32 s69, s69, 294912\n"
    "v_add_u32 v70, 0x800, v68\n"
    "v_add_u32 v71, 0x800, v69\n"
    "v_lshlrev_b32 v72, 8, v3\n"
    "v_lshl_add_u32 v72, v2, 2, v72\n"
    "v_add_u32 v72, s65, v72\n"
    "v_add_u32 v72, 0x1000, v72\n"
    "v_lshl_add_u32 v184, v2, 4, s56\n"
    "v_lshl_add_u32 v183, v3, 12, v184\n"
    "v_lshl_add_u32 v187, v3, 10, v184\n"
    "buffer_load_dwordx4 v217, s[24:27], s69 offen offset:1024 lds\n"
    "v_add_u32 v182, s57, v4\n"
    "v_add_u32 v181, s58, v4\n"
    "v_lshl_add_u32 v180, v2, 3, s59\n"
    "buffer_load_dwordx4 v217, s[28:31], s69 offen offset:3072 lds\n"
    "s_waitcnt lgkmcnt(0)\n"
    "s_bitcmp1_b32 s48, 0\n"
    "s_cbranch_scc1 LM_aw_23\n"
    "s_call_b64 s[0:1], LGS_entry\n"
    "s_waitcnt vmcnt(0)\n"
    "s_barrier\n"
    "ds_read_b64_tr_b16 a[192:193], v6 offset:0\n"
    "ds_read_b64_tr_b16 a[194:195], v6 offset:1024\n"
    "ds_read_b64_tr_b16 a[196:197], v6 offset:2048\n"
    "ds_read_b64_tr_b16 a[198:199], v6 offset:3072\n"
    "ds_read_b64_tr_b16 a[200:201], v7 offset:0\n"
    "ds_read_b64_tr_b16 a[202:203], v7 offset:1024\n"
    "ds_read_b64_tr_b16 a[204:205], v7 offset:2048\n"
    "ds_read_b64_tr_b16 a[206:207], v7 offset:3072\n"
    "ds_read_b64_tr_b16 a[208:209], v6 offset:16384\n"
    "ds_read_b64_tr_b16 a[210:211], v6 offset:17408\n"
    "ds_read_b64_tr_b16 a[212:213], v6 offset:18432\n"
    "ds_read_b64_tr_b16 a[214:215], v6 offset:19456\n"
    "ds_read_b64_tr_b16 a[216:217], v7 offset:16384\n"
    "ds_read_b64_tr_b16 a[218:219], v7 offset:17408\n"
    "ds_read_b64_tr_b16 a[220:221], v7 offset:18432\n"
    "ds_read_b64_tr_b16 a[222:223], v7 offset:19456\n"
    "ds_read_b64_tr_b16 a[224:225], v6 offset:32768\n"
    "ds_read_b64_tr_b16 a[226:227], v6 offset:33792\n"
    "ds_read_b64_tr_b16 a[228:229], v6 offset:34816\n"
    "ds_read_b64_tr_b16 a[230:231], v6 offset:35840\n"
    "ds_read_b64_tr_b16 a[232:233], v7 offset:32768\n"
    "ds_read_b64_tr_b16 a[234:235], v7 offset:33792\n"
    "ds_read_b64_tr_b16 a[236:237], v7 offset:34816\n"
    "ds_read_b64_tr_b16 a[238:239], v7 offset:35840\n"
    "ds_read_b64_tr_b16 a[240:241], v222 offset:0\n"
    "ds_read_b64_tr_b16 a[242:243], v222 offset:1024\n"
    "ds_read_b64_tr_b16 a[244:245], v222 offset:2048\n"
    "ds_read_b64_tr_b16 a[246:247], v222 offset:3072\n"
    "ds_read_b64_tr_b16 a[248:249], v223 offset:0\n"
    "ds_read_b64_tr_b16 a[250:251], v223 offset:1024\n"
    "LS_2:\n"
    "s_cbranch_execz LS_6\n"
    "ds_read_b64_tr_b16 a[252:253], v223 offset:2048\n"
    "ds_read_b64_tr_b16 a[254:255], v223 offset:3072\n"
    "ds_read_b64_tr_b16 v[224:225], v222 offset:16384\n"
    "ds_read_b64_tr_b16 v[226:227], v222 offset:17408\n"
    "ds_read_b64_tr_b16 v[228:229], v222 offset:18432\n"
    "ds_read_b64_tr_b16 v[230:231], v222 offset:19456\n"
    "ds_read_b64_tr_b16 v[232:233], v223 offset:16384\n"
    "ds_read_b64_tr_b16 v[234:235], v223 offset:17408\n"
    "ds_read_b64_tr_b16 v[236:237], v223 offset:18432\n"
    "ds_read_b64_tr_b16 v[238:239], v223 offset:19456\n"
    "ds_read_b64_tr_b16 v[240:241], v222 offset:32768\n"
    "ds_read_b64_tr_b16 v[242:243], v222 offset:33792\n"
    "ds_read_b64_tr_b16 v[244:245], v222 offset:34816\n"
    "ds_read_b64_tr_b16 v[246:247], v222 offset:35840\n"
    "ds_read_b64_tr_b16 v[248:249], v223 offset:32768\n"
    "ds_read_b64_tr_b16 v[250:251], v223 offset:33792\n"
    "ds_read_b64_tr_b16 v[252:253], v223 offset:34816\n"
    "ds_read_b64_tr_b16 v[254:255], v223 offset:35840\n"
    "s_min_u32 s46, s46, 6\n"
    "s_waitcnt lgkmcnt(0)\n"
    "s_barrier\n"
    "s_mov_b32 s68, 2\n"
    "s_call_b64 s[0:1], LW_entry\n"
    "v_mov_b32 v76, s45\n"
    "v_mov_b32 v77, s45\n"
    "v_mov_b32 v78, s45\n"
    "v_mov_b32 v79, s45\n"
    "s_mul_i32 s66, s33, 0x4800\n"
    "s_mul_i32 s67, s32, 0x1200\n"
    "s_add_u32 s66, s66, s67\n"
    "s_add_u32 s66, s66, 0xa22000\n"
    "buffer_store_dwordx4 v[76:79], v4, s[20:23], s66 offen offset:0 sc1\n"
    "buffer_store_dwordx4 v[76:79], v4, s[20:23], s66 offen offset:1024 sc1\n"
    "buffer_store_dwordx4 v[76:79], v4, s[20:23], s66 offen offset:2048 sc1\n"
    "buffer_store_dwordx4 v[76:79], v4, s[20:23], s66 offen offset:3072 sc1\n"
    "buffer_store_dwordx4 v[76:79], v4, s[20:23], s66 offen offset:3584 sc1\n"
    "s_mov_b32 s68, 9\n"
    "s_call_b64 s[0:1], LW_entry\n"
    "LM_aw_23:\n"
    "buffer_load_dwordx2 v[178:179], v186, s[20:23], s53 offen sc1\n"
    "LM_chk_24:\n"
    "s_waitcnt vmcnt(0)\n"
    "v_xor_b32 v176, s40, v178\n"
    "v_xor_b32 v177, s41, v179\n"
    "v_or_b32 v176, v176, v177\n"
    "v_cmp_ne_u32 vcc, 0, v176\n"
    "s_nop 1\n"
    "s_cbranch_vccz LM_ok_25\n"
    "s_sub_u32 s39, s39, 1\n"
    "s_cbranch_scc1 LM_ok_25\n"
    "s_sleep 1\n"
    "s_mov_b64 exec, vcc\n"
    "buffer_load_dwordx2 v[178:179], v186, s[20:23], s53 offen sc1\n"
    "s_waitcnt vmcnt(0)\n"
    "s_branch LM_chk_24\n"
    "LM_ok_25:\n"
    "s_max_i32 s39, s39, 0\n"
    "s_mov_b64 exec, -1\n"
    "s_barrier\n"
    "s_mov_b32 m0, s65\n"
    "s_nop 0\n"
    "buffer_load_dwordx4 v185, s[20:23], s60 offen offset:0 sc1 lds\n"
    "buffer_load_dwordx4 v185, s[20:23], s60 offen offset:1024 sc1 lds\n"
    "buffer_load_dwordx4 v185, s[20:23], s60 offen offset:2048 sc1 lds\n"
    "buffer_load_dwordx4 v185, s[20:23], s60 offen offset:3072 sc1 lds\n"
    "s_add_u32 m0, s65, 0x1000\n"
    "s_nop 0\n"
    "buffer_load_dwordx4 v188, s[20:23], s63 offen sc1 lds\n"
    "s_bitcmp0_b32 s48, 0\n"
    "s_cbranch_scc1 LM_br_26\n"
    "ds_read_b64_tr_b16 a[192:193], v6 offset:0\n"
    "ds_read_b64_tr_b16 a[194:195], v6 offset:1024\n"
    "ds_read_b64_tr_b16 a[196:197], v6 offset:2048\n"
    "ds_read_b64_tr_b16 a[198:199], v6 offset:3072\n"
    "ds_read_b64_tr_b16 a[200:201], v7 offset:0\n"
    "ds_read_b64_tr_b16 a[202:203], v7 offset:1024\n"
    "ds_read_b64_tr_b16 a[204:205], v7 offset:2048\n"
    "ds_read_b64_tr_b16 a[206:207], v7 offset:3072\n"
    "ds_read_b64_tr_b16 a[208:209], v6 offset:16384\n"
    "ds_read_b64_tr_b16 a[210:211], v6 offset:17408\n"
    "ds_read_b64_tr_b16 a[212:213], v6 offset:18432\n"
    "ds_read_b64_tr_b16 a[214:215], v6 offset:19456\n"
    "ds_read_b64_tr_b16 a[216:217], v7 offset:16384\n"
    "ds_read_b64_tr_b16 a[218:219], v7 offset:17408\n"
    "ds_read_b64_tr_b16 a[220:221], v7 offset:18432\n"
    "ds_read_b64_tr_b16 a[222:223], v7 offset:19456\n"
    "ds_read_b64_tr_b16 a[224:225], v6 offset:32768\n"
    "ds_read_b64_tr_b16 a[226:227], v6 offset:33792\n"
    "ds_read_b64_tr_b16 a[228:229], v6 offset:34816\n"
    "ds_read_b64_tr_b16 a[230:231], v6 offset:35840\n"
    "ds_read_b64_tr_b16 a[232:233], v7 offset:32768\n"
    "ds_read_b64_tr_b16 a[234:235], v7 offset:33792\n"
    "ds_read_b64_tr_b16 a[236:237], v7 offset:34816\n"
    "ds_read_b64_tr_b16 a[238:239], v7 offset:35840\n"
    "ds_read_b64_tr_b16 a[240:241], v222 offset:0\n"
    "ds_read_b64_tr_b16 a[242:243], v222 offset:1024\n"
    "ds_read_b64_tr_b16 a[244:245], v222 offset:2048\n"
    "ds_read_b64_tr_b16 a[246:247], v222 offset:3072\n"
    "ds_read_b64_tr_b16 a[248:249], v223 offset:0\n"
    "ds_read_b64_tr_b16 a[250:251], v223 offset:1024\n"
    "ds_read_b64_tr_b16 a[252:253], v223 offset:2048\n"
    "ds_read_b64_tr_b16 a[254:255], v223 offset:3072\n"
    "ds_read_b64_tr_b16 v[224:225], v222 offset:16384\n"
    "ds_read_b64_tr_b16 v[226:227], v222 offset:17408\n"
    "ds_read_b64_tr_b16 v[228:229], v222 offset:18432\n"
    "ds_read_b64_tr_b16 v[230:231], v222 offset:19456\n"
    "ds_read_b64_tr_b16 v[232:233], v223 offset:16384\n"
    "ds_read_b64_tr_b16 v[234:235], v223 offset:17408\n"
    "ds_read_b64_tr_b16 v[236:237], v223 offset:18432\n"
    "ds_read_b64_tr_b16 v[238:239], v223 offset:19456\n"
    "ds_read_b64_tr_b16 v[240:241], v222 offset:32768\n"
    "ds_read_b64_tr_b16 v[242:243], v222 offset:33792\n"
    "ds_read_b64_tr_b16 v[244:245], v222 offset:34816\n"
    "ds_read_b64_tr_b16 v[246:247], v222 offset:35840\n"
    "ds_read_b64_tr_b16 v[248:249], v223 offset:32768\n"
    "ds_read_b64_tr_b16 v[250:251], v223 offset:33792\n"
    "ds_read_b64_tr_b16 v[252:253], v223 offset:34816\n"
    "ds_read_b64_tr_b16 v[254:255], v223 offset:35840\n"
    "LM_br_26:\n"
    "v_mov_b32 v186, 0x35e38e39\n"
    "v_min_u32 v184, 1, v3\n"
    "v_cvt_f32_u32 v184, v184\n"
    "v_add_f32 v184, 1.0, v184\n"
    "v_mul_f32 v184, 0x35638e39, v184\n"
    "v_sub_u32 v179, 3, v3\n"
    "v_min_u32 v179, 2, v179\n"
    "v_cvt_f32_u32 v179, v179\n"
    "v_mul_f32 v179, 0x35638e39, v179\n"
    "v_and_b32 v178, 3, v2\n"
    "v_lshrrev_b32 v177, 2, v2\n"
    "v_lshl_add_u32 v178, v178, 2, v177\n"
    "v_cvt_f32_u32 v178, v178\n"
    "v_mul_f32 v178, 0x3ce38e39, v178\n"
    "v_mul_f32 v176, 4.0, v189\n"
    "v_add_f32 v175, 0x41800000, v189\n"
    "v_add_f32 v174, 0, v178\n"
    "v_mul_f32 v173, v174, v176\n"
    "v_cos_f32 v170, v173\n"
    "v_sin_f32 v171, v173\n"
    "v_mul_f32 v170, v184, v170\n"
    "v_fmamk_f32 v172, v174, 0x3f800000, v173\n"
    "v_mul_f32 v171, v184, v171\n"
    "v_cvt_pk_bf16_f32 v76, v170, -v171\n"
    "v_cos_f32 v168, v172\n"
    "v_sin_f32 v169, v172\n"
    "v_mul_f32 v168, v186, v168\n"
    "v_fmamk_f32 v189, v174, 0x40000000, v173\n"
    "v_mul_f32 v169, v186, v169\n"
    "v_cvt_pk_bf16_f32 v77, v168, -v169\n"
    "v_cos_f32 v166, v189\n"
    "v_sin_f32 v167, v189\n"
    "v_mul_f32 v166, v186, v166\n"
    "v_fmamk_f32 v177, v174, 0x40400000, v173\n"
    "v_mul_f32 v167, v186, v167\n"
    "v_cvt_pk_bf16_f32 v78, v166, -v167\n"
    "v_cos_f32 v170, v177\n"
    "v_sin_f32 v171, v177\n"
    "v_mul_f32 v170, v186, v170\n"
    "v_mul_f32 v172, v174, v175\n"
    "v_mul_f32 v171, v186, v171\n"
    "v_cvt_pk_bf16_f32 v79, v170, -v171\n"
    "v_cos_f32 v168, v172\n"
    "v_sin_f32 v169, v172\n"
    "v_mul_f32 v168, v179, v168\n"
    "v_mov_b32 v81, 0\n"
    "v_mul_f32 v169, v179, v169\n"
    "v_cvt_pk_bf16_f32 v80, v168, -v169\n"
    "v_mov_b32 v82, 0\n"
    "v_mov_b32 v83, 0\n"
    "v_add_f32 v189, 0x3eaaaaab, v178\n"
    "v_mul_f32 v177, v189, v176\n"
    "v_cos_f32 v166, v177\n"
    "v_sin_f32 v167, v177\n"
    "v_mul_f32 v166, v184, v166\n"
    "v_fmamk_f32 v174, v189, 0x3f800000, v177\n"
    "v_mul_f32 v167, v184, v167\n"
    "v_cvt_pk_bf16_f32 v84, v166, -v167\n"
    "v_cos_f32 v172, v174\n"
    "v_sin_f32 v173, v174\n"
    "v_mul_f32 v172, v186, v172\n"
    "v_fmamk_f32 v171, v189, 0x40000000, v177\n"
    "v_mul_f32 v173, v186, v173\n"
    "v_cvt_pk_bf16_f32 v85, v172, -v173\n"
    "v_cos_f32 v168, v171\n"
    "v_sin_f32 v169, v171\n"
    "v_mul_f32 v168, v186, v168\n"
    "v_fmamk_f32 v170, v189, 0x40400000, v177\n"
    "v_mul_f32 v169, v186, v169\n"
    "v_cvt_pk_bf16_f32 v86, v168, -v169\n"
    "v_cos_f32 v166, v170\n"
    "v_sin_f32 v167, v170\n"
    "v_mul_f32 v166, v186, v166\n"
    "v_mul_f32 v174, v189, v175\n"
    "v_mul_f32 v167, v186, v167\n"
    "v_cvt_pk_bf16_f32 v87, v166, -v167\n"
    "v_cos_f32 v172, v174\n"
    "v_sin_f32 v173, v174\n"
    "v_mul_f32 v172, v179, v172\n"
    "v_mov_b32 v89, 0\n"
    "v_mul_f32 v173, v179, v173\n"
    "v_cvt_pk_bf16_f32 v88, v172, -v173\n"
    "v_mov_b32 v90, 0\n"
    "v_mov_b32 v91, 0\n"
    "v_add_f32 v177, 0x3f2aaaab, v178\n"
    "v_mul_f32 v189, v177, v176\n"
    "v_cos_f32 v170, v189\n"
    "v_sin_f32 v171, v189\n"
    "v_mul_f32 v170, v184, v170\n"
    "v_fmamk_f32 v174, v177, 0x3f800000, v189\n"
    "v_mul_f32 v171, v184, v171\n"
    "v_cvt_pk_bf16_f32 v92, v170, -v171\n"
    "v_cos_f32 v168, v174\n"
    "v_sin_f32 v169, v174\n"
    "v_mul_f32 v168, v186, v168\n"
    "v_fmamk_f32 v173, v177, 0x40000000, v189\n"
    "v_mul_f32 v169, v186, v169\n"
    "v_cvt_pk_bf16_f32 v93, v168, -v169\n"
    "v_cos_f32 v166, v173\n"
    "v_sin_f32 v167, v173\n"
    "v_mul_f32 v166, v186, v166\n"
    "v_fmamk_f32 v178, v177, 0x40400000, v189\n"
    "v_mul_f32 v167, v186, v167\n"
    "v_cvt_pk_bf16_f32 v94, v166, -v167\n"
    "v_cos_f32 v170, v178\n"
    "v_sin_f32 v171, v178\n"
    "v_mul_f32 v170, v186, v170\n"
    "v_mul_f32 v184, v177, v175\n"
    "v_mul_f32 v171, v186, v171\n"
    "v_cvt_pk_bf16_f32 v95, v170, -v171\n"
    "v_cos_f32 v168, v184\n"
    "v_sin_f32 v169, v184\n"
    "v_mul_f32 v168, v179, v168\n"
    "v_mov_b32 v97, 0\n"
    "v_mul_f32 v169, v179, v169\n"
    "v_cvt_pk_bf16_f32 v96, v168, -v169\n"
    "v_mov_b32 v98, 0\n"
    "v_mov_b32 v99, 0\n"
    "v_mov_b32 v101, 0\n"
    "v_mov_b32 v102, 0\n"
    "v_mov_b32 v103, 0\n"
    "v_mov_b32 v105, 0\n"
    "v_mov_b32 v106, 0\n"
    "v_mov_b32 v107, 0\n"
    "v_mov_b32 v109, 0\n"
    "v_mov_b32 v110, 0\n"
    "v_mov_b32 v111, 0\n"
    "v_mov_b32 v113, 0\n"
    "v_mov_b32 v114, 0\n"
    "v_mov_b32 v115, 0\n"
    "v_lshlrev_b32 v116, 16, v8\n"
    "v_lshlrev_b32 v117, 16, v10\n"
    "v_lshlrev_b32 v118, 16, v12\n"
    "v_mov_b32 v119, 0\n"
    "v_and_b32 v120, 0xffff0000, v8\n"
    "v_and_b32 v121, 0xffff0000, v10\n"
    "v_and_b32 v122, 0xffff0000, v12\n"
    "v_mov_b32 v123, 0\n"
    "v_lshlrev_b32 v124, 16, v9\n"
    "v_lshlrev_b32 v125, 16, v11\n"
    "v_lshlrev_b32 v126, 16, v13\n"
    "v_mov_b32 v127, 0\n"
    "v_and_b32 v128, 0xffff0000, v9\n"
    "v_and_b32 v129, 0xffff0000, v11\n"
    "v_and_b32 v130, 0xffff0000, v13\n"
    "v_mov_b32 v131, 0\n"
    "v_lshlrev_b32 v132, 16, v14\n"
    "v_lshlrev_b32 v133, 16, v16\n"
    "v_lshlrev_b32 v134, 16, v18\n"
    "v_mov_b32 v135, 0\n"
    "v_and_b32 v136, 0xffff0000, v14\n"
    "v_and_b32 v137, 0xffff0000, v16\n"
    "v_and_b32 v138, 0xffff0000, v18\n"
    "v_mov_b32 v139, 0\n"
    "v_lshlrev_b32 v140, 16, v15\n"
    "v_lshlrev_b32 v141, 16, v17\n"
    "v_lshlrev_b32 v142, 16, v19\n"
    "v_mov_b32 v143, 0\n"
    "v_and_b32 v144, 0xffff0000, v15\n"
    "v_and_b32 v145, 0xffff0000, v17\n"
    "v_and_b32 v146, 0xffff0000, v19\n"
    "v_mov_b32 v147, 0\n"
    "v_lshlrev_b32 v148, 16, v20\n"
    "v_lshlrev_b32 v149, 16, v22\n"
    "v_lshlrev_b32 v150, 16, v24\n"
    "v_mov_b32 v151, 0\n"
    "v_and_b32 v152, 0xffff0000, v20\n"
    "v_and_b32 v153, 0xffff0000, v22\n"
    "v_and_b32 v154, 0xffff0000, v24\n"
    "v_mov_b32 v155, 0\n"
    "v_lshlrev_b32 v156, 16, v21\n"
    "v_lshlrev_b32 v157, 16, v23\n"
    "v_lshlrev_b32 v158, 16, v25\n"
    "v_mov_b32 v159, 0\n"
    "v_and_b32 v160, 0xffff0000, v21\n"
    "v_and_b32 v161, 0xffff0000, v23\n"
    "v_and_b32 v162, 0xffff0000, v25\n"
    "v_mov_b32 v163, 0\n"
    "s_waitcnt vmcnt(0)\n"
    "LM_z2_27:\n"
    "ds_read2_b32 v[164:165], v68 offset0:0 offset1:32\n"
    "ds_read2_b32 v[166:167], v68 offset0:64 offset1:96\n"
    "ds_read2_b32 v[168:169], v69 offset0:0 offset1:32\n"
    "ds_read2_b32 v[170:171], v69 offset0:64 offset1:96\n"
    "ds_read2_b32 v[172:173], v70 offset0:0 offset1:32\n"
    "ds_read2_b32 v[174:175], v70 offset0:64 offset1:96\n"
    "ds_read2_b32 v[176:177], v71 offset0:0 offset1:32\n"
    "ds_read2_b32 v[178:179], v71 offset0:64 offset1:96\n"
    "ds_read2_b32 v[8:9], v72 offset0:0 offset1:16\n"
    "ds_read2_b32 v[10:11], v72 offset0:32 offset1:48\n"
    "s_waitcnt lgkmcnt(8)\n"
    "v_mfma_f32_16x16x32_bf16 v[12:15], v[28:31], v[164:167], 0\n"
    "v_max3_u32 v213, v164, v165, v166\n"
    "s_waitcnt lgkmcnt(7)\n"
    "v_max3_u32 v213, v213, v167, v168\n"
    "v_mfma_f32_16x16x32_bf16 v[16:19], v[32:35], v[164:167], 0\n"
    "s_waitcnt lgkmcnt(6)\n"
    "v_max3_u32 v213, v213, v169, v170\n"
    "v_mfma_f32_16x16x32_bf16 v[20:23], v[36:39], v[168:171], 0\n"
    "s_waitcnt lgkmcnt(5)\n"
    "v_max3_u32 v213, v213, v171, v172\n"
    "v_mfma_f32_16x16x32_bf16 v[24:27], v[40:43], v[168:171], 0\n"
    "s_waitcnt lgkmcnt(4)\n"
    "v_max3_u32 v213, v213, v173, v174\n"
    "v_mfma_f32_16x16x32_bf16 v[192:195], v[44:47], v[172:175], 0\n"
    "v_cvt_pk_bf16_f32 v196, v12, v16\n"
    "s_waitcnt lgkmcnt(3)\n"
    "v_max3_u32 v213, v213, v175, v176\n"
    "v_mfma_f32_16x16x32_bf16 v[200:203], v[48:51], v[172:175], 0\n"
    "v_cvt_pk_bf16_f32 v204, v13, v17\n"
    "s_waitcnt lgkmcnt(2)\n"
    "v_max3_u32 v213, v213, v177, v178\n"
    "v_mfma_f32_16x16x32_bf16 v[164:167], v[52:55], v[176:179], 0\n"
    "v_cvt_pk_bf16_f32 v208, v14, v18\n"
    "s_waitcnt lgkmcnt(1)\n"
    "v_max3_u32 v213, v213, v179, v8\n"
    "v_mfma_f32_16x16x32_bf16 v[168:171], v[56:59], v[176:179], 0\n"
    "v_cvt_pk_bf16_f32 v172, v15, v19\n"
    "s_waitcnt lgkmcnt(0)\n"
    "v_max3_u32 v213, v213, v9, v10\n"
    "v_mfma_f32_16x16x32_bf16 v[12:15], v[60:63], v[8:11], 0\n"
    "v_max_u32 v213, v213, v11\n"
    "v_cmp_eq_u32 vcc, s45, v213\n"
    "v_mfma_f32_16x16x32_bf16 v[16:19], v[64:67], v[8:11], 0\n"
    "v_cvt_pk_bf16_f32 v197, v20, v24\n"
    "v_cvt_pk_bf16_f32 v205, v21, v25\n"
    "v_cvt_pk_bf16_f32 v209, v22, v26\n"
    "v_cvt_pk_bf16_f32 v173, v23, v27\n"
    "v_cvt_pk_bf16_f32 v198, v192, v200\n"
    "v_cvt_pk_bf16_f32 v206, v193, v201\n"
    "v_cvt_pk_bf16_f32 v210, v194, v202\n"
    "v_cvt_pk_bf16_f32 v174, v195, v203\n"
    "v_cvt_pk_bf16_f32 v199, v164, v168\n"
    "ds_write_b128 v183, v[196:199] offset:0\n"
    "v_cvt_pk_bf16_f32 v207, v165, v169\n"
    "v_cvt_pk_bf16_f32 v211, v166, v170\n"
    "v_cvt_pk_bf16_f32 v175, v167, v171\n"
    "v_cvt_pk_bf16_f32 v8, v12, v16\n"
    "v_cvt_pk_bf16_f32 v9, v13, v17\n"
    "v_cvt_pk_bf16_f32 v10, v14, v18\n"
    "v_cvt_pk_bf16_f32 v11, v15, v19\n"
    "ds_write_b128 v183, v[204:207] offset:1024\n"
    "ds_write_b128 v183, v[208:211] offset:2048\n"
    "ds_write_b128 v183, v[172:175] offset:3072\n"
    "ds_write_b128 v187, v[8:11] offset:16384\n"
    "s_cbranch_vccz LM_z2ok_28\n"
    "s_mov_b32 m0, s65\n"
    "s_nop 0\n"
    "buffer_load_dwordx4 v185, s[20:23], s60 offen offset:0 sc1 lds\n"
    "buffer_load_dwordx4 v185, s[20:23], s60 offen offset:1024 sc1 lds\n"
    "buffer_load_dwordx4 v185, s[20:23], s60 offen offset:2048 sc1 lds\n"
    "buffer_load_dwordx4 v185, s[20:23], s60 offen offset:3072 sc1 lds\n"
    "s_add_u32 m0, s65, 0x1000\n"
    "s_nop 0\n"
    "buffer_load_dwordx4 v188, s[20:23], s63 offen sc1 lds\n"
    "s_waitcnt vmcnt(0)\n"
    "s_sub_u32 s39, s39, 1\n"
    "s_cbranch_scc0 LM_z2_27\n"
    "s_mov_b32 s39, 0\n"
    "LM_z2ok_28:\n"
    "s_waitcnt lgkmcnt(0)\n"
    "s_barrier\n"
    "s_lshl_b32 s50, s33, 2\n"
    "s_add_u32 s50, s50, s32\n"
    "s_mul_i32 s50, s50, 0x1500\n"
    "s_add_u32 s50, s50, 0x28007c\n"
    "ds_read_b128 v[8:11], v182 offset:0\n"
    "v_min_u32 v214, 20, v1\n"
    "v_lshl_add_u32 v214, v214, 8, s50\n"
    "v_min_u32 v214, 0x50fff8, v214\n"
    "ds_read_b32 v100, v181 offset:16384\n"
    "global_load_dwordx2 v[190:191], v214, s[14:15]\n"
    "ds_read_b128 v[12:15], v182 offset:1024\n"
    "v_mov_b32 v189, 0x7ffffff0\n"
    "v_cmp_gt_u32 vcc, 32, v1\n"
    "v_add_u32 v16, 0, v4\n"
    "ds_read_b32 v104, v181 offset:16388\n"
    "v_cndmask_b32 v189, v189, v4, vcc\n"
    "ds_read_b128 v[20:23], v182 offset:2048\n"
    "v_add_u32 v17, 0x1000, v4\n"
    "v_add_u32 v18, 0x2000, v4\n"
    "v_add_u32 v19, 0x3000, v4\n"
    "ds_read_b32 v108, v181 offset:16392\n"
    "v_lshlrev_b32 v188, 3, v2\n"
    "ds_read_b128 v[24:27], v182 offset:3072\n"
    "v_mov_b32 v187, 0x3727c5ac\n"
    "ds_read_b32 v112, v181 offset:16396\n"
    "s_waitcnt lgkmcnt(7)\n"
    "v_mfma_f32_16x16x32_bf16 v[116:119], v[76:79], v[8:11], v[116:119]\n"
    "s_waitcnt lgkmcnt(6)\n"
    "v_mfma_f32_16x16x32_bf16 v[116:119], v[80:83], v[100:103], v[116:119]\n"
    "v_mfma_f32_16x16x32_bf16 v[132:135], v[84:87], v[8:11], v[132:135]\n"
    "v_mfma_f32_16x16x32_bf16 v[132:135], v[88:91], v[100:103], v[132:135]\n"
    "s_nop 1\n"
    "v_add_f32 v28, v116, v117\n"
    "v_mfma_f32_16x16x32_bf16 v[148:151], v[92:95], v[8:11], v[148:151]\n"
    "v_add_f32 v28, v28, v118\n"
    "v_mfma_f32_16x16x32_bf16 v[148:151], v[96:99], v[100:103], v[148:151]\n"
    "s_nop 1\n"
    "v_add_f32 v28, v28, v132\n"
    "s_waitcnt lgkmcnt(5)\n"
    "v_mfma_f32_16x16x32_bf16 v[120:123], v[76:79], v[12:15], v[120:123]\n"
    "v_add_f32 v28, v28, v133\n"
    "v_add_f32 v28, v28, v134\n"
    "s_waitcnt lgkmcnt(4)\n"
    "v_mfma_f32_16x16x32_bf16 v[120:123], v[80:83], v[104:107], v[120:123]\n"
    "v_add_f32 v28, v28, v148\n"
    "v_mfma_f32_16x16x32_bf16 v[136:139], v[84:87], v[12:15], v[136:139]\n"
    "v_add_f32 v28, v28, v149\n"
    "v_add_f32 v28, v28, v150\n"
    "v_mfma_f32_16x16x32_bf16 v[136:139], v[88:91], v[104:107], v[136:139]\n"
    "s_nop 1\n"
    "v_add_f32 v29, v120, v121\n"
    "v_mfma_f32_16x16x32_bf16 v[152:155], v[92:95], v[12:15], v[152:155]\n"
    "v_add_f32 v29, v29, v122\n"
    "v_cvt_pk_bf16_f32 v192, v116, v120\n"
    "v_mfma_f32_16x16x32_bf16 v[152:155], v[96:99], v[104:107], v[152:155]\n"
    "v_cvt_pk_bf16_f32 v194, v117, v121\n"
    "v_add_f32 v29, v29, v136\n"
    "s_waitcnt lgkmcnt(3)\n"
    "v_mfma_f32_16x16x32_bf16 v[124:127], v[76:79], v[20:23], v[124:127]\n"
    "v_add_f32 v29, v29, v137\n"
    "v_add_f32 v29, v29, v138\n"
    "s_waitcnt lgkmcnt(2)\n"
    "v_mfma_f32_16x16x32_bf16 v[124:127], v[80:83], v[108:111], v[124:127]\n"
    "v_cvt_pk_bf16_f32 v196, v118, v122\n"
    "v_add_f32 v29, v29, v152\n"
    "v_mfma_f32_16x16x32_bf16 v[140:143], v[84:87], v[20:23], v[140:143]\n"
    "v_add_f32 v29, v29, v153\n"
    "v_add_f32 v29, v29, v154\n"
    "v_mfma_f32_16x16x32_bf16 v[140:143], v[88:91], v[108:111], v[140:143]\n"
    "v_add_f32 v28, v28, v29\n"
    "s_nop 0\n"
    "v_add_f32 v30, v124, v125\n"
    "v_mfma_f32_16x16x32_bf16 v[156:159], v[92:95], v[20:23], v[156:159]\n"
    "v_add_f32 v30, v30, v126\n"
    "v_cvt_pk_bf16_f32 v198, v132, v136\n"
    "v_mfma_f32_16x16x32_bf16 v[156:159], v[96:99], v[108:111], v[156:159]\n"
    "v_cvt_pk_bf16_f32 v200, v133, v137\n"
    "v_add_f32 v30, v30, v140\n"
    "s_waitcnt lgkmcnt(1)\n"
    "v_mfma_f32_16x16x32_bf16 v[128:131], v[76:79], v[24:27], v[128:131]\n"
    "v_add_f32 v30, v30, v141\n"
    "v_add_f32 v30, v30, v142\n"
    "s_waitcnt lgkmcnt(0)\n"
    "v_mfma_f32_16x16x32_bf16 v[128:131], v[80:83], v[112:115], v[128:131]\n"
    "v_cvt_pk_bf16_f32 v202, v134, v138\n"
    "v_add_f32 v30, v30, v156\n"
    "v_mfma_f32_16x16x32_bf16 v[144:147], v[84:87], v[24:27], v[144:147]\n"
    "v_add_f32 v30, v30, v157\n"
    "v_add_f32 v30, v30, v158\n"
    "v_mfma_f32_16x16x32_bf16 v[144:147], v[88:91], v[112:115], v[144:147]\n"
    "v_cvt_pk_bf16_f32 v204, v148, v152\n"
    "s_nop 0\n"
    "v_add_f32 v31, v128, v129\n"
    "v_mfma_f32_16x16x32_bf16 v[160:163], v[92:95], v[24:27], v[160:163]\n"
    "v_add_f32 v31, v31, v130\n"
    "v_cvt_pk_bf16_f32 v193, v124, v128\n"
    "v_mfma_f32_16x16x32_bf16 v[160:163], v[96:99], v[112:115], v[160:163]\n"
    "v_cvt_pk_bf16_f32 v195, v125, v129\n"
    "v_add_f32 v31, v31, v144\n"
    "v_add_f32 v31, v31, v145\n"
    "v_add_f32 v31, v31, v146\n"
    "v_cvt_pk_bf16_f32 v197, v126, v130\n"
    "v_cvt_pk_bf16_f32 v199, v140, v144\n"
    "v_cvt_pk_bf16_f32 v201, v141, v145\n"
    "v_cvt_pk_bf16_f32 v203, v142, v146\n"
    "v_cvt_pk_bf16_f32 v206, v149, v153\n"
    "v_add_f32 v31, v31, v160\n"
    "v_add_f32 v31, v31, v161\n"
    "v_add_f32 v31, v31, v162\n"
    "v_add_f32 v30, v30, v31\n"
    "v_add_f32 v186, v28, v30\n"
    "v_mov_b32 v185, v186\n"
    "v_cvt_pk_bf16_f32 v205, v156, v160\n"
    "v_cvt_pk_bf16_f32 v207, v157, v161\n"
    "v_permlane16_swap_b32 v186, v185\n"
    "v_add_f32 v186, v186, v185\n"
    "v_mov_b32 v185, v186\n"
    "v_cvt_pk_bf16_f32 v208, v150, v154\n"
    "v_cvt_pk_bf16_f32 v209, v158, v162\n"
    "v_permlane32_swap_b32 v186, v185\n"
    "v_add_f32 v186, v186, v185\n"
    "v_mul_f32 v182, 0x3be38e39, v186\n"
    "buffer_store_dwordx4 v[218:221], v215, s[20:23], s52 offen sc1\n"
    "v_mov_b32 v210, v208\n"
    "v_mov_b32 v211, v209\n"
    "v_sub_f32 v116, v116, v182\n"
    "v_permlane32_swap_b32 v208, v210\n"
    "v_permlane32_swap_b32 v209, v211\n"
    "v_mul_f32 v8, v116, v116\n"
    "v_sub_f32 v117, v117, v182\n"
    "v_fmac_f32 v8, v117, v117\n"
    "v_sub_f32 v118, v118, v182\n"
    "v_fmac_f32 v8, v118, v118\n"
    "v_sub_f32 v132, v132, v182\n"
    "v_fmac_f32 v8, v132, v132\n"
    "v_sub_f32 v133, v133, v182\n"
    "v_fmac_f32 v8, v133, v133\n"
    "v_sub_f32 v134, v134, v182\n"
    "v_fmac_f32 v8, v134, v134\n"
    "v_sub_f32 v148, v148, v182\n"
    "v_fmac_f32 v8, v148, v148\n"
    "v_sub_f32 v149, v149, v182\n"
    "v_fmac_f32 v8, v149, v149\n"
    "v_sub_f32 v150, v150, v182\n"
    "v_fmac_f32 v8, v150, v150\n"
    "v_sub_f32 v120, v120, v182\n"
    "v_mul_f32 v9, v120, v120\n"
    "v_sub_f32 v121, v121, v182\n"
    "v_fmac_f32 v9, v121, v121\n"
    "v_sub_f32 v122, v122, v182\n"
    "v_fmac_f32 v9, v122, v122\n"
    "v_sub_f32 v136, v136, v182\n"
    "v_fmac_f32 v9, v136, v136\n"
    "v_sub_f32 v137, v137, v182\n"
    "v_fmac_f32 v9, v137, v137\n"
    "v_sub_f32 v138, v138, v182\n"
    "v_fmac_f32 v9, v138, v138\n"
    "v_sub_f32 v152, v152, v182\n"
    "v_fmac_f32 v9, v152, v152\n"
    "v_sub_f32 v153, v153, v182\n"
    "v_fmac_f32 v9, v153, v153\n"
    "v_sub_f32 v154, v154, v182\n"
    "v_fmac_f32 v9, v154, v154\n"
    "v_sub_f32 v124, v124, v182\n"
    "v_mul_f32 v10, v124, v124\n"
    "v_sub_f32 v125, v125, v182\n"
    "v_fmac_f32 v10, v125, v125\n"
    "v_sub_f32 v126, v126, v182\n"
    "v_fmac_f32 v10, v126, v126\n"
    "v_sub_f32 v140, v140, v182\n"
    "v_fmac_f32 v10, v140, v140\n"
    "v_sub_f32 v141, v141, v182\n"
    "v_fmac_f32 v10, v141, v141\n"
    "v_sub_f32 v142, v142, v182\n"
    "v_fmac_f32 v10, v142, v142\n"
    "v_sub_f32 v156, v156, v182\n"
    "v_fmac_f32 v10, v156, v156\n"
    "v_sub_f32 v157, v157, v182\n"
    "v_fmac_f32 v10, v157, v157\n"
    "v_sub_f32 v158, v158, v182\n"
    "v_fmac_f32 v10, v158, v158\n"
    "v_sub_f32 v128, v128, v182\n"
    "v_mul_f32 v11, v128, v128\n"
    "v_sub_f32 v129, v129, v182\n"
    "v_fmac_f32 v11, v129, v129\n"
    "v_sub_f32 v130, v130, v182\n"
    "v_fmac_f32 v11, v130, v130\n"
    "v_sub_f32 v144, v144, v182\n"
    "v_fmac_f32 v11, v144, v144\n"
    "v_sub_f32 v145, v145, v182\n"
    "v_fmac_f32 v11, v145, v145\n"
    "v_sub_f32 v146, v146, v182\n"
    "v_fmac_f32 v11, v146, v146\n"
    "v_sub_f32 v160, v160, v182\n"
    "v_fmac_f32 v11, v160, v160\n"
    "v_sub_f32 v161, v161, v182\n"
    "v_fmac_f32 v11, v161, v161\n"
    "v_sub_f32 v162, v162, v182\n"
    "v_fmac_f32 v11, v162, v162\n"
    "v_add_f32 v8, v8, v9\n"
    "v_add_f32 v10, v10, v11\n"
    "v_add_f32 v186, v8, v10\n"
    "v_mov_b32 v185, v186\n"
    "s_nop 1\n"
    "v_permlane16_swap_b32 v186, v185\n"
    "v_add_f32 v186, v186, v185\n"
    "v_mov_b32 v185, v186\n"
    "s_nop 1\n"
    "v_permlane32_swap_b32 v186, v185\n"
    "v_add_f32 v186, v186, v185\n"
    "v_mov_b32 v183, v186\n"
    "ds_write_b64 v180, v[182:183] offset:24576\n"
    "s_waitcnt lgkmcnt(0)\n"
    "s_barrier\n"
    "ds_read_b64 v[178:179], v188 offset:24576\n"
    "ds_read_b64 v[176:177], v188 offset:24704\n"
    "ds_read_b64 v[174:175], v188 offset:24832\n"
    "ds_read_b64 v[172:173], v188 offset:24960\n"
    "s_waitcnt lgkmcnt(2)\n"
    "v_add_f32 v184, v178, v176\n"
    "v_add_f32 v181, v179, v177\n"
    "s_waitcnt lgkmcnt(0)\n"
    "v_add_f32 v171, v174, v172\n"
    "v_add_f32 v184, v184, v171\n"
    "v_mul_f32 v184, 0x3e800000, v184\n"
    "v_add_f32 v186, v175, v173\n"
    "v_add_f32 v181, v181, v186\n"
    "v_sub_f32 v8, v178, v184\n"
    "v_sub_f32 v9, v176, v184\n"
    "v_sub_f32 v10, v174, v184\n"
    "v_sub_f32 v11, v172, v184\n"
    "v_mul_f32 v188, v8, v8\n"
    "v_fmac_f32 v188, v9, v9\n"
    "v_fmac_f32 v188, v10, v10\n"
    "v_fmac_f32 v188, v11, v11\n"
    "v_fmamk_f32 v181, v188, 0x43100000, v181\n"
    "v_fmamk_f32 v181, v181, 0x3ae38e39, v187\n"
    "v_rsq_f32 v170, v181\n"
    "v_sub_f32 v186, v182, v184\n"
    "v_mul_f32 v170, 0x3eeff558, v170\n"
    "v_mul_f32 v186, v186, v170\n"
    "v_fma_f32 v150, v150, v170, v186\n"
    "v_fma_f32 v154, v154, v170, v186\n"
    "v_fma_f32 v158, v158, v170, v186\n"
    "v_fma_f32 v162, v162, v170, v186\n"
    "v_cvt_pk_bf16_f32 v12, v150, v154\n"
    "v_cvt_pk_bf16_f32 v13, v158, v162\n"
    "v_mov_b32 v14, v12\n"
    "v_mov_b32 v15, v13\n"
    "v_fma_f32 v116, v116, v170, v186\n"
    "v_permlane32_swap_b32 v12, v14\n"
    "v_permlane32_swap_b32 v13, v15\n"
    "buffer_store_dwordx4 v[12:15], v189, s[20:23], s61 offen sc1\n"
    "v_fma_f32 v120, v120, v170, v186\n"
    "v_fma_f32 v124, v124, v170, v186\n"
    "v_fma_f32 v128, v128, v170, v186\n"
    "v_cvt_pk_bf16_f32 v8, v116, v120\n"
    "v_cvt_pk_bf16_f32 v9, v124, v128\n"
    "v_fma_f32 v117, v117, v170, v186\n"
    "v_fma_f32 v121, v121, v170, v186\n"
    "v_fma_f32 v125, v125, v170, v186\n"
    "v_fma_f32 v129, v129, v170, v186\n"
    "v_cvt_pk_bf16_f32 v10, v117, v121\n"
    "v_cvt_pk_bf16_f32 v11, v125, v129\n"
    "buffer_store_dwordx4 v[8:11], v16, s[20:23], s64 offen sc1\n"
    "v_fma_f32 v118, v118, v170, v186\n"
    "v_fma_f32 v122, v122, v170, v186\n"
    "v_fma_f32 v126, v126, v170, v186\n"
    "v_fma_f32 v130, v130, v170, v186\n"
    "v_cvt_pk_bf16_f32 v20, v118, v122\n"
    "v_cvt_pk_bf16_f32 v21, v126, v130\n"
    "v_fma_f32 v132, v132, v170, v186\n"
    "v_fma_f32 v136, v136, v170, v186\n"
    "v_fma_f32 v140, v140, v170, v186\n"
    "v_fma_f32 v144, v144, v170, v186\n"
    "v_cvt_pk_bf16_f32 v22, v132, v136\n"
    "v_cvt_pk_bf16_f32 v23, v140, v144\n"
    "buffer_store_dwordx4 v[20:23], v17, s[20:23], s64 offen sc1\n"
    "v_fma_f32 v133, v133, v170, v186\n"
    "v_fma_f32 v137, v137, v170, v186\n"
    "v_fma_f32 v141, v141, v170, v186\n"
    "v_fma_f32 v145, v145, v170, v186\n"
    "v_cvt_pk_bf16_f32 v24, v133, v137\n"
    "v_cvt_pk_bf16_f32 v25, v141, v145\n"
    "v_fma_f32 v134, v134, v170, v186\n"
    "v_fma_f32 v138, v138, v170, v186\n"
    "v_fma_f32 v142, v142, v170, v186\n"
    "v_fma_f32 v146, v146, v170, v186\n"
    "v_cvt_pk_bf16_f32 v26, v134, v138\n"
    "v_cvt_pk_bf16_f32 v27, v142, v146\n"
    "buffer_store_dwordx4 v[24:27], v18, s[20:23], s64 offen sc1\n"
    "v_fma_f32 v148, v148, v170, v186\n"
    "v_fma_f32 v152, v152, v170, v186\n"
    "v_fma_f32 v156, v156, v170, v186\n"
    "v_fma_f32 v160, v160, v170, v186\n"
    "v_cvt_pk_bf16_f32 v28, v148, v152\n"
    "v_cvt_pk_bf16_f32 v29, v156, v160\n"
    "v_fma_f32 v149, v149, v170, v186\n"
    "v_fma_f32 v153, v153, v170, v186\n"
    "v_fma_f32 v157, v157, v170, v186\n"
    "v_fma_f32 v161, v161, v170, v186\n"
    "v_cvt_pk_bf16_f32 v30, v149, v153\n"
    "v_cvt_pk_bf16_f32 v31, v157, v161\n"
    "buffer_store_dwordx4 v[28:31], v19, s[20:23], s64 offen sc1\n"
    "s_bitcmp0_b32 s38, 17\n"
    "s_cbranch_scc1 LM_ot_29\n"
    "s_lshl_b32 s50, s32, 10\n"
    "s_add_u32 s50, s50, 0xd800\n"
    "v_add_u32 v188, s50, v4\n"
    "s_lshl_b32 s50, s32, 9\n"
    "s_add_u32 s50, s50, 0x11800\n"
    "v_and_b32 v189, 0x1ff, v4\n"
    "v_add_u32 v189, s50, v189\n"
    "ds_write_b128 v188, v[8:11] offset:0\n"
    "ds_write_b128 v188, v[20:23] offset:4096\n"
    "ds_write_b128 v188, v[24:27] offset:8192\n"
    "ds_write_b128 v188, v[28:31] offset:12288\n"
    "ds_write_b128 v189, v[12:15]\n"
    "LM_ot_29:\n"
    "LS_3:\n"
    "s_cbranch_execz LS_7\n"
    "s_add_u32 s56, s64, 0x240000\n"
    "s_add_u32 s57, s61, 0x240000\n"
    "s_mov_b32 s54, s22\n"
    "LM_J_6:\n"
    "s_mov_b64 s[52:53], s[20:21]\n"
    "s_mov_b32 s55, s23\n"
    "s_lshl_b32 s77, s48, 10\n"
    "v_lshl_add_u32 v123, v0, 2, s77\n"
    "s_getpc_b64 s[50:51]\n"
    "LCP1:\n"
    "s_add_u32 s50, s50, LG1_e0-LCP1\n"
    "s_addc_u32 s51, s51, 0\n"
    "global_load_dword v124, v123, s[50:51]\n"
    "v_add_u32 v120, 0x9000, v4\n"
    "s_bitcmp1_b32 s38, 17\n"
    "s_cbranch_scc1 LG1_e0\n"
    "v_and_b32 v160, 1, v1\n"
    "v_mad_u32_u24 v160, v160, 7, s19\n"
    "v_and_b32 v160, 0x7f, v160\n"
    "v_lshl_add_u32 v212, v160, 7, s49\n"
    "s_lshl_b32 s9, s33, 7\n"
    "s_add_u32 s9, s9, 0x1a000\n"
    "s_mul_i32 s58, s32, 0x1200\n"
    "v_add_u32 v213, s58, v4\n"
    "s_add_u32 s59, s19, 0\n"
    "s_and_b32 s59, s59, 0x7f\n"
    "s_mul_i32 s59, s59, 0x4800\n"
    "s_add_u32 s59, s59, s58\n"
    "s_add_u32 s59, s59, 0xa22000\n"
    "s_add_u32 s60, s19, 7\n"
    "s_and_b32 s60, s60, 0x7f\n"
    "s_mul_i32 s60, s60, 0x4800\n"
    "s_add_u32 s60, s60, s58\n"
    "s_add_u32 s60, s60, 0xa22000\n"
    "s_mov_b64 s[28:29], s[14:15]\n"
    "s_mov_b32 s30, 0x510000\n"
    "s_mov_b64 s[24:25], s[20:21]\n"
    "v_mov_b32 v116, s45\n"
    "v_mov_b32 v117, s45\n"
    "v_mov_b32 v118, s45\n"
    "v_mov_b32 v119, s45\n"
    "v_lshrrev_b32 v160, 3, v1\n"
    "v_mul_u32_u24 v160, 0x480, v160\n"
    "v_and_b32 v161, 7, v1\n"
    "v_lshl_add_u32 v111, v161, 4, v160\n"
    "v_bfe_u32 v161, v1, 2, 2\n"
    "v_and_b32 v162, 3, v1\n"
    "v_lshl_add_u32 v161, v3, 2, v161\n"
    "v_lshl_add_u32 v161, v161, 7, s84\n"
    "s_bitcmp0_b32 s18, 0\n"
    "s_cselect_b32 s77, 4, 3\n"
    "s_cselect_b32 s78, 8, 32\n"
    "v_lshlrev_b32 v160, s77, v162\n"
    "v_add_u32 v114, v160, v161\n"
    "v_add_u32 v126, s78, v114\n"
    "s_bitcmp0_b32 s18, 1\n"
    "s_cselect_b32 s77, 4, 3\n"
    "s_cselect_b32 s78, 8, 32\n"
    "v_lshlrev_b32 v160, s77, v162\n"
    "v_add_u32 v115, v160, v161\n"
    "v_add_u32 v127, s78, v115\n"
    "v_lshrrev_b32 v163, 1, v1\n"
    "v_min_u32 v163, 8, v163\n"
    "v_add_u32 v163, s8, v163\n"
    "v_subrev_u32 v164, 9, v163\n"
    "v_min_u32 v163, v163, v164\n"
    "s_lshl_b32 s78, s10, 1\n"
    "s_sub_u32 s78, s7, s78\n"
    "s_add_u32 s78, s78, 16\n"
    "v_and_b32 v160, 1, v1\n"
    "v_add_u32 v160, s78, v160\n"
    "v_mad_u32_u24 v160, v163, 14, v160\n"
    "v_and_b32 v160, 0x7f, v160\n"
    "v_mul_u32_u24 v164, 0x2493, v160\n"
    "v_lshrrev_b32 v164, 16, v164\n"
    "v_mul_u32_u24 v164, 7, v164\n"
    "v_sub_u32 v160, v160, v164\n"
    "s_mul_i32 s78, s10, 9\n"
    "v_add_u32 v163, s78, v163\n"
    "v_mad_u32_u24 v160, v163, 7, v160\n"
    "v_and_b32 v163, 3, v1\n"
    "s_and_b32 s78, s2, 0xf9\n"
    "s_add_u32 s78, s78, 0x100\n"
    "v_lshl_add_u32 v163, v163, 1, s78\n"
    "v_cmp_gt_u32 vcc, 18, v1\n"
    "s_nop 1\n"
    "v_cndmask_b32 v160, v163, v160, vcc\n"
    "s_add_u32 s78, s7, 0x300\n"
    "v_and_b32 v163, 1, v1\n"
    "v_add_u32 v163, s78, v163\n"
    "v_cmp_gt_u32 vcc, 32, v1\n"
    "v_mov_b32 v125, 0x7ffffff0\n"
    "s_add_u32 s15, s13, 0x1000\n"
    "v_cndmask_b32 v160, v163, v160, vcc\n"
    "v_cndmask_b32 v125, v125, v4, vcc\n"
    "v_lshl_add_u32 v110, v160, 7, s49\n"
    "s_mov_b64 s[4:5], s[20:21]\n"
    "s_mov_b32 s7, s23\n"
    "s_lshl_b32 s77, s32, 6\n"
    "s_lshl_b32 s78, s10, 1\n"
    "s_add_u32 s77, s77, s78\n"
    "v_and_b32 v160, 1, v1\n"
    "v_add_u32 v160, s77, v160\n"
    "v_lshrrev_b32 v163, 1, v2\n"
    "v_lshl_add_u32 v160, v163, 3, v160\n"
    "v_lshl_add_u32 v109, v160, 7, s49\n"
    "v_mov_b32 v112, 0x409d30fc\n"
    "v_add_u32 v121, 0x10000, v4\n"
    "v_add_u32 v122, 0x20000, v4\n"
    "v_mad_u32_u24 v160, v1, 14, s75\n"
    "v_and_b32 v160, 0x7f, v160\n"
    "v_mul_u32_u24 v160, 0x4800, v160\n"
    "v_add_u32 v160, s73, v160\n"
    "v_cmp_gt_u32 vcc, 16, v1\n"
    "s_add_u32 s77, s19, 9\n"
    "v_mad_u32_u24 v163, v1, 7, s77\n"
    "v_and_b32 v163, 0x7f, v163\n"
    "v_mul_u32_u24 v163, 0x24000, v163\n"
    "v_add_u32 v163, s76, v163\n"
    "v_cndmask_b32 v214, v163, v160, vcc\n"
    "v_add_u32 v160, s8, v2\n"
    "v_subrev_u32 v163, 9, v160\n"
    "v_min_u32 v160, v160, v163\n"
    "v_lshlrev_b32 v163, 12, v160\n"
    "v_mul_u32_u24 v160, 0x9000, v160\n"
    "v_cndmask_b32 v5, v163, v160, vcc\n"
    "v_add_u32 v6, s80, v4\n"
    "v_add_u32 v7, 0x1000, v6\n"
    "v_add_u32 v217, 0x1c00, v6\n"
    "v_add_u32 v222, s82, v4\n"
    "v_add_u32 v223, 0x24000, v222\n"
    "v_min_u32 v160, 17, v1\n"
    "v_mad_u32_u24 v160, v160, 7, s19\n"
    "v_and_b32 v160, 0x7f, v160\n"
    "v_lshl_add_u32 v108, v160, 7, s49\n"
    "s_mov_b32 s85, 0x16000\n"
    "buffer_load_dwordx2 v[104:105], v212, s[20:23], s85 offen sc1\n"
    "s_waitcnt vmcnt(0) lgkmcnt(0)\n"
    "s_barrier\n"
    "buffer_store_dwordx4 v[218:221], v215, s[52:55], s9 offen sc1\n"
    "s_bitcmp0_b32 s38, 16\n"
    "s_cbranch_scc1 LM_NG_30\n"
    "LM_chk_31:\n"
    "v_xor_b32 v188, s40, v104\n"
    "v_xor_b32 v189, s41, v105\n"
    "v_or_b32 v188, v188, v189\n"
    "v_cmp_ne_u32 vcc, 0, v188\n"
    "s_nop 1\n"
    "s_cbranch_vccz LM_ok_32\n"
    "s_sub_u32 s39, s39, 1\n"
    "s_cbranch_scc1 LM_ok_32\n"
    "s_sleep 2\n"
    "buffer_load_dwordx2 v[104:105], v212, s[20:23], s85 offen sc1\n"
    "s_waitcnt vmcnt(0)\n"
    "s_branch LM_chk_31\n"
    "LM_ok_32:\n"
    "s_max_i32 s39, s39, 0\n"
    "s_mov_b32 s26, 0\n"
    "s_add_u32 m0, s58, 0\n"
    "s_nop 0\n"
    "buffer_load_dwordx4 v4, s[20:23], s59 offen offset:0 sc1 lds\n"
    "buffer_load_dwordx4 v4, s[20:23], s59 offen offset:1024 sc1 lds\n"
    "buffer_load_dwordx4 v4, s[20:23], s59 offen offset:2048 sc1 lds\n"
    "buffer_load_dwordx4 v4, s[20:23], s59 offen offset:3072 sc1 lds\n"
    "buffer_load_dwordx4 v4, s[20:23], s59 offen offset:3584 sc1 lds\n"
    "s_add_u32 m0, s58, 18432\n"
    "s_nop 0\n"
    "buffer_load_dwordx4 v4, s[20:23], s60 offen offset:0 sc1 lds\n"
    "buffer_load_dwordx4 v4, s[20:23], s60 offen offset:1024 sc1 lds\n"
    "buffer_load_dwordx4 v4, s[20:23], s60 offen offset:2048 sc1 lds\n"
    "buffer_load_dwordx4 v4, s[20:23], s60 offen offset:3072 sc1 lds\n"
    "buffer_load_dwordx4 v4, s[20:23], s60 offen offset:3584 sc1 lds\n"
    "LG1_e1_w:\n"
    "s_waitcnt vmcnt(5)\n"
    "ds_read_b128 v[128:131], v213 offset:0\n"
    "ds_read_b128 v[132:135], v213 offset:1024\n"
    "ds_read_b128 v[136:139], v213 offset:2048\n"
    "ds_read_b128 v[140:143], v213 offset:3072\n"
    "ds_read_b128 v[144:147], v213 offset:3584\n"
    "v_readlane_b32 s71, v214, 1\n"
    "s_waitcnt lgkmcnt(0)\n"
    "v_max3_u32 v106, v128, v129, v130\n"
    "v_max3_u32 v106, v106, v131, v132\n"
    "v_max3_u32 v106, v106, v133, v134\n"
    "v_max3_u32 v106, v106, v135, v136\n"
    "v_max3_u32 v106, v106, v137, v138\n"
    "v_max3_u32 v106, v106, v139, v140\n"
    "v_max3_u32 v106, v106, v141, v142\n"
    "v_max3_u32 v106, v106, v143, v144\n"
    "v_max3_u32 v106, v106, v145, v146\n"
    "v_max_u32 v106, v106, v147\n"
    "v_cmp_eq_u32 vcc, s45, v106\n"
    "s_nop 1\n"
    "s_cbranch_vccnz LG1_a_s\n"
    "LG1_a_r:\n"
    "s_barrier\n"
    "ds_read_b128 v[40:43], v4 offset:0\n"
    "ds_read_b128 v[44:47], v4 offset:1024\n"
    "ds_read_b128 v[48:51], v4 offset:2048\n"
    "ds_read_b128 v[52:55], v4 offset:3072\n"
    "ds_read_b128 v[56:59], v4 offset:4096\n"
    "ds_read_b128 v[60:63], v4 offset:5120\n"
    "s_branch LG1_p0\n"
    "LG1_e0:\n"
    "s_mov_b32 s26, s22\n"
    "s_waitcnt lgkmcnt(0)\n"
    "s_barrier\n"
    "ds_read_b128 v[64:67], v120 offset:18432\n"
    "ds_read_b128 v[68:71], v120 offset:19456\n"
    "ds_read_b128 v[72:75], v120 offset:20480\n"
    "ds_read_b128 v[76:79], v120 offset:21504\n"
    "ds_read_b128 v[80:83], v120 offset:22528\n"
    "ds_read_b128 v[84:87], v120 offset:23552\n"
    "s_waitcnt lgkmcnt(0)\n"
    "v_mfma_f32_16x16x32_bf16 v[24:27], a[0:3], v[64:67], 0\n"
    "v_and_b32 v160, 1, v1\n"
    "v_mad_u32_u24 v160, v160, 7, s19\n"
    "v_mfma_f32_16x16x32_bf16 v[24:27], a[16:19], v[68:71], v[24:27]\n"
    "ds_read_b128 v[40:43], v120 offset:24576\n"
    "v_and_b32 v160, 0x7f, v160\n"
    "v_mfma_f32_16x16x32_bf16 v[24:27], a[32:35], v[72:75], v[24:27]\n"
    "v_lshl_add_u32 v212, v160, 7, s49\n"
    "s_lshl_b32 s9, s33, 7\n"
    "v_mfma_f32_16x16x32_bf16 v[24:27], a[48:51], v[76:79], v[24:27]\n"
    "s_add_u32 s9, s9, 0x1a000\n"
    "s_mul_i32 s58, s32, 0x1200\n"
    "v_mfma_f32_16x16x32_bf16 v[24:27], a[64:67], v[80:83], v[24:27]\n"
    "ds_read_b128 v[44:47], v120 offset:25600\n"
    "v_add_u32 v213, s58, v4\n"
    "v_mfma_f32_16x16x32_bf16 v[24:27], a[80:83], v[84:87], v[24:27]\n"
    "s_add_u32 s59, s19, 0\n"
    "s_and_b32 s59, s59, 0x7f\n"
    "v_mfma_f32_16x16x32_bf16 v[28:31], a[4:7], v[64:67], 0\n"
    "s_mul_i32 s59, s59, 0x4800\n"
    "s_add_u32 s59, s59, s58\n"
    "v_mfma_f32_16x16x32_bf16 v[28:31], a[20:23], v[68:71], v[28:31]\n"
    "ds_read_b128 v[48:51], v120 offset:26624\n"
    "s_add_u32 s59, s59, 0xa22000\n"
    "v_mfma_f32_16x16x32_bf16 v[28:31], a[36:39], v[72:75], v[28:31]\n"
    "s_add_u32 s60, s19, 7\n"
    "s_and_b32 s60, s60, 0x7f\n"
    "v_mfma_f32_16x16x32_bf16 v[28:31], a[52:55], v[76:79], v[28:31]\n"
    "s_mul_i32 s60, s60, 0x4800\n"
    "s_add_u32 s60, s60, s58\n"
    "v_mfma_f32_16x16x32_bf16 v[28:31], a[68:71], v[80:83], v[28:31]\n"
    "ds_read_b128 v[52:55], v120 offset:27648\n"
    "s_add_u32 s60, s60, 0xa22000\n"
    "v_mfma_f32_16x16x32_bf16 v[28:31], a[84:87], v[84:87], v[28:31]\n"
    "s_mov_b64 s[28:29], s[14:15]\n"
    "s_mov_b32 s30, 0x510000\n"
    "v_mfma_f32_16x16x32_bf16 v[32:35], a[8:11], v[64:67], 0\n"
    "v_mfma_f32_16x16x32_bf16 v[32:35], a[24:27], v[68:71], v[32:35]\n"
    "ds_read_b128 v[56:59], v120 offset:28672\n"
    "v_mfma_f32_16x16x32_bf16 v[32:35], a[40:43], v[72:75], v[32:35]\n"
    "s_mov_b64 s[24:25], s[20:21]\n"
    "v_mov_b32 v116, s45\n"
    "v_mov_b32 v117, s45\n"
    "v_mfma_f32_16x16x32_bf16 v[32:35], a[56:59], v[76:79], v[32:35]\n"
    "v_mov_b32 v118, s45\n"
    "v_mov_b32 v119, s45\n"
    "v_lshrrev_b32 v160, 3, v1\n"
    "v_mfma_f32_16x16x32_bf16 v[32:35], a[72:75], v[80:83], v[32:35]\n"
    "ds_read_b128 v[60:63], v120 offset:29696\n"
    "v_mul_u32_u24 v160, 0x480, v160\n"
    "v_and_b32 v161, 7, v1\n"
    "v_mfma_f32_16x16x32_bf16 v[32:35], a[88:91], v[84:87], v[32:35]\n"
    "v_lshl_add_u32 v111, v161, 4, v160\n"
    "v_bfe_u32 v161, v1, 2, 2\n"
    "v_and_b32 v162, 3, v1\n"
    "v_lshl_add_u32 v161, v3, 2, v161\n"
    "v_mfma_f32_16x16x32_bf16 v[36:39], a[12:15], v[64:67], 0\n"
    "v_lshl_add_u32 v161, v161, 7, s84\n"
    "s_bitcmp0_b32 s18, 0\n"
    "s_cselect_b32 s77, 4, 3\n"
    "v_mfma_f32_16x16x32_bf16 v[36:39], a[28:31], v[68:71], v[36:39]\n"
    "s_cselect_b32 s78, 8, 32\n"
    "v_lshlrev_b32 v160, s77, v162\n"
    "v_add_u32 v114, v160, v161\n"
    "v_mfma_f32_16x16x32_bf16 v[36:39], a[44:47], v[72:75], v[36:39]\n"
    "v_add_u32 v126, s78, v114\n"
    "s_bitcmp0_b32 s18, 1\n"
    "s_cselect_b32 s77, 4, 3\n"
    "s_cselect_b32 s78, 8, 32\n"
    "v_mfma_f32_16x16x32_bf16 v[36:39], a[60:63], v[76:79], v[36:39]\n"
    "v_lshlrev_b32 v160, s77, v162\n"
    "v_add_u32 v115, v160, v161\n"
    "v_add_u32 v127, s78, v115\n"
    "v_mfma_f32_16x16x32_bf16 v[36:39], a[76:79], v[80:83], v[36:39]\n"
    "v_lshrrev_b32 v163, 1, v1\n"
    "v_min_u32 v163, 8, v163\n"
    "v_add_u32 v163, s8, v163\n"
    "v_mfma_f32_16x16x32_bf16 v[36:39], a[92:95], v[84:87], v[36:39]\n"
    "v_subrev_u32 v164, 9, v163\n"
    "v_min_u32 v163, v163, v164\n"
    "s_lshl_b32 s78, s10, 1\n"
    "s_waitcnt lgkmcnt(0)\n"
    "v_mfma_f32_16x16x32_bf16 v[24:27], a[96:99], v[40:43], v[24:27]\n"
    "s_sub_u32 s78, s7, s78\n"
    "s_add_u32 s78, s78, 16\n"
    "v_and_b32 v160, 1, v1\n"
    "v_mfma_f32_16x16x32_bf16 v[24:27], a[112:115], v[44:47], v[24:27]\n"
    "ds_read_b128 v[64:67], v120 offset:30720\n"
    "v_add_u32 v160, s78, v160\n"
    "v_mad_u32_u24 v160, v163, 14, v160\n"
    "v_mfma_f32_16x16x32_bf16 v[24:27], a[128:131], v[48:51], v[24:27]\n"
    "s_mov_b32 s85, 90112\n"
    "v_and_b32 v160, 0x7f, v160\n"
    "v_mul_u32_u24 v164, 0x2493, v160\n"
    "v_lshrrev_b32 v164, 16, v164\n"
    "v_mfma_f32_16x16x32_bf16 v[24:27], a[144:147], v[52:55], v[24:27]\n"
    "buffer_load_dwordx2 v[104:105], v212, s[20:23], s85 offen sc1\n"
    "s_waitcnt vmcnt(1)\n"
    "v_mul_u32_u24 v164, 7, v164\n"
    "v_mfma_f32_16x16x32_bf16 v[24:27], a[160:163], v[56:59], v[24:27]\n"
    "ds_read_b128 v[68:71], v120 offset:31744\n"
    "s_barrier\n"
    "v_sub_u32 v160, v160, v164\n"
    "v_mfma_f32_16x16x32_bf16 v[24:27], a[176:179], v[60:63], v[24:27]\n"
    "buffer_store_dwordx4 v[218:221], v215, s[52:55], s9 offen sc1\n"
    "s_mul_i32 s78, s10, 9\n"
    "v_add_u32 v163, s78, v163\n"
    "v_mad_u32_u24 v160, v163, 7, v160\n"
    "v_mfma_f32_16x16x32_bf16 v[28:31], a[100:103], v[40:43], v[28:31]\n"
    "v_and_b32 v163, 3, v1\n"
    "s_and_b32 s78, s2, 0xf9\n"
    "s_add_u32 s78, s78, 0x100\n"
    "v_mfma_f32_16x16x32_bf16 v[28:31], a[116:119], v[44:47], v[28:31]\n"
    "ds_read_b128 v[72:75], v120 offset:32768\n"
    "v_lshl_add_u32 v163, v163, 1, s78\n"
    "v_cmp_gt_u32 vcc, 18, v1\n"
    "v_mfma_f32_16x16x32_bf16 v[28:31], a[132:135], v[48:51], v[28:31]\n"
    "s_nop 1\n"
    "v_cndmask_b32 v160, v163, v160, vcc\n"
    "s_add_u32 s78, s7, 0x300\n"
    "v_and_b32 v163, 1, v1\n"
    "v_mfma_f32_16x16x32_bf16 v[28:31], a[148:151], v[52:55], v[28:31]\n"
    "v_add_u32 v163, s78, v163\n"
    "v_cmp_gt_u32 vcc, 32, v1\n"
    "v_mov_b32 v125, 0x7ffffff0\n"
    "v_mfma_f32_16x16x32_bf16 v[28:31], a[164:167], v[56:59], v[28:31]\n"
    "ds_read_b128 v[76:79], v120 offset:33792\n"
    "s_add_u32 s15, s13, 0x1000\n"
    "v_cndmask_b32 v160, v163, v160, vcc\n"
    "v_mfma_f32_16x16x32_bf16 v[28:31], a[180:183], v[60:63], v[28:31]\n"
    "v_cndmask_b32 v125, v125, v4, vcc\n"
    "v_lshl_add_u32 v110, v160, 7, s49\n"
    "s_mov_b64 s[4:5], s[20:21]\n"
    "s_mov_b32 s7, s23\n"
    "v_mfma_f32_16x16x32_bf16 v[32:35], a[104:107], v[40:43], v[32:35]\n"
    "s_lshl_b32 s77, s32, 6\n"
    "s_lshl_b32 s78, s10, 1\n"
    "s_add_u32 s77, s77, s78\n"
    "v_mfma_f32_16x16x32_bf16 v[32:35], a[120:123], v[44:47], v[32:35]\n"
    "ds_read_b128 v[80:83], v120 offset:34816\n"
    "v_and_b32 v160, 1, v1\n"
    "v_add_u32 v160, s77, v160\n"
    "v_mfma_f32_16x16x32_bf16 v[32:35], a[136:139], v[48:51], v[32:35]\n"
    "v_lshrrev_b32 v163, 1, v2\n"
    "v_lshl_add_u32 v160, v163, 3, v160\n"
    "v_lshl_add_u32 v109, v160, 7, s49\n"
    "v_mov_b32 v112, 0x409d30fc\n"
    "v_mfma_f32_16x16x32_bf16 v[32:35], a[152:155], v[52:55], v[32:35]\n"
    "v_add_u32 v121, 0x10000, v4\n"
    "v_add_u32 v122, 0x20000, v4\n"
    "v_mad_u32_u24 v160, v1, 14, s75\n"
    "v_mfma_f32_16x16x32_bf16 v[32:35], a[168:171], v[56:59], v[32:35]\n"
    "ds_read_b128 v[84:87], v120 offset:35840\n"
    "v_and_b32 v160, 0x7f, v160\n"
    "v_mul_u32_u24 v160, 0x4800, v160\n"
    "v_mfma_f32_16x16x32_bf16 v[32:35], a[184:187], v[60:63], v[32:35]\n"
    "v_add_u32 v160, s73, v160\n"
    "v_cmp_gt_u32 vcc, 16, v1\n"
    "s_add_u32 s77, s19, 9\n"
    "v_mad_u32_u24 v163, v1, 7, s77\n"
    "v_mfma_f32_16x16x32_bf16 v[36:39], a[108:111], v[40:43], v[36:39]\n"
    "v_and_b32 v163, 0x7f, v163\n"
    "v_mul_u32_u24 v163, 0x24000, v163\n"
    "v_add_u32 v163, s76, v163\n"
    "v_mfma_f32_16x16x32_bf16 v[36:39], a[124:127], v[44:47], v[36:39]\n"
    "v_cndmask_b32 v214, v163, v160, vcc\n"
    "v_add_u32 v160, s8, v2\n"
    "v_subrev_u32 v163, 9, v160\n"
    "v_mfma_f32_16x16x32_bf16 v[36:39], a[140:143], v[48:51], v[36:39]\n"
    "v_min_u32 v160, v160, v163\n"
    "v_lshlrev_b32 v163, 12, v160\n"
    "v_mul_u32_u24 v160, 0x9000, v160\n"
    "v_cndmask_b32 v5, v163, v160, vcc\n"
    "v_mfma_f32_16x16x32_bf16 v[36:39], a[156:159], v[52:55], v[36:39]\n"
    "v_add_u32 v6, s80, v4\n"
    "v_add_u32 v7, 0x1000, v6\n"
    "v_add_u32 v217, 0x1c00, v6\n"
    "v_mfma_f32_16x16x32_bf16 v[36:39], a[172:175], v[56:59], v[36:39]\n"
    "v_add_u32 v222, s82, v4\n"
    "v_add_u32 v223, 0x24000, v222\n"
    "v_min_u32 v160, 17, v1\n"
    "v_mfma_f32_16x16x32_bf16 v[36:39], a[188:191], v[60:63], v[36:39]\n"
    "v_mad_u32_u24 v160, v160, 7, s19\n"
    "v_and_b32 v160, 0x7f, v160\n"
    "v_lshl_add_u32 v108, v160, 7, s49\n"
    "s_waitcnt lgkmcnt(0)\n"
    "v_mfma_f32_16x16x32_bf16 v[24:27], a[192:195], v[64:67], v[24:27]\n"
    "s_waitcnt vmcnt(1)\n"
    "v_xor_b32 v106, s40, v104\n"
    "v_mfma_f32_16x16x32_bf16 v[24:27], a[208:211], v[68:71], v[24:27]\n"
    "v_xor_b32 v107, s41, v105\n"
    "v_or_b32 v106, v106, v107\n"
    "v_mfma_f32_16x16x32_bf16 v[24:27], a[224:227], v[72:75], v[24:27]\n"
    "v_cmp_ne_u32 vcc, 0, v106\n"
    "s_nop 1\n"
    "v_mfma_f32_16x16x32_bf16 v[24:27], a[240:243], v[76:79], v[24:27]\n"
    "s_cbranch_vccnz LG1_hx_s\n"
    "LG1_hx_r:\n"
    "s_add_u32 m0, s58, 0\n"
    "v_mfma_f32_16x16x32_bf16 v[24:27], v[224:227], v[80:83], v[24:27]\n"
    "buffer_load_dwordx4 v4, s[20:23], s59 offen offset:0 sc1 lds\n"
    "v_mfma_f32_16x16x32_bf16 v[24:27], v[240:243], v[84:87], v[24:27]\n"
    "buffer_load_dwordx4 v4, s[20:23], s59 offen offset:1024 sc1 lds\n"
    "v_mfma_f32_16x16x32_bf16 v[28:31], a[196:199], v[64:67], v[28:31]\n"
    "buffer_load_dwordx4 v4, s[20:23], s59 offen offset:2048 sc1 lds\n"
    "v_mfma_f32_16x16x32_bf16 v[28:31], a[212:215], v[68:71], v[28:31]\n"
    "buffer_load_dwordx4 v4, s[20:23], s59 offen offset:3072 sc1 lds\n"
    "v_mfma_f32_16x16x32_bf16 v[28:31], a[228:231], v[72:75], v[28:31]\n"
    "buffer_load_dwordx4 v4, s[20:23], s59 offen offset:3584 sc1 lds\n"
    "s_add_u32 m0, s58, 18432\n"
    "v_mfma_f32_16x16x32_bf16 v[28:31], a[244:247], v[76:79], v[28:31]\n"
    "buffer_load_dwordx4 v4, s[20:23], s60 offen offset:0 sc1 lds\n"
    "v_mfma_f32_16x16x32_bf16 v[28:31], v[228:231], v[80:83], v[28:31]\n"
    "buffer_load_dwordx4 v4, s[20:23], s60 offen offset:1024 sc1 lds\n"
    "v_mfma_f32_16x16x32_bf16 v[28:31], v[244:247], v[84:87], v[28:31]\n"
    "buffer_load_dwordx4 v4, s[20:23], s60 offen offset:2048 sc1 lds\n"
    "v_mfma_f32_16x16x32_bf16 v[32:35], a[200:203], v[64:67], v[32:35]\n"
    "buffer_load_dwordx4 v4, s[20:23], s60 offen offset:3072 sc1 lds\n"
    "v_mfma_f32_16x16x32_bf16 v[32:35], a[216:219], v[68:71], v[32:35]\n"
    "buffer_load_dwordx4 v4, s[20:23], s60 offen offset:3584 sc1 lds\n"
    "v_mfma_f32_16x16x32_bf16 v[32:35], a[232:235], v[72:75], v[32:35]\n"
    "v_mfma_f32_16x16x32_bf16 v[32:35], a[248:251], v[76:79], v[32:35]\n"
    "v_mfma_f32_16x16x32_bf16 v[32:35], v[232:235], v[80:83], v[32:35]\n"
    "v_mfma_f32_16x16x32_bf16 v[32:35], v[248:251], v[84:87], v[32:35]\n"
    "v_mfma_f32_16x16x32_bf16 v[36:39], a[204:207], v[64:67], v[36:39]\n"
    "v_mfma_f32_16x16x32_bf16 v[36:39], a[220:223], v[68:71], v[36:39]\n"
    "v_mfma_f32_16x16x32_bf16 v[36:39], a[236:239], v[72:75], v[36:39]\n"
    "v_mfma_f32_16x16x32_bf16 v[36:39], a[252:255], v[76:79], v[36:39]\n"
    "v_mfma_f32_16x16x32_bf16 v[36:39], v[236:239], v[80:83], v[36:39]\n"
    "v_mfma_f32_16x16x32_bf16 v[36:39], v[252:255], v[84:87], v[36:39]\n"
    "s_branch LG1_e1_w\n"
    "LG1_p0:\n"
    "s_waitcnt lgkmcnt(0)\n"
    "v_mfma_f32_16x16x32_bf16 v[8:11], a[0:3], v[40:43], 0\n"
    "v_fma_f32 v96, v24, v24, v112\n"
    "v_fma_f32 v97, v25, v25, v112\n"
    "v_mfma_f32_16x16x32_bf16 v[8:11], a[16:19], v[44:47], v[8:11]\n"
    "ds_read_b128 v[64:67], v4 offset:6144\n"
    "v_mul_f32 v96, v96, v24\n"
    "v_mfma_f32_16x16x32_bf16 v[8:11], a[32:35], v[48:51], v[8:11]\n"
    "v_mul_f32 v97, v97, v25\n"
    "v_mfma_f32_16x16x32_bf16 v[8:11], a[48:51], v[52:55], v[8:11]\n"
    "v_exp_f32 v96, -v96\n"
    "v_mfma_f32_16x16x32_bf16 v[8:11], a[64:67], v[56:59], v[8:11]\n"
    "ds_read_b128 v[68:71], v4 offset:7168\n"
    "v_mfma_f32_16x16x32_bf16 v[8:11], a[80:83], v[60:63], v[8:11]\n"
    "v_exp_f32 v97, -v97\n"
    "v_mfma_f32_16x16x32_bf16 v[12:15], a[4:7], v[40:43], 0\n"
    "v_mul_f32 v98, v24, v28\n"
    "v_mul_f32 v99, v25, v29\n"
    "v_mfma_f32_16x16x32_bf16 v[12:15], a[20:23], v[44:47], v[12:15]\n"
    "ds_read_b128 v[72:75], v4 offset:8192\n"
    "v_add_f32 v96, 1.0, v96\n"
    "v_mfma_f32_16x16x32_bf16 v[12:15], a[36:39], v[48:51], v[12:15]\n"
    "s_mov_b32 s85, 106496\n"
    "v_add_f32 v97, 1.0, v97\n"
    "v_mfma_f32_16x16x32_bf16 v[12:15], a[52:55], v[52:55], v[12:15]\n"
    "buffer_load_dwordx2 v[104:105], v108, s[20:23], s85 offen sc1\n"
    "v_mfma_f32_16x16x32_bf16 v[12:15], a[68:71], v[56:59], v[12:15]\n"
    "ds_read_b128 v[76:79], v4 offset:9216\n"
    "v_rcp_f32 v96, v96\n"
    "v_mfma_f32_16x16x32_bf16 v[12:15], a[84:87], v[60:63], v[12:15]\n"
    "v_rcp_f32 v97, v97\n"
    "v_mfma_f32_16x16x32_bf16 v[16:19], a[8:11], v[40:43], 0\n"
    "v_fma_f32 v100, v26, v26, v112\n"
    "v_fma_f32 v101, v27, v27, v112\n"
    "v_mfma_f32_16x16x32_bf16 v[16:19], a[24:27], v[44:47], v[16:19]\n"
    "ds_read_b128 v[80:83], v4 offset:10240\n"
    "v_mul_f32 v98, v98, v96\n"
    "v_mfma_f32_16x16x32_bf16 v[16:19], a[40:43], v[48:51], v[16:19]\n"
    "s_waitcnt vmcnt(1)\n"
    "v_mul_f32 v99, v99, v97\n"
    "v_mfma_f32_16x16x32_bf16 v[16:19], a[56:59], v[52:55], v[16:19]\n"
    "ds_read_b128 v[128:131], v213 offset:18432\n"
    "v_cvt_pk_bf16_f32 v92, v98, v99\n"
    "v_mfma_f32_16x16x32_bf16 v[16:19], a[72:75], v[56:59], v[16:19]\n"
    "ds_read_b128 v[84:87], v4 offset:11264\n"
    "v_mul_f32 v100, v100, v26\n"
    "v_mfma_f32_16x16x32_bf16 v[16:19], a[88:91], v[60:63], v[16:19]\n"
    "ds_read_b128 v[132:135], v213 offset:19456\n"
    "v_mul_f32 v101, v101, v27\n"
    "v_mfma_f32_16x16x32_bf16 v[20:23], a[12:15], v[40:43], 0\n"
    "ds_read_b128 v[136:139], v213 offset:20480\n"
    "v_mfma_f32_16x16x32_bf16 v[20:23], a[28:31], v[44:47], v[20:23]\n"
    "ds_read_b128 v[140:143], v213 offset:21504\n"
    "v_mfma_f32_16x16x32_bf16 v[20:23], a[44:47], v[48:51], v[20:23]\n"
    "ds_read_b128 v[144:147], v213 offset:22016\n"
    "v_exp_f32 v100, -v100\n"
    "v_mfma_f32_16x16x32_bf16 v[20:23], a[60:63], v[52:55], v[20:23]\n"
    "v_exp_f32 v101, -v101\n"
    "v_mfma_f32_16x16x32_bf16 v[20:23], a[76:79], v[56:59], v[20:23]\n"
    "v_mul_f32 v102, v26, v30\n"
    "v_mul_f32 v103, v27, v31\n"
    "v_mfma_f32_16x16x32_bf16 v[20:23], a[92:95], v[60:63], v[20:23]\n"
    "v_add_f32 v100, 1.0, v100\n"
    "s_waitcnt lgkmcnt(0)\n"
    "v_mfma_f32_16x16x32_bf16 v[8:11], a[96:99], v[64:67], v[8:11]\n"
    "v_max3_u32 v106, v128, v129, v130\n"
    "v_max3_u32 v106, v106, v131, v132\n"
    "v_mfma_f32_16x16x32_bf16 v[8:11], a[112:115], v[68:71], v[8:11]\n"
    "ds_read_b128 v[40:43], v4 offset:12288\n"
    "v_max3_u32 v106, v106, v133, v134\n"
    "v_mfma_f32_16x16x32_bf16 v[8:11], a[128:131], v[72:75], v[8:11]\n"
    "v_max3_u32 v106, v106, v135, v136\n"
    "v_max3_u32 v106, v106, v137, v138\n"
    "v_mfma_f32_16x16x32_bf16 v[8:11], a[144:147], v[76:79], v[8:11]\n"
    "v_max3_u32 v106, v106, v139, v140\n"
    "v_max3_u32 v106, v106, v141, v142\n"
    "v_mfma_f32_16x16x32_bf16 v[8:11], a[160:163], v[80:83], v[8:11]\n"
    "ds_read_b128 v[44:47], v4 offset:13312\n"
    "v_max3_u32 v106, v106, v143, v144\n"
    "v_mfma_f32_16x16x32_bf16 v[8:11], a[176:179], v[84:87], v[8:11]\n"
    "v_max3_u32 v106, v106, v145, v146\n"
    "v_max_u32 v106, v106, v147\n"
    "v_mfma_f32_16x16x32_bf16 v[12:15], a[100:103], v[64:67], v[12:15]\n"
    "v_cmp_eq_u32 vcc, s45, v106\n"
    "s_nop 1\n"
    "v_add_f32 v101, 1.0, v101\n"
    "v_mfma_f32_16x16x32_bf16 v[12:15], a[116:119], v[68:71], v[12:15]\n"
    "ds_read_b128 v[48:51], v4 offset:14336\n"
    "s_cbranch_vccnz LG1_b_s\n"
    "LG1_b_r:\n"
    "v_mfma_f32_16x16x32_bf16 v[12:15], a[132:135], v[72:75], v[12:15]\n"
    "v_rcp_f32 v100, v100\n"
    "v_mfma_f32_16x16x32_bf16 v[12:15], a[148:151], v[76:79], v[12:15]\n"
    "v_rcp_f32 v101, v101\n"
    "v_mfma_f32_16x16x32_bf16 v[12:15], a[164:167], v[80:83], v[12:15]\n"
    "ds_read_b128 v[52:55], v4 offset:15360\n"
    "v_fma_f32 v96, v32, v32, v112\n"
    "v_mfma_f32_16x16x32_bf16 v[12:15], a[180:183], v[84:87], v[12:15]\n"
    "v_fma_f32 v97, v33, v33, v112\n"
    "v_mul_f32 v102, v102, v100\n"
    "v_mfma_f32_16x16x32_bf16 v[16:19], a[104:107], v[64:67], v[16:19]\n"
    "v_mul_f32 v103, v103, v101\n"
    "v_cvt_pk_bf16_f32 v93, v102, v103\n"
    "v_mfma_f32_16x16x32_bf16 v[16:19], a[120:123], v[68:71], v[16:19]\n"
    "ds_read_b128 v[56:59], v4 offset:16384\n"
    "v_mul_f32 v96, v96, v32\n"
    "v_mfma_f32_16x16x32_bf16 v[16:19], a[136:139], v[72:75], v[16:19]\n"
    "v_mul_f32 v97, v97, v33\n"
    "v_mfma_f32_16x16x32_bf16 v[16:19], a[152:155], v[76:79], v[16:19]\n"
    "v_exp_f32 v96, -v96\n"
    "v_mfma_f32_16x16x32_bf16 v[16:19], a[168:171], v[80:83], v[16:19]\n"
    "ds_read_b128 v[60:63], v4 offset:17408\n"
    "v_exp_f32 v97, -v97\n"
    "v_mfma_f32_16x16x32_bf16 v[16:19], a[184:187], v[84:87], v[16:19]\n"
    "v_mul_f32 v98, v32, v36\n"
    "v_mul_f32 v99, v33, v37\n"
    "v_mfma_f32_16x16x32_bf16 v[20:23], a[108:111], v[64:67], v[20:23]\n"
    "v_add_f32 v96, 1.0, v96\n"
    "v_add_f32 v97, 1.0, v97\n"
    "v_mfma_f32_16x16x32_bf16 v[20:23], a[124:127], v[68:71], v[20:23]\n"
    "v_rcp_f32 v96, v96\n"
    "v_mfma_f32_16x16x32_bf16 v[20:23], a[140:143], v[72:75], v[20:23]\n"
    "s_waitcnt vmcnt(0)\n"
    "v_xor_b32 v106, s40, v104\n"
    "v_mfma_f32_16x16x32_bf16 v[20:23], a[156:159], v[76:79], v[20:23]\n"
    "v_xor_b32 v107, s41, v105\n"
    "v_or_b32 v106, v106, v107\n"
    "v_mfma_f32_16x16x32_bf16 v[20:23], a[172:175], v[80:83], v[20:23]\n"
    "v_cmp_ne_u32 vcc, 0, v106\n"
    "s_nop 1\n"
    "v_mfma_f32_16x16x32_bf16 v[20:23], a[188:191], v[84:87], v[20:23]\n"
    "s_waitcnt lgkmcnt(0)\n"
    "s_barrier\n"
    "v_mfma_f32_16x16x32_bf16 v[8:11], a[192:195], v[40:43], v[8:11]\n"
    "s_cbranch_vccnz LG1_fx_s\n"
    "LG1_fx_r:\n"
    "v_mfma_f32_16x16x32_bf16 v[8:11], a[208:211], v[44:47], v[8:11]\n"
    "ds_read_b128 v[64:67], v4 offset:18432\n"
    "s_add_u32 s77, s71, 0\n"
    "v_mfma_f32_16x16x32_bf16 v[8:11], a[224:227], v[48:51], v[8:11]\n"
    "s_add_u32 m0, s70, 36864\n"
    "v_rcp_f32 v97, v97\n"
    "v_mfma_f32_16x16x32_bf16 v[8:11], a[240:243], v[52:55], v[8:11]\n"
    "buffer_load_dwordx4 v4, s[20:23], s77 offen offset:0 sc1 lds\n"
    "v_fma_f32 v100, v34, v34, v112\n"
    "v_mfma_f32_16x16x32_bf16 v[8:11], v[224:227], v[56:59], v[8:11]\n"
    "ds_read_b128 v[68:71], v4 offset:19456\n"
    "v_fma_f32 v101, v35, v35, v112\n"
    "v_mfma_f32_16x16x32_bf16 v[8:11], v[240:243], v[60:63], v[8:11]\n"
    "v_mul_f32 v98, v98, v96\n"
    "v_mul_f32 v99, v99, v97\n"
    "v_mfma_f32_16x16x32_bf16 v[12:15], a[196:199], v[40:43], v[12:15]\n"
    "v_cvt_pk_bf16_f32 v94, v98, v99\n"
    "v_mfma_f32_16x16x32_bf16 v[12:15], a[212:215], v[44:47], v[12:15]\n"
    "ds_read_b128 v[72:75], v4 offset:20480\n"
    "v_mul_f32 v100, v100, v34\n"
    "v_mfma_f32_16x16x32_bf16 v[12:15], a[228:231], v[48:51], v[12:15]\n"
    "buffer_load_dwordx4 v4, s[20:23], s77 offen offset:1024 sc1 lds\n"
    "v_mul_f32 v101, v101, v35\n"
    "v_mfma_f32_16x16x32_bf16 v[12:15], a[244:247], v[52:55], v[12:15]\n"
    "v_exp_f32 v100, -v100\n"
    "v_mfma_f32_16x16x32_bf16 v[12:15], v[228:231], v[56:59], v[12:15]\n"
    "ds_read_b128 v[76:79], v4 offset:21504\n"
    "v_mfma_f32_16x16x32_bf16 v[12:15], v[244:247], v[60:63], v[12:15]\n"
    "v_exp_f32 v101, -v101\n"
    "v_mfma_f32_16x16x32_bf16 v[16:19], a[200:203], v[40:43], v[16:19]\n"
    "buffer_load_dwordx4 v4, s[20:23], s77 offen offset:2048 sc1 lds\n"
    "v_mul_f32 v102, v34, v38\n"
    "v_mul_f32 v103, v35, v39\n"
    "v_mfma_f32_16x16x32_bf16 v[16:19], a[216:219], v[44:47], v[16:19]\n"
    "ds_read_b128 v[80:83], v4 offset:22528\n"
    "v_add_f32 v100, 1.0, v100\n"
    "v_mfma_f32_16x16x32_bf16 v[16:19], a[232:235], v[48:51], v[16:19]\n"
    "v_add_f32 v101, 1.0, v101\n"
    "v_mfma_f32_16x16x32_bf16 v[16:19], a[248:251], v[52:55], v[16:19]\n"
    "v_rcp_f32 v100, v100\n"
    "v_mfma_f32_16x16x32_bf16 v[16:19], v[232:235], v[56:59], v[16:19]\n"
    "ds_read_b128 v[84:87], v4 offset:23552\n"
    "v_mfma_f32_16x16x32_bf16 v[16:19], v[248:251], v[60:63], v[16:19]\n"
    "buffer_load_dwordx4 v4, s[20:23], s77 offen offset:3072 sc1 lds\n"
    "s_add_u32 s77, s71, 4096\n"
    "v_mfma_f32_16x16x32_bf16 v[20:23], a[204:207], v[40:43], v[20:23]\n"
    "s_add_u32 m0, s70, 40960\n"
    "v_mfma_f32_16x16x32_bf16 v[20:23], a[220:223], v[44:47], v[20:23]\n"
    "v_rcp_f32 v101, v101\n"
    "v_mfma_f32_16x16x32_bf16 v[20:23], a[236:239], v[48:51], v[20:23]\n"
    "s_nop 0\n"
    "v_mul_f32 v102, v102, v100\n"
    "v_mfma_f32_16x16x32_bf16 v[20:23], a[252:255], v[52:55], v[20:23]\n"
    "buffer_load_dwordx4 v4, s[20:23], s77 offen offset:0 sc1 lds\n"
    "v_mul_f32 v103, v103, v101\n"
    "v_mfma_f32_16x16x32_bf16 v[20:23], v[236:239], v[56:59], v[20:23]\n"
    "v_cvt_pk_bf16_f32 v95, v102, v103\n"
    "v_mfma_f32_16x16x32_bf16 v[20:23], v[252:255], v[60:63], v[20:23]\n"
    "buffer_store_dwordx4 v[92:95], v4, s[24:27], s74 offen sc1\n"
    "s_waitcnt lgkmcnt(0)\n"
    "v_mfma_f32_16x16x32_bf16 v[24:27], a[0:3], v[64:67], 0\n"
    "v_readlane_b32 s74, v214, 17\n"
    "v_fma_f32 v96, v8, v8, v112\n"
    "v_mfma_f32_16x16x32_bf16 v[24:27], a[16:19], v[68:71], v[24:27]\n"
    "ds_read_b128 v[40:43], v4 offset:24576\n"
    "v_fma_f32 v97, v9, v9, v112\n"
    "v_mfma_f32_16x16x32_bf16 v[24:27], a[32:35], v[72:75], v[24:27]\n"
    "buffer_load_dwordx4 v4, s[20:23], s77 offen offset:1024 sc1 lds\n"
    "v_mul_f32 v96, v96, v8\n"
    "v_mfma_f32_16x16x32_bf16 v[24:27], a[48:51], v[76:79], v[24:27]\n"
    "LS_4:\n"
    "s_cbranch_execz LS_8\n"
    "s_mov_b32 s26, s22\n"
    "v_mul_f32 v97, v97, v9\n"
    "v_mfma_f32_16x16x32_bf16 v[24:27], a[64:67], v[80:83], v[24:27]\n"
    "ds_read_b128 v[44:47], v4 offset:25600\n"
    "v_mfma_f32_16x16x32_bf16 v[24:27], a[80:83], v[84:87], v[24:27]\n"
    "v_exp_f32 v96, -v96\n"
    "v_mfma_f32_16x16x32_bf16 v[28:31], a[4:7], v[64:67], 0\n"
    "buffer_load_dwordx4 v4, s[20:23], s77 offen offset:2048 sc1 lds\n"
    "v_mfma_f32_16x16x32_bf16 v[28:31], a[20:23], v[68:71], v[28:31]\n"
    "ds_read_b128 v[48:51], v4 offset:26624\n"
    "v_mfma_f32_16x16x32_bf16 v[28:31], a[36:39], v[72:75], v[28:31]\n"
    "v_exp_f32 v97, -v97\n"
    "v_mfma_f32_16x16x32_bf16 v[28:31], a[52:55], v[76:79], v[28:31]\n"
    "v_mul_f32 v98, v8, v12\n"
    "v_mul_f32 v99, v9, v13\n"
    "v_mfma_f32_16x16x32_bf16 v[28:31], a[68:71], v[80:83], v[28:31]\n"
    "ds_read_b128 v[52:55], v4 offset:27648\n"
    "v_add_f32 v96, 1.0, v96\n"
    "v_mfma_f32_16x16x32_bf16 v[28:31], a[84:87], v[84:87], v[28:31]\n"
    "buffer_load_dwordx4 v4, s[20:23], s77 offen offset:3072 sc1 lds\n"
    "s_add_u32 s77, s71, 8192\n"
    "v_mfma_f32_16x16x32_bf16 v[32:35], a[8:11], v[64:67], 0\n"
    "s_add_u32 m0, s70, 45056\n"
    "v_add_f32 v97, 1.0, v97\n"
    "v_mfma_f32_16x16x32_bf16 v[32:35], a[24:27], v[68:71], v[32:35]\n"
    "ds_read_b128 v[56:59], v4 offset:28672\n"
    "v_mfma_f32_16x16x32_bf16 v[32:35], a[40:43], v[72:75], v[32:35]\n"
    "v_rcp_f32 v96, v96\n"
    "v_mfma_f32_16x16x32_bf16 v[32:35], a[56:59], v[76:79], v[32:35]\n"
    "buffer_load_dwordx4 v4, s[20:23], s77 offen offset:0 sc1 lds\n"
    "v_readlane_b32 s71, v214, 2\n"
    "v_mfma_f32_16x16x32_bf16 v[32:35], a[72:75], v[80:83], v[32:35]\n"
    "ds_read_b128 v[60:63], v4 offset:29696\n"
    "v_mfma_f32_16x16x32_bf16 v[32:35], a[88:91], v[84:87], v[32:35]\n"
    "v_rcp_f32 v97, v97\n"
    "v_mfma_f32_16x16x32_bf16 v[36:39], a[12:15], v[64:67], 0\n"
    "v_fma_f32 v100, v10, v10, v112\n"
    "v_fma_f32 v101, v11, v11, v112\n"
    "v_mfma_f32_16x16x32_bf16 v[36:39], a[28:31], v[68:71], v[36:39]\n"
    "v_mul_f32 v98, v98, v96\n"
    "v_mul_f32 v99, v99, v97\n"
    "v_mfma_f32_16x16x32_bf16 v[36:39], a[44:47], v[72:75], v[36:39]\n"
    "v_cvt_pk_bf16_f32 v88, v98, v99\n"
    "v_mul_f32 v100, v100, v10\n"
    "v_mfma_f32_16x16x32_bf16 v[36:39], a[60:63], v[76:79], v[36:39]\n"
    "v_mul_f32 v101, v101, v11\n"
    "v_mfma_f32_16x16x32_bf16 v[36:39], a[76:79], v[80:83], v[36:39]\n"
    "v_exp_f32 v100, -v100\n"
    "v_mfma_f32_16x16x32_bf16 v[36:39], a[92:95], v[84:87], v[36:39]\n"
    "s_waitcnt lgkmcnt(0)\n"
    "v_mfma_f32_16x16x32_bf16 v[24:27], a[96:99], v[40:43], v[24:27]\n"
    "v_exp_f32 v101, -v101\n"
    "v_mfma_f32_16x16x32_bf16 v[24:27], a[112:115], v[44:47], v[24:27]\n"
    "ds_read_b128 v[64:67], v4 offset:30720\n"
    "v_mul_f32 v102, v10, v14\n"
    "v_mfma_f32_16x16x32_bf16 v[24:27], a[128:131], v[48:51], v[24:27]\n"
    "v_mul_f32 v103, v11, v15\n"
    "v_mfma_f32_16x16x32_bf16 v[24:27], a[144:147], v[52:55], v[24:27]\n"
    "v_add_f32 v100, 1.0, v100\n"
    "v_add_f32 v101, 1.0, v101\n"
    "v_mfma_f32_16x16x32_bf16 v[24:27], a[160:163], v[56:59], v[24:27]\n"
    "ds_read_b128 v[68:71], v4 offset:31744\n"
    "v_mfma_f32_16x16x32_bf16 v[24:27], a[176:179], v[60:63], v[24:27]\n"
    "v_rcp_f32 v100, v100\n"
    "v_mfma_f32_16x16x32_bf16 v[28:31], a[100:103], v[40:43], v[28:31]\n"
    "v_mfma_f32_16x16x32_bf16 v[28:31], a[116:119], v[44:47], v[28:31]\n"
    "ds_read_b128 v[72:75], v4 offset:32768\n"
    "v_mfma_f32_16x16x32_bf16 v[28:31], a[132:135], v[48:51], v[28:31]\n"
    "v_rcp_f32 v101, v101\n"
    "v_mfma_f32_16x16x32_bf16 v[28:31], a[148:151], v[52:55], v[28:31]\n"
    "v_fma_f32 v96, v16, v16, v112\n"
    "v_fma_f32 v97, v17, v17, v112\n"
    "v_mfma_f32_16x16x32_bf16 v[28:31], a[164:167], v[56:59], v[28:31]\n"
    "ds_read_b128 v[76:79], v4 offset:33792\n"
    "v_mul_f32 v102, v102, v100\n"
    "v_mfma_f32_16x16x32_bf16 v[28:31], a[180:183], v[60:63], v[28:31]\n"
    "v_mul_f32 v103, v103, v101\n"
    "v_cvt_pk_bf16_f32 v89, v102, v103\n"
    "v_mfma_f32_16x16x32_bf16 v[32:35], a[104:107], v[40:43], v[32:35]\n"
    "v_mul_f32 v96, v96, v16\n"
    "v_mul_f32 v97, v97, v17\n"
    "v_mfma_f32_16x16x32_bf16 v[32:35], a[120:123], v[44:47], v[32:35]\n"
    "ds_read_b128 v[80:83], v4 offset:34816\n"
    "v_mfma_f32_16x16x32_bf16 v[32:35], a[136:139], v[48:51], v[32:35]\n"
    "v_exp_f32 v96, -v96\n"
    "v_mfma_f32_16x16x32_bf16 v[32:35], a[152:155], v[52:55], v[32:35]\n"
    "v_mfma_f32_16x16x32_bf16 v[32:35], a[168:171], v[56:59], v[32:35]\n"
    "ds_read_b128 v[84:87], v4 offset:35840\n"
    "v_mfma_f32_16x16x32_bf16 v[32:35], a[184:187], v[60:63], v[32:35]\n"
    "v_exp_f32 v97, -v97\n"
    "v_mfma_f32_16x16x32_bf16 v[36:39], a[108:111], v[40:43], v[36:39]\n"
    "v_mul_f32 v98, v16, v20\n"
    "v_mul_f32 v99, v17, v21\n"
    "v_mfma_f32_16x16x32_bf16 v[36:39], a[124:127], v[44:47], v[36:39]\n"
    "v_add_f32 v96, 1.0, v96\n"
    "v_add_f32 v97, 1.0, v97\n"
    "v_mfma_f32_16x16x32_bf16 v[36:39], a[140:143], v[48:51], v[36:39]\n"
    "v_rcp_f32 v96, v96\n"
    "v_mfma_f32_16x16x32_bf16 v[36:39], a[156:159], v[52:55], v[36:39]\n"
    "v_mfma_f32_16x16x32_bf16 v[36:39], a[172:175], v[56:59], v[36:39]\n"
    "v_rcp_f32 v97, v97\n"
    "v_mfma_f32_16x16x32_bf16 v[36:39], a[188:191], v[60:63], v[36:39]\n"
    "s_waitcnt vmcnt(0) lgkmcnt(0)\n"
    "s_barrier\n"
    "v_mfma_f32_16x16x32_bf16 v[24:27], a[192:195], v[64:67], v[24:27]\n"
    "v_fma_f32 v100, v18, v18, v112\n"
    "v_fma_f32 v101, v19, v19, v112\n"
    "v_mfma_f32_16x16x32_bf16 v[24:27], a[208:211], v[68:71], v[24:27]\n"
    "ds_read_b128 v[40:43], v120 offset:0\n"
    "v_mul_f32 v98, v98, v96\n"
    "v_mfma_f32_16x16x32_bf16 v[24:27], a[224:227], v[72:75], v[24:27]\n"
    "s_add_u32 s77, s71, 0\n"
    "s_add_u32 m0, s70, 0\n"
    "v_mfma_f32_16x16x32_bf16 v[24:27], a[240:243], v[76:79], v[24:27]\n"
    "buffer_load_dwordx4 v4, s[20:23], s77 offen offset:0 sc1 lds\n"
    "v_mul_f32 v99, v99, v97\n"
    "v_mfma_f32_16x16x32_bf16 v[24:27], v[224:227], v[80:83], v[24:27]\n"
    "ds_read_b128 v[44:47], v120 offset:1024\n"
    "v_mfma_f32_16x16x32_bf16 v[24:27], v[240:243], v[84:87], v[24:27]\n"
    "v_cvt_pk_bf16_f32 v90, v98, v99\n"
    "v_mul_f32 v100, v100, v18\n"
    "v_mfma_f32_16x16x32_bf16 v[28:31], a[196:199], v[64:67], v[28:31]\n"
    "v_mul_f32 v101, v101, v19\n"
    "v_mfma_f32_16x16x32_bf16 v[28:31], a[212:215], v[68:71], v[28:31]\n"
    "ds_read_b128 v[48:51], v120 offset:2048\n"
    "v_mfma_f32_16x16x32_bf16 v[28:31], a[228:231], v[72:75], v[28:31]\n"
    "buffer_load_dwordx4 v4, s[20:23], s77 offen offset:1024 sc1 lds\n"
    "v_mfma_f32_16x16x32_bf16 v[28:31], a[244:247], v[76:79], v[28:31]\n"
    "v_exp_f32 v100, -v100\n"
    "v_mfma_f32_16x16x32_bf16 v[28:31], v[228:231], v[80:83], v[28:31]\n"
    "ds_read_b128 v[52:55], v120 offset:3072\n"
    "v_mfma_f32_16x16x32_bf16 v[28:31], v[244:247], v[84:87], v[28:31]\n"
    "v_exp_f32 v101, -v101\n"
    "v_mfma_f32_16x16x32_bf16 v[32:35], a[200:203], v[64:67], v[32:35]\n"
    "v_mul_f32 v102, v18, v22\n"
    "v_mul_f32 v103, v19, v23\n"
    "v_mfma_f32_16x16x32_bf16 v[32:35], a[216:219], v[68:71], v[32:35]\n"
    "ds_read_b128 v[56:59], v120 offset:4096\n"
    "v_add_f32 v100, 1.0, v100\n"
    "v_mfma_f32_16x16x32_bf16 v[32:35], a[232:235], v[72:75], v[32:35]\n"
    "buffer_load_dwordx4 v4, s[20:23], s77 offen offset:2048 sc1 lds\n"
    "v_add_f32 v101, 1.0, v101\n"
    "v_mfma_f32_16x16x32_bf16 v[32:35], a[248:251], v[76:79], v[32:35]\n"
    "v_rcp_f32 v100, v100\n"
    "v_mfma_f32_16x16x32_bf16 v[32:35], v[232:235], v[80:83], v[32:35]\n"
    "ds_read_b128 v[60:63], v120 offset:5120\n"
    "v_mfma_f32_16x16x32_bf16 v[32:35], v[248:251], v[84:87], v[32:35]\n"
    "v_rcp_f32 v101, v101\n"
    "v_mfma_f32_16x16x32_bf16 v[36:39], a[204:207], v[64:67], v[36:39]\n"
    "s_nop 0\n"
    "v_mfma_f32_16x16x32_bf16 v[36:39], a[220:223], v[68:71], v[36:39]\n"
    "buffer_load_dwordx4 v4, s[20:23], s77 offen offset:3072 sc1 lds\n"
    "v_mul_f32 v102, v102, v100\n"
    "v_mfma_f32_16x16x32_bf16 v[36:39], a[236:239], v[72:75], v[36:39]\n"
    "v_mul_f32 v103, v103, v101\n"
    "v_cvt_pk_bf16_f32 v91, v102, v103\n"
    "v_mfma_f32_16x16x32_bf16 v[36:39], a[252:255], v[76:79], v[36:39]\n"
    "v_mfma_f32_16x16x32_bf16 v[36:39], v[236:239], v[80:83], v[36:39]\n"
    "v_mfma_f32_16x16x32_bf16 v[36:39], v[252:255], v[84:87], v[36:39]\n"
    "s_waitcnt lgkmcnt(0)\n"
    "v_mfma_f32_16x16x32_bf16 v[8:11], a[0:3], v[40:43], 0\n"
    "v_fma_f32 v96, v24, v24, v112\n"
    "v_fma_f32 v97, v25, v25, v112\n"
    "v_mfma_f32_16x16x32_bf16 v[8:11], a[16:19], v[44:47], v[8:11]\n"
    "ds_read_b128 v[64:67], v120 offset:6144\n"
    "v_mul_f32 v96, v96, v24\n"
    "v_mfma_f32_16x16x32_bf16 v[8:11], a[32:35], v[48:51], v[8:11]\n"
    "s_add_u32 s77, s71, 4096\n"
    "s_add_u32 m0, s70, 4096\n"
    "v_mfma_f32_16x16x32_bf16 v[8:11], a[48:51], v[52:55], v[8:11]\n"
    "buffer_load_dwordx4 v4, s[20:23], s77 offen offset:0 sc1 lds\n"
    "v_mul_f32 v97, v97, v25\n"
    "v_mfma_f32_16x16x32_bf16 v[8:11], a[64:67], v[56:59], v[8:11]\n"
    "ds_read_b128 v[68:71], v120 offset:7168\n"
    "v_mfma_f32_16x16x32_bf16 v[8:11], a[80:83], v[60:63], v[8:11]\n"
    "v_exp_f32 v96, -v96\n"
    "v_mfma_f32_16x16x32_bf16 v[12:15], a[4:7], v[40:43], 0\n"
    "v_exp_f32 v97, -v97\n"
    "v_mfma_f32_16x16x32_bf16 v[12:15], a[20:23], v[44:47], v[12:15]\n"
    "ds_read_b128 v[72:75], v120 offset:8192\n"
    "v_mul_f32 v98, v24, v28\n"
    "v_mfma_f32_16x16x32_bf16 v[12:15], a[36:39], v[48:51], v[12:15]\n"
    "buffer_load_dwordx4 v4, s[20:23], s77 offen offset:1024 sc1 lds\n"
    "v_mul_f32 v99, v25, v29\n"
    "v_mfma_f32_16x16x32_bf16 v[12:15], a[52:55], v[52:55], v[12:15]\n"
    "v_add_f32 v96, 1.0, v96\n"
    "v_add_f32 v97, 1.0, v97\n"
    "v_mfma_f32_16x16x32_bf16 v[12:15], a[68:71], v[56:59], v[12:15]\n"
    "ds_read_b128 v[76:79], v120 offset:9216\n"
    "v_mfma_f32_16x16x32_bf16 v[12:15], a[84:87], v[60:63], v[12:15]\n"
    "v_rcp_f32 v96, v96\n"
    "v_mfma_f32_16x16x32_bf16 v[16:19], a[8:11], v[40:43], 0\n"
    "v_rcp_f32 v97, v97\n"
    "v_mfma_f32_16x16x32_bf16 v[16:19], a[24:27], v[44:47], v[16:19]\n"
    "ds_read_b128 v[80:83], v120 offset:10240\n"
    "v_fma_f32 v100, v26, v26, v112\n"
    "v_mfma_f32_16x16x32_bf16 v[16:19], a[40:43], v[48:51], v[16:19]\n"
    "buffer_load_dwordx4 v4, s[20:23], s77 offen offset:2048 sc1 lds\n"
    "v_fma_f32 v101, v27, v27, v112\n"
    "v_mfma_f32_16x16x32_bf16 v[16:19], a[56:59], v[52:55], v[16:19]\n"
    "v_mul_f32 v98, v98, v96\n"
    "v_mul_f32 v99, v99, v97\n"
    "v_mfma_f32_16x16x32_bf16 v[16:19], a[72:75], v[56:59], v[16:19]\n"
    "ds_read_b128 v[84:87], v120 offset:11264\n"
    "v_cvt_pk_bf16_f32 v92, v98, v99\n"
    "v_mfma_f32_16x16x32_bf16 v[16:19], a[88:91], v[60:63], v[16:19]\n"
    "v_mul_f32 v100, v100, v26\n"
    "v_mul_f32 v101, v101, v27\n"
    "v_mfma_f32_16x16x32_bf16 v[20:23], a[12:15], v[40:43], 0\n"
    "v_exp_f32 v100, -v100\n"
    "v_mfma_f32_16x16x32_bf16 v[20:23], a[28:31], v[44:47], v[20:23]\n"
    "buffer_load_dwordx4 v4, s[20:23], s77 offen offset:3072 sc1 lds\n"
    "s_add_u32 s77, s71, 8192\n"
    "v_mfma_f32_16x16x32_bf16 v[20:23], a[44:47], v[48:51], v[20:23]\n"
    "s_add_u32 m0, s70, 8192\n"
    "v_mfma_f32_16x16x32_bf16 v[20:23], a[60:63], v[52:55], v[20:23]\n"
    "v_exp_f32 v101, -v101\n"
    "v_mfma_f32_16x16x32_bf16 v[20:23], a[76:79], v[56:59], v[20:23]\n"
    "v_mul_f32 v102, v26, v30\n"
    "v_mul_f32 v103, v27, v31\n"
    "v_mfma_f32_16x16x32_bf16 v[20:23], a[92:95], v[60:63], v[20:23]\n"
    "v_add_f32 v100, 1.0, v100\n"
    "s_waitcnt lgkmcnt(0)\n"
    "v_mfma_f32_16x16x32_bf16 v[8:11], a[96:99], v[64:67], v[8:11]\n"
    "buffer_load_dwordx4 v4, s[20:23], s77 offen offset:0 sc1 lds\n"
    "v_readlane_b32 s71, v214, 3\n"
    "v_mfma_f32_16x16x32_bf16 v[8:11], a[112:115], v[68:71], v[8:11]\n"
    "ds_read_b128 v[40:43], v120 offset:12288\n"
    "v_add_f32 v101, 1.0, v101\n"
    "v_mfma_f32_16x16x32_bf16 v[8:11], a[128:131], v[72:75], v[8:11]\n"
    "v_rcp_f32 v100, v100\n"
    "v_mfma_f32_16x16x32_bf16 v[8:11], a[144:147], v[76:79], v[8:11]\n"
    "v_rcp_f32 v101, v101\n"
    "v_mfma_f32_16x16x32_bf16 v[8:11], a[160:163], v[80:83], v[8:11]\n"
    "ds_read_b128 v[44:47], v120 offset:13312\n"
    "v_fma_f32 v96, v32, v32, v112\n"
    "v_mfma_f32_16x16x32_bf16 v[8:11], a[176:179], v[84:87], v[8:11]\n"
    "v_fma_f32 v97, v33, v33, v112\n"
    "v_mul_f32 v102, v102, v100\n"
    "v_mfma_f32_16x16x32_bf16 v[12:15], a[100:103], v[64:67], v[12:15]\n"
    "buffer_store_dwordx4 v[88:91], v4, s[24:27], s74 offen sc1\n"
    "v_readlane_b32 s74, v214, 18\n"
    "v_mfma_f32_16x16x32_bf16 v[12:15], a[116:119], v[68:71], v[12:15]\n"
    "ds_read_b128 v[48:51], v120 offset:14336\n"
    "s_add_u32 s78, s86, 0\n"
    "v_mfma_f32_16x16x32_bf16 v[12:15], a[132:135], v[72:75], v[12:15]\n"
    "s_add_u32 m0, s84, 0\n"
    "v_mul_f32 v103, v103, v101\n"
    "v_mfma_f32_16x16x32_bf16 v[12:15], a[148:151], v[76:79], v[12:15]\n"
    "buffer_load_dwordx4 v111, s[28:31], s78 offen lds\n"
    "s_add_u32 s78, s86, 9216\n"
    "v_mfma_f32_16x16x32_bf16 v[12:15], a[164:167], v[80:83], v[12:15]\n"
    "ds_read_b128 v[52:55], v120 offset:15360\n"
    "s_add_u32 m0, s84, 1024\n"
    "v_mfma_f32_16x16x32_bf16 v[12:15], a[180:183], v[84:87], v[12:15]\n"
    "v_cvt_pk_bf16_f32 v93, v102, v103\n"
    "v_mul_f32 v96, v96, v32\n"
    "v_mfma_f32_16x16x32_bf16 v[16:19], a[104:107], v[64:67], v[16:19]\n"
    "v_mul_f32 v97, v97, v33\n"
    "v_mfma_f32_16x16x32_bf16 v[16:19], a[120:123], v[68:71], v[16:19]\n"
    "ds_read_b128 v[56:59], v120 offset:16384\n"
    "v_mfma_f32_16x16x32_bf16 v[16:19], a[136:139], v[72:75], v[16:19]\n"
    "v_exp_f32 v96, -v96\n"
    "v_mfma_f32_16x16x32_bf16 v[16:19], a[152:155], v[76:79], v[16:19]\n"
    "buffer_load_dwordx4 v111, s[28:31], s78 offen lds\n"
    "s_add_u32 s78, s86, 18432\n"
    "v_mfma_f32_16x16x32_bf16 v[16:19], a[168:171], v[80:83], v[16:19]\n"
    "ds_read_b128 v[60:63], v120 offset:17408\n"
    "s_add_u32 m0, s84, 2048\n"
    "v_mfma_f32_16x16x32_bf16 v[16:19], a[184:187], v[84:87], v[16:19]\n"
    "v_exp_f32 v97, -v97\n"
    "v_mfma_f32_16x16x32_bf16 v[20:23], a[108:111], v[64:67], v[20:23]\n"
    "v_mul_f32 v98, v32, v36\n"
    "v_mul_f32 v99, v33, v37\n"
    "v_mfma_f32_16x16x32_bf16 v[20:23], a[124:127], v[68:71], v[20:23]\n"
    "v_add_f32 v96, 1.0, v96\n"
    "v_add_f32 v97, 1.0, v97\n"
    "v_mfma_f32_16x16x32_bf16 v[20:23], a[140:143], v[72:75], v[20:23]\n"
    "v_rcp_f32 v96, v96\n"
    "v_mfma_f32_16x16x32_bf16 v[20:23], a[156:159], v[76:79], v[20:23]\n"
    "buffer_load_dwordx4 v111, s[28:31], s78 offen lds\n"
    "s_add_u32 s78, s86, 27648\n"
    "v_mfma_f32_16x16x32_bf16 v[20:23], a[172:175], v[80:83], v[20:23]\n"
    "s_add_u32 m0, s84, 3072\n"
    "v_mfma_f32_16x16x32_bf16 v[20:23], a[188:191], v[84:87], v[20:23]\n"
    "s_waitcnt lgkmcnt(0)\n"
    "v_mfma_f32_16x16x32_bf16 v[8:11], a[192:195], v[40:43], v[8:11]\n"
    "v_rcp_f32 v97, v97\n"
    "v_mfma_f32_16x16x32_bf16 v[8:11], a[208:211], v[44:47], v[8:11]\n"
    "ds_read_b128 v[64:67], v120 offset:18432\n"
    "v_fma_f32 v100, v34, v34, v112\n"
    "v_mfma_f32_16x16x32_bf16 v[8:11], a[224:227], v[48:51], v[8:11]\n"
    "v_fma_f32 v101, v35, v35, v112\n"
    "v_mul_f32 v98, v98, v96\n"
    "v_mfma_f32_16x16x32_bf16 v[8:11], a[240:243], v[52:55], v[8:11]\n"
    "buffer_load_dwordx4 v111, s[28:31], s78 offen lds\n"
    "s_add_u32 s78, s87, 0\n"
    "v_mfma_f32_16x16x32_bf16 v[8:11], v[224:227], v[56:59], v[8:11]\n"
    "ds_read_b128 v[68:71], v120 offset:19456\n"
    "s_add_u32 m0, s84, 4096\n"
    "v_mfma_f32_16x16x32_bf16 v[8:11], v[240:243], v[60:63], v[8:11]\n"
    "v_mul_f32 v99, v99, v97\n"
    "v_cvt_pk_bf16_f32 v94, v98, v99\n"
    "v_mfma_f32_16x16x32_bf16 v[12:15], a[196:199], v[40:43], v[12:15]\n"
    "v_mul_f32 v100, v100, v34\n"
    "v_mul_f32 v101, v101, v35\n"
    "v_mfma_f32_16x16x32_bf16 v[12:15], a[212:215], v[44:47], v[12:15]\n"
    "ds_read_b128 v[72:75], v120 offset:20480\n"
    "v_mfma_f32_16x16x32_bf16 v[12:15], a[228:231], v[48:51], v[12:15]\n"
    "v_exp_f32 v100, -v100\n"
    "v_mfma_f32_16x16x32_bf16 v[12:15], a[244:247], v[52:55], v[12:15]\n"
    "buffer_load_dwordx4 v111, s[28:31], s78 offen lds\n"
    "s_add_u32 s78, s87, 9216\n"
    "v_mfma_f32_16x16x32_bf16 v[12:15], v[228:231], v[56:59], v[12:15]\n"
    "ds_read_b128 v[76:79], v120 offset:21504\n"
    "s_add_u32 m0, s84, 5120\n"
    "v_mfma_f32_16x16x32_bf16 v[12:15], v[244:247], v[60:63], v[12:15]\n"
    "v_exp_f32 v101, -v101\n"
    "v_mfma_f32_16x16x32_bf16 v[16:19], a[200:203], v[40:43], v[16:19]\n"
    "v_mul_f32 v102, v34, v38\n"
    "v_mul_f32 v103, v35, v39\n"
    "v_mfma_f32_16x16x32_bf16 v[16:19], a[216:219], v[44:47], v[16:19]\n"
    "ds_read_b128 v[80:83], v120 offset:22528\n"
    "v_add_f32 v100, 1.0, v100\n"
    "v_mfma_f32_16x16x32_bf16 v[16:19], a[232:235], v[48:51], v[16:19]\n"
    "v_add_f32 v101, 1.0, v101\n"
    "v_mfma_f32_16x16x32_bf16 v[16:19], a[248:251], v[52:55], v[16:19]\n"
    "buffer_load_dwordx4 v111, s[28:31], s78 offen lds\n"
    "s_add_u32 s78, s87, 18432\n"
    "v_mfma_f32_16x16x32_bf16 v[16:19], v[232:235], v[56:59], v[16:19]\n"
    "ds_read_b128 v[84:87], v120 offset:23552\n"
    "s_add_u32 m0, s84, 6144\n"
    "v_mfma_f32_16x16x32_bf16 v[16:19], v[248:251], v[60:63], v[16:19]\n"
    "v_rcp_f32 v100, v100\n"
    "v_mfma_f32_16x16x32_bf16 v[20:23], a[204:207], v[40:43], v[20:23]\n"
    "v_rcp_f32 v101, v101\n"
    "v_mfma_f32_16x16x32_bf16 v[20:23], a[220:223], v[44:47], v[20:23]\n"
    "s_nop 0\n"
    "v_mul_f32 v102, v102, v100\n"
    "v_mfma_f32_16x16x32_bf16 v[20:23], a[236:239], v[48:51], v[20:23]\n"
    "v_mul_f32 v103, v103, v101\n"
    "v_cvt_pk_bf16_f32 v95, v102, v103\n"
    "v_mfma_f32_16x16x32_bf16 v[20:23], a[252:255], v[52:55], v[20:23]\n"
    "buffer_load_dwordx4 v111, s[28:31], s78 offen lds\n"
    "s_add_u32 s78, s87, 27648\n"
    "v_mfma_f32_16x16x32_bf16 v[20:23], v[236:239], v[56:59], v[20:23]\n"
    "s_add_u32 m0, s84, 7168\n"
    "v_mfma_f32_16x16x32_bf16 v[20:23], v[252:255], v[60:63], v[20:23]\n"
    "buffer_store_dwordx4 v[92:95], v4, s[24:27], s74 offen sc1\n"
    "s_waitcnt lgkmcnt(0)\n"
    "v_mfma_f32_16x16x32_bf16 v[24:27], a[0:3], v[64:67], 0\n"
    "v_readlane_b32 s74, v214, 19\n"
    "v_fma_f32 v96, v8, v8, v112\n"
    "v_mfma_f32_16x16x32_bf16 v[24:27], a[16:19], v[68:71], v[24:27]\n"
    "ds_read_b128 v[40:43], v120 offset:24576\n"
    "v_fma_f32 v97, v9, v9, v112\n"
    "v_mfma_f32_16x16x32_bf16 v[24:27], a[32:35], v[72:75], v[24:27]\n"
    "v_mul_f32 v96, v96, v8\n"
    "v_mul_f32 v97, v97, v9\n"
    "v_mfma_f32_16x16x32_bf16 v[24:27], a[48:51], v[76:79], v[24:27]\n"
    "buffer_load_dwordx4 v111, s[28:31], s78 offen lds\n"
    "v_mfma_f32_16x16x32_bf16 v[24:27], a[64:67], v[80:83], v[24:27]\n"
    "ds_read_b128 v[44:47], v120 offset:25600\n"
    "v_mfma_f32_16x16x32_bf16 v[24:27], a[80:83], v[84:87], v[24:27]\n"
    "v_exp_f32 v96, -v96\n"
    "v_mfma_f32_16x16x32_bf16 v[28:31], a[4:7], v[64:67], 0\n"
    "v_exp_f32 v97, -v97\n"
    "v_mfma_f32_16x16x32_bf16 v[28:31], a[20:23], v[68:71], v[28:31]\n"
    "ds_read_b128 v[48:51], v120 offset:26624\n"
    "v_mul_f32 v98, v8, v12\n"
    "v_mfma_f32_16x16x32_bf16 v[28:31], a[36:39], v[72:75], v[28:31]\n"
    "v_mul_f32 v99, v9, v13\n"
    "v_add_f32 v96, 1.0, v96\n"
    "v_mfma_f32_16x16x32_bf16 v[28:31], a[52:55], v[76:79], v[28:31]\n"
    "v_add_f32 v97, 1.0, v97\n"
    "v_mfma_f32_16x16x32_bf16 v[28:31], a[68:71], v[80:83], v[28:31]\n"
    "ds_read_b128 v[52:55], v120 offset:27648\n"
    "v_mfma_f32_16x16x32_bf16 v[28:31], a[84:87], v[84:87], v[28:31]\n"
    "v_rcp_f32 v96, v96\n"
    "v_mfma_f32_16x16x32_bf16 v[32:35], a[8:11], v[64:67], 0\n"
    "v_rcp_f32 v97, v97\n"
    "v_mfma_f32_16x16x32_bf16 v[32:35], a[24:27], v[68:71], v[32:35]\n"
    "ds_read_b128 v[56:59], v120 offset:28672\n"
    "v_fma_f32 v100, v10, v10, v112\n"
    "v_mfma_f32_16x16x32_bf16 v[32:35], a[40:43], v[72:75], v[32:35]\n"
    "v_fma_f32 v101, v11, v11, v112\n"
    "v_mfma_f32_16x16x32_bf16 v[32:35], a[56:59], v[76:79], v[32:35]\n"
    "v_mul_f32 v98, v98, v96\n"
    "v_mul_f32 v99, v99, v97\n"
    "v_mfma_f32_16x16x32_bf16 v[32:35], a[72:75], v[80:83], v[32:35]\n"
    "ds_read_b128 v[60:63], v120 offset:29696\n"
    "v_mfma_f32_16x16x32_bf16 v[32:35], a[88:91], v[84:87], v[32:35]\n"
    "v_cvt_pk_bf16_f32 v88, v98, v99\n"
    "v_mul_f32 v100, v100, v10\n"
    "v_mfma_f32_16x16x32_bf16 v[36:39], a[12:15], v[64:67], 0\n"
    "v_mul_f32 v101, v101, v11\n"
    "v_mfma_f32_16x16x32_bf16 v[36:39], a[28:31], v[68:71], v[36:39]\n"
    "v_mfma_f32_16x16x32_bf16 v[36:39], a[44:47], v[72:75], v[36:39]\n"
    "v_exp_f32 v100, -v100\n"
    "v_mfma_f32_16x16x32_bf16 v[36:39], a[60:63], v[76:79], v[36:39]\n"
    "v_exp_f32 v101, -v101\n"
    "v_mfma_f32_16x16x32_bf16 v[36:39], a[76:79], v[80:83], v[36:39]\n"
    "v_mul_f32 v102, v10, v14\n"
    "v_mfma_f32_16x16x32_bf16 v[36:39], a[92:95], v[84:87], v[36:39]\n"
    "v_mul_f32 v103, v11, v15\n"
    "s_waitcnt lgkmcnt(0)\n"
    "v_mfma_f32_16x16x32_bf16 v[24:27], a[96:99], v[40:43], v[24:27]\n"
    "v_add_f32 v100, 1.0, v100\n"
    "v_mfma_f32_16x16x32_bf16 v[24:27], a[112:115], v[44:47], v[24:27]\n"
    "ds_read_b128 v[64:67], v120 offset:30720\n"
    "v_add_f32 v101, 1.0, v101\n"
    "v_mfma_f32_16x16x32_bf16 v[24:27], a[128:131], v[48:51], v[24:27]\n"
    "v_mfma_f32_16x16x32_bf16 v[24:27], a[144:147], v[52:55], v[24:27]\n"
    "v_rcp_f32 v100, v100\n"
    "v_mfma_f32_16x16x32_bf16 v[24:27], a[160:163], v[56:59], v[24:27]\n"
    "ds_read_b128 v[68:71], v120 offset:31744\n"
    "v_mfma_f32_16x16x32_bf16 v[24:27], a[176:179], v[60:63], v[24:27]\n"
    "v_rcp_f32 v101, v101\n"
    "v_mfma_f32_16x16x32_bf16 v[28:31], a[100:103], v[40:43], v[28:31]\n"
    "v_fma_f32 v96, v16, v16, v112\n"
    "v_fma_f32 v97, v17, v17, v112\n"
    "v_mfma_f32_16x16x32_bf16 v[28:31], a[116:119], v[44:47], v[28:31]\n"
    "ds_read_b128 v[72:75], v120 offset:32768\n"
    "v_mul_f32 v102, v102, v100\n"
    "v_mfma_f32_16x16x32_bf16 v[28:31], a[132:135], v[48:51], v[28:31]\n"
    "v_mul_f32 v103, v103, v101\n"
    "v_mfma_f32_16x16x32_bf16 v[28:31], a[148:151], v[52:55], v[28:31]\n"
    "v_cvt_pk_bf16_f32 v89, v102, v103\n"
    "v_mul_f32 v96, v96, v16\n"
    "v_mfma_f32_16x16x32_bf16 v[28:31], a[164:167], v[56:59], v[28:31]\n"
    "ds_read_b128 v[76:79], v120 offset:33792\n"
    "v_mfma_f32_16x16x32_bf16 v[28:31], a[180:183], v[60:63], v[28:31]\n"
    "v_mul_f32 v97, v97, v17\n"
    "v_mfma_f32_16x16x32_bf16 v[32:35], a[104:107], v[40:43], v[32:35]\n"
    "v_exp_f32 v96, -v96\n"
    "v_mfma_f32_16x16x32_bf16 v[32:35], a[120:123], v[44:47], v[32:35]\n"
    "ds_read_b128 v[80:83], v120 offset:34816\n"
    "v_mfma_f32_16x16x32_bf16 v[32:35], a[136:139], v[48:51], v[32:35]\n"
    "v_exp_f32 v97, -v97\n"
    "v_mfma_f32_16x16x32_bf16 v[32:35], a[152:155], v[52:55], v[32:35]\n"
    "v_mul_f32 v98, v16, v20\n"
    "v_mfma_f32_16x16x32_bf16 v[32:35], a[168:171], v[56:59], v[32:35]\n"
    "ds_read_b128 v[84:87], v120 offset:35840\n"
    "v_mul_f32 v99, v17, v21\n"
    "v_mfma_f32_16x16x32_bf16 v[32:35], a[184:187], v[60:63], v[32:35]\n"
    "v_add_f32 v96, 1.0, v96\n"
    "v_add_f32 v97, 1.0, v97\n"
    "v_mfma_f32_16x16x32_bf16 v[36:39], a[108:111], v[40:43], v[36:39]\n"
    "v_mfma_f32_16x16x32_bf16 v[36:39], a[124:127], v[44:47], v[36:39]\n"
    "v_rcp_f32 v96, v96\n"
    "v_mfma_f32_16x16x32_bf16 v[36:39], a[140:143], v[48:51], v[36:39]\n"
    "v_rcp_f32 v97, v97\n"
    "v_mfma_f32_16x16x32_bf16 v[36:39], a[156:159], v[52:55], v[36:39]\n"
    "v_fma_f32 v100, v18, v18, v112\n"
    "v_mfma_f32_16x16x32_bf16 v[36:39], a[172:175], v[56:59], v[36:39]\n"
    "v_fma_f32 v101, v19, v19, v112\n"
    "v_mfma_f32_16x16x32_bf16 v[36:39], a[188:191], v[60:63], v[36:39]\n"
    "s_waitcnt vmcnt(10) lgkmcnt(0)\n"
    "s_barrier\n"
    "v_mfma_f32_16x16x32_bf16 v[24:27], a[192:195], v[64:67], v[24:27]\n"
    "v_mul_f32 v98, v98, v96\n"
    "v_mul_f32 v99, v99, v97\n"
    "v_mfma_f32_16x16x32_bf16 v[24:27], a[208:211], v[68:71], v[24:27]\n"
    "ds_read_b128 v[40:43], v4 offset:0\n"
    "v_mfma_f32_16x16x32_bf16 v[24:27], a[224:227], v[72:75], v[24:27]\n"
    "s_add_u32 s77, s71, 0\n"
    "s_add_u32 m0, s70, 36864\n"
    "v_mfma_f32_16x16x32_bf16 v[24:27], a[240:243], v[76:79], v[24:27]\n"
    "buffer_load_dwordx4 v4, s[20:23], s77 offen offset:0 sc1 lds\n"
    "v_cvt_pk_bf16_f32 v90, v98, v99\n"
    "v_mfma_f32_16x16x32_bf16 v[24:27], v[224:227], v[80:83], v[24:27]\n"
    "ds_read_b128 v[44:47], v4 offset:1024\n"
    "v_mul_f32 v100, v100, v18\n"
    "v_mfma_f32_16x16x32_bf16 v[24:27], v[240:243], v[84:87], v[24:27]\n"
    "v_mul_f32 v101, v101, v19\n"
    "v_mfma_f32_16x16x32_bf16 v[28:31], a[196:199], v[64:67], v[28:31]\n"
    "v_mfma_f32_16x16x32_bf16 v[28:31], a[212:215], v[68:71], v[28:31]\n"
    "ds_read_b128 v[48:51], v4 offset:2048\n"
    "v_mfma_f32_16x16x32_bf16 v[28:31], a[228:231], v[72:75], v[28:31]\n"
    "buffer_load_dwordx4 v4, s[20:23], s77 offen offset:1024 sc1 lds\n"
    "v_mfma_f32_16x16x32_bf16 v[28:31], a[244:247], v[76:79], v[28:31]\n"
    "v_exp_f32 v100, -v100\n"
    "v_mfma_f32_16x16x32_bf16 v[28:31], v[228:231], v[80:83], v[28:31]\n"
    "ds_read_b128 v[52:55], v4 offset:3072\n"
    "v_mfma_f32_16x16x32_bf16 v[28:31], v[244:247], v[84:87], v[28:31]\n"
    "v_exp_f32 v101, -v101\n"
    "v_mfma_f32_16x16x32_bf16 v[32:35], a[200:203], v[64:67], v[32:35]\n"
    "v_mul_f32 v102, v18, v22\n"
    "v_mul_f32 v103, v19, v23\n"
    "v_mfma_f32_16x16x32_bf16 v[32:35], a[216:219], v[68:71], v[32:35]\n"
    "ds_read_b128 v[56:59], v4 offset:4096\n"
    "v_add_f32 v100, 1.0, v100\n"
    "v_mfma_f32_16x16x32_bf16 v[32:35], a[232:235], v[72:75], v[32:35]\n"
    "buffer_load_dwordx4 v4, s[20:23], s77 offen offset:2048 sc1 lds\n"
    "v_add_f32 v101, 1.0, v101\n"
    "v_mfma_f32_16x16x32_bf16 v[32:35], a[248:251], v[76:79], v[32:35]\n"
    "v_rcp_f32 v100, v100\n"
    "v_mfma_f32_16x16x32_bf16 v[32:35], v[232:235], v[80:83], v[32:35]\n"
    "ds_read_b128 v[60:63], v4 offset:5120\n"
    "v_mfma_f32_16x16x32_bf16 v[32:35], v[248:251], v[84:87], v[32:35]\n"
    "v_rcp_f32 v101, v101\n"
    "v_mfma_f32_16x16x32_bf16 v[36:39], a[204:207], v[64:67], v[36:39]\n"
    "s_nop 0\n"
    "v_mul_f32 v102, v102, v100\n"
    "v_mfma_f32_16x16x32_bf16 v[36:39], a[220:223], v[68:71], v[36:39]\n"
    "buffer_load_dwordx4 v4, s[20:23], s77 offen offset:3072 sc1 lds\n"
    "v_mfma_f32_16x16x32_bf16 v[36:39], a[236:239], v[72:75], v[36:39]\n"
    "v_mul_f32 v103, v103, v101\n"
    "v_cvt_pk_bf16_f32 v91, v102, v103\n"
    "v_mfma_f32_16x16x32_bf16 v[36:39], a[252:255], v[76:79], v[36:39]\n"
    "v_mfma_f32_16x16x32_bf16 v[36:39], v[236:239], v[80:83], v[36:39]\n"
    "v_mfma_f32_16x16x32_bf16 v[36:39], v[252:255], v[84:87], v[36:39]\n"
    "s_waitcnt lgkmcnt(0)\n"
    "v_mfma_f32_16x16x32_bf16 v[8:11], a[0:3], v[40:43], 0\n"
    "v_fma_f32 v96, v24, v24, v112\n"
    "v_fma_f32 v97, v25, v25, v112\n"
    "v_mfma_f32_16x16x32_bf16 v[8:11], a[16:19], v[44:47], v[8:11]\n"
    "ds_read_b128 v[64:67], v4 offset:6144\n"
    "v_mul_f32 v96, v96, v24\n"
    "v_mfma_f32_16x16x32_bf16 v[8:11], a[32:35], v[48:51], v[8:11]\n"
    "s_add_u32 s77, s71, 4096\n"
    "s_add_u32 m0, s70, 40960\n"
    "v_mfma_f32_16x16x32_bf16 v[8:11], a[48:51], v[52:55], v[8:11]\n"
    "buffer_load_dwordx4 v4, s[20:23], s77 offen offset:0 sc1 lds\n"
    "v_mul_f32 v97, v97, v25\n"
    "v_mfma_f32_16x16x32_bf16 v[8:11], a[64:67], v[56:59], v[8:11]\n"
    "ds_read_b128 v[68:71], v4 offset:7168\n"
    "v_mfma_f32_16x16x32_bf16 v[8:11], a[80:83], v[60:63], v[8:11]\n"
    "v_exp_f32 v96, -v96\n"
    "v_mfma_f32_16x16x32_bf16 v[12:15], a[4:7], v[40:43], 0\n"
    "v_exp_f32 v97, -v97\n"
    "v_mfma_f32_16x16x32_bf16 v[12:15], a[20:23], v[44:47], v[12:15]\n"
    "ds_read_b128 v[72:75], v4 offset:8192\n"
    "v_mul_f32 v98, v24, v28\n"
    "v_mfma_f32_16x16x32_bf16 v[12:15], a[36:39], v[48:51], v[12:15]\n"
    "buffer_load_dwordx4 v4, s[20:23], s77 offen offset:1024 sc1 lds\n"
    "v_mul_f32 v99, v25, v29\n"
    "v_mfma_f32_16x16x32_bf16 v[12:15], a[52:55], v[52:55], v[12:15]\n"
    "v_add_f32 v96, 1.0, v96\n"
    "v_add_f32 v97, 1.0, v97\n"
    "v_mfma_f32_16x16x32_bf16 v[12:15], a[68:71], v[56:59], v[12:15]\n"
    "ds_read_b128 v[76:79], v4 offset:9216\n"
    "v_mfma_f32_16x16x32_bf16 v[12:15], a[84:87], v[60:63], v[12:15]\n"
    "v_rcp_f32 v96, v96\n"
    "v_mfma_f32_16x16x32_bf16 v[16:19], a[8:11], v[40:43], 0\n"
    "v_rcp_f32 v97, v97\n"
    "v_mfma_f32_16x16x32_bf16 v[16:19], a[24:27], v[44:47], v[16:19]\n"
    "ds_read_b128 v[80:83], v4 offset:10240\n"
    "v_fma_f32 v100, v26, v26, v112\n"
    "v_mfma_f32_16x16x32_bf16 v[16:19], a[40:43], v[48:51], v[16:19]\n"
    "buffer_load_dwordx4 v4, s[20:23], s77 offen offset:2048 sc1 lds\n"
    "v_fma_f32 v101, v27, v27, v112\n"
    "v_mfma_f32_16x16x32_bf16 v[16:19], a[56:59], v[52:55], v[16:19]\n"
    "v_mul_f32 v98, v98, v96\n"
    "v_mul_f32 v99, v99, v97\n"
    "v_mfma_f32_16x16x32_bf16 v[16:19], a[72:75], v[56:59], v[16:19]\n"
    "ds_read_b128 v[84:87], v4 offset:11264\n"
    "v_mfma_f32_16x16x32_bf16 v[16:19], a[88:91], v[60:63], v[16:19]\n"
    "v_cvt_pk_bf16_f32 v92, v98, v99\n"
    "v_mul_f32 v100, v100, v26\n"
    "v_mfma_f32_16x16x32_bf16 v[20:23], a[12:15], v[40:43], 0\n"
    "v_mul_f32 v101, v101, v27\n"
    "v_mfma_f32_16x16x32_bf16 v[20:23], a[28:31], v[44:47], v[20:23]\n"
    "buffer_load_dwordx4 v4, s[20:23], s77 offen offset:3072 sc1 lds\n"
    "s_add_u32 s77, s71, 8192\n"
    "v_mfma_f32_16x16x32_bf16 v[20:23], a[44:47], v[48:51], v[20:23]\n"
    "s_add_u32 m0, s70, 45056\n"
    "v_mfma_f32_16x16x32_bf16 v[20:23], a[60:63], v[52:55], v[20:23]\n"
    "v_exp_f32 v100, -v100\n"
    "v_mfma_f32_16x16x32_bf16 v[20:23], a[76:79], v[56:59], v[20:23]\n"
    "v_exp_f32 v101, -v101\n"
    "v_mfma_f32_16x16x32_bf16 v[20:23], a[92:95], v[60:63], v[20:23]\n"
    "v_mul_f32 v102, v26, v30\n"
    "s_waitcnt lgkmcnt(0)\n"
    "v_mfma_f32_16x16x32_bf16 v[8:11], a[96:99], v[64:67], v[8:11]\n"
    "buffer_load_dwordx4 v4, s[20:23], s77 offen offset:0 sc1 lds\n"
    "v_readlane_b32 s71, v214, 4\n"
    "v_mfma_f32_16x16x32_bf16 v[8:11], a[112:115], v[68:71], v[8:11]\n"
    "ds_read_b128 v[40:43], v4 offset:12288\n"
    "v_mul_f32 v103, v27, v31\n"
    "v_mfma_f32_16x16x32_bf16 v[8:11], a[128:131], v[72:75], v[8:11]\n"
    "v_add_f32 v100, 1.0, v100\n"
    "v_add_f32 v101, 1.0, v101\n"
    "v_mfma_f32_16x16x32_bf16 v[8:11], a[144:147], v[76:79], v[8:11]\n"
    "v_mfma_f32_16x16x32_bf16 v[8:11], a[160:163], v[80:83], v[8:11]\n"
    "ds_read_b128 v[44:47], v4 offset:13312\n"
    "v_mfma_f32_16x16x32_bf16 v[8:11], a[176:179], v[84:87], v[8:11]\n"
    "v_rcp_f32 v100, v100\n"
    "v_mfma_f32_16x16x32_bf16 v[12:15], a[100:103], v[64:67], v[12:15]\n"
    "buffer_store_dwordx4 v[88:91], v4, s[24:27], s74 offen sc1\n"
    "LS_5:\n"
    "s_cbranch_execz LS_9\n"
    "v_readlane_b32 s74, v214, 20\n"
    "v_mfma_f32_16x16x32_bf16 v[12:15], a[116:119], v[68:71], v[12:15]\n"
    "ds_read_b128 v[48:51], v4 offset:14336\n"
    "v_mfma_f32_16x16x32_bf16 v[12:15], a[132:135], v[72:75], v[12:15]\n"
    "v_rcp_f32 v101, v101\n"
    "v_mfma_f32_16x16x32_bf16 v[12:15], a[148:151], v[76:79], v[12:15]\n"
    "v_fma_f32 v96, v32, v32, v112\n"
    "v_fma_f32 v97, v33, v33, v112\n"
    "v_mfma_f32_16x16x32_bf16 v[12:15], a[164:167], v[80:83], v[12:15]\n"
    "ds_read_b128 v[52:55], v4 offset:15360\n"
    "v_mul_f32 v102, v102, v100\n"
    "v_mfma_f32_16x16x32_bf16 v[12:15], a[180:183], v[84:87], v[12:15]\n"
    "v_mul_f32 v103, v103, v101\n"
    "v_cvt_pk_bf16_f32 v93, v102, v103\n"
    "v_mfma_f32_16x16x32_bf16 v[16:19], a[104:107], v[64:67], v[16:19]\n"
    "s_waitcnt vmcnt(10)\n"
    "v_mul_f32 v96, v96, v32\n"
    "v_mfma_f32_16x16x32_bf16 v[16:19], a[120:123], v[68:71], v[16:19]\n"
    "ds_read_b128 v[56:59], v4 offset:16384\n"
    "v_mul_f32 v97, v97, v33\n"
    "v_mfma_f32_16x16x32_bf16 v[16:19], a[136:139], v[72:75], v[16:19]\n"
    "ds_read_b64_tr_b16 v[128:129], v114 offset:0\n"
    "v_mfma_f32_16x16x32_bf16 v[16:19], a[152:155], v[76:79], v[16:19]\n"
    "v_exp_f32 v96, -v96\n"
    "v_mfma_f32_16x16x32_bf16 v[16:19], a[168:171], v[80:83], v[16:19]\n"
    "ds_read_b128 v[60:63], v4 offset:17408\n"
    "v_mfma_f32_16x16x32_bf16 v[16:19], a[184:187], v[84:87], v[16:19]\n"
    "ds_read_b64_tr_b16 v[130:131], v114 offset:2048\n"
    "v_mfma_f32_16x16x32_bf16 v[20:23], a[108:111], v[64:67], v[20:23]\n"
    "v_exp_f32 v97, -v97\n"
    "v_mfma_f32_16x16x32_bf16 v[20:23], a[124:127], v[68:71], v[20:23]\n"
    "v_mul_f32 v98, v32, v36\n"
    "v_mul_f32 v99, v33, v37\n"
    "v_mfma_f32_16x16x32_bf16 v[20:23], a[140:143], v[72:75], v[20:23]\n"
    "v_add_f32 v96, 1.0, v96\n"
    "v_add_f32 v97, 1.0, v97\n"
    "v_mfma_f32_16x16x32_bf16 v[20:23], a[156:159], v[76:79], v[20:23]\n"
    "ds_read_b64_tr_b16 v[132:133], v126 offset:0\n"
    "v_mfma_f32_16x16x32_bf16 v[20:23], a[172:175], v[80:83], v[20:23]\n"
    "v_rcp_f32 v96, v96\n"
    "v_mfma_f32_16x16x32_bf16 v[20:23], a[188:191], v[84:87], v[20:23]\n"
    "s_waitcnt lgkmcnt(0)\n"
    "v_mfma_f32_16x16x32_bf16 v[8:11], a[192:195], v[40:43], v[8:11]\n"
    "ds_read_b64_tr_b16 v[134:135], v126 offset:2048\n"
    "v_mfma_f32_16x16x32_bf16 v[8:11], a[208:211], v[44:47], v[8:11]\n"
    "ds_read_b128 v[64:67], v4 offset:18432\n"
    "v_mfma_f32_16x16x32_bf16 v[8:11], a[224:227], v[48:51], v[8:11]\n"
    "v_rcp_f32 v97, v97\n"
    "v_mfma_f32_16x16x32_bf16 v[8:11], a[240:243], v[52:55], v[8:11]\n"
    "v_fma_f32 v100, v34, v34, v112\n"
    "v_fma_f32 v101, v35, v35, v112\n"
    "v_mfma_f32_16x16x32_bf16 v[8:11], v[224:227], v[56:59], v[8:11]\n"
    "ds_read_b128 v[68:71], v4 offset:19456\n"
    "v_mul_f32 v98, v98, v96\n"
    "v_mfma_f32_16x16x32_bf16 v[8:11], v[240:243], v[60:63], v[8:11]\n"
    "ds_read_b64_tr_b16 v[136:137], v114 offset:64\n"
    "v_mul_f32 v99, v99, v97\n"
    "v_mfma_f32_16x16x32_bf16 v[12:15], a[196:199], v[40:43], v[12:15]\n"
    "v_cvt_pk_bf16_f32 v94, v98, v99\n"
    "v_mul_f32 v100, v100, v34\n"
    "v_mfma_f32_16x16x32_bf16 v[12:15], a[212:215], v[44:47], v[12:15]\n"
    "ds_read_b128 v[72:75], v4 offset:20480\n"
    "v_mul_f32 v101, v101, v35\n"
    "v_mfma_f32_16x16x32_bf16 v[12:15], a[228:231], v[48:51], v[12:15]\n"
    "ds_read_b64_tr_b16 v[138:139], v114 offset:2112\n"
    "v_mfma_f32_16x16x32_bf16 v[12:15], a[244:247], v[52:55], v[12:15]\n"
    "v_exp_f32 v100, -v100\n"
    "v_mfma_f32_16x16x32_bf16 v[12:15], v[228:231], v[56:59], v[12:15]\n"
    "ds_read_b128 v[76:79], v4 offset:21504\n"
    "v_mfma_f32_16x16x32_bf16 v[12:15], v[244:247], v[60:63], v[12:15]\n"
    "ds_read_b64_tr_b16 v[140:141], v126 offset:64\n"
    "v_mfma_f32_16x16x32_bf16 v[16:19], a[200:203], v[40:43], v[16:19]\n"
    "v_exp_f32 v101, -v101\n"
    "v_mfma_f32_16x16x32_bf16 v[16:19], a[216:219], v[44:47], v[16:19]\n"
    "ds_read_b128 v[80:83], v4 offset:22528\n"
    "v_mul_f32 v102, v34, v38\n"
    "v_mfma_f32_16x16x32_bf16 v[16:19], a[232:235], v[48:51], v[16:19]\n"
    "ds_read_b64_tr_b16 v[142:143], v126 offset:2112\n"
    "v_mul_f32 v103, v35, v39\n"
    "v_mfma_f32_16x16x32_bf16 v[16:19], a[248:251], v[52:55], v[16:19]\n"
    "v_add_f32 v100, 1.0, v100\n"
    "v_add_f32 v101, 1.0, v101\n"
    "v_mfma_f32_16x16x32_bf16 v[16:19], v[232:235], v[56:59], v[16:19]\n"
    "ds_read_b128 v[84:87], v4 offset:23552\n"
    "v_mfma_f32_16x16x32_bf16 v[16:19], v[248:251], v[60:63], v[16:19]\n"
    "v_rcp_f32 v100, v100\n"
    "v_mfma_f32_16x16x32_bf16 v[20:23], a[204:207], v[40:43], v[20:23]\n"
    "ds_read_b64_tr_b16 v[144:145], v115 offset:4096\n"
    "v_mfma_f32_16x16x32_bf16 v[20:23], a[220:223], v[44:47], v[20:23]\n"
    "v_rcp_f32 v101, v101\n"
    "v_mfma_f32_16x16x32_bf16 v[20:23], a[236:239], v[48:51], v[20:23]\n"
    "s_nop 0\n"
    "v_mul_f32 v102, v102, v100\n"
    "v_mfma_f32_16x16x32_bf16 v[20:23], a[252:255], v[52:55], v[20:23]\n"
    "ds_read_b64_tr_b16 v[146:147], v115 offset:6144\n"
    "v_mul_f32 v103, v103, v101\n"
    "v_mfma_f32_16x16x32_bf16 v[20:23], v[236:239], v[56:59], v[20:23]\n"
    "v_cvt_pk_bf16_f32 v95, v102, v103\n"
    "v_mfma_f32_16x16x32_bf16 v[20:23], v[252:255], v[60:63], v[20:23]\n"
    "buffer_store_dwordx4 v[92:95], v4, s[24:27], s74 offen sc1\n"
    "s_waitcnt lgkmcnt(0)\n"
    "v_mfma_f32_16x16x32_bf16 v[24:27], a[0:3], v[64:67], 0\n"
    "v_readlane_b32 s74, v214, 21\n"
    "v_fma_f32 v96, v8, v8, v112\n"
    "v_mfma_f32_16x16x32_bf16 v[24:27], a[16:19], v[68:71], v[24:27]\n"
    "ds_read_b128 v[40:43], v4 offset:24576\n"
    "v_fma_f32 v97, v9, v9, v112\n"
    "v_mfma_f32_16x16x32_bf16 v[24:27], a[32:35], v[72:75], v[24:27]\n"
    "ds_read_b64_tr_b16 v[148:149], v127 offset:4096\n"
    "v_mul_f32 v96, v96, v8\n"
    "v_mfma_f32_16x16x32_bf16 v[24:27], a[48:51], v[76:79], v[24:27]\n"
    "buffer_store_dwordx4 v[128:131], v4, s[20:23], s88 offen sc1\n"
    "v_mul_f32 v97, v97, v9\n"
    "v_mfma_f32_16x16x32_bf16 v[24:27], a[64:67], v[80:83], v[24:27]\n"
    "ds_read_b128 v[44:47], v4 offset:25600\n"
    "v_mfma_f32_16x16x32_bf16 v[24:27], a[80:83], v[84:87], v[24:27]\n"
    "ds_read_b64_tr_b16 v[150:151], v127 offset:6144\n"
    "v_mfma_f32_16x16x32_bf16 v[28:31], a[4:7], v[64:67], 0\n"
    "v_exp_f32 v96, -v96\n"
    "v_mfma_f32_16x16x32_bf16 v[28:31], a[20:23], v[68:71], v[28:31]\n"
    "ds_read_b128 v[48:51], v4 offset:26624\n"
    "v_mfma_f32_16x16x32_bf16 v[28:31], a[36:39], v[72:75], v[28:31]\n"
    "ds_read_b64_tr_b16 v[152:153], v115 offset:4160\n"
    "v_mfma_f32_16x16x32_bf16 v[28:31], a[52:55], v[76:79], v[28:31]\n"
    "v_exp_f32 v97, -v97\n"
    "v_mfma_f32_16x16x32_bf16 v[28:31], a[68:71], v[80:83], v[28:31]\n"
    "ds_read_b128 v[52:55], v4 offset:27648\n"
    "v_mul_f32 v98, v8, v12\n"
    "v_mfma_f32_16x16x32_bf16 v[28:31], a[84:87], v[84:87], v[28:31]\n"
    "ds_read_b64_tr_b16 v[154:155], v115 offset:6208\n"
    "v_mul_f32 v99, v9, v13\n"
    "v_mfma_f32_16x16x32_bf16 v[32:35], a[8:11], v[64:67], 0\n"
    "buffer_store_dwordx4 v[132:135], v4, s[20:23], s89 offen sc1\n"
    "v_add_f32 v96, 1.0, v96\n"
    "v_mfma_f32_16x16x32_bf16 v[32:35], a[24:27], v[68:71], v[32:35]\n"
    "ds_read_b128 v[56:59], v4 offset:28672\n"
    "v_add_f32 v97, 1.0, v97\n"
    "v_mfma_f32_16x16x32_bf16 v[32:35], a[40:43], v[72:75], v[32:35]\n"
    "v_rcp_f32 v96, v96\n"
    "v_mfma_f32_16x16x32_bf16 v[32:35], a[56:59], v[76:79], v[32:35]\n"
    "ds_read_b64_tr_b16 v[156:157], v127 offset:4160\n"
    "v_mfma_f32_16x16x32_bf16 v[32:35], a[72:75], v[80:83], v[32:35]\n"
    "ds_read_b128 v[60:63], v4 offset:29696\n"
    "v_mfma_f32_16x16x32_bf16 v[32:35], a[88:91], v[84:87], v[32:35]\n"
    "v_rcp_f32 v97, v97\n"
    "v_mfma_f32_16x16x32_bf16 v[36:39], a[12:15], v[64:67], 0\n"
    "ds_read_b64_tr_b16 v[158:159], v127 offset:6208\n"
    "v_fma_f32 v100, v10, v10, v112\n"
    "v_mfma_f32_16x16x32_bf16 v[36:39], a[28:31], v[68:71], v[36:39]\n"
    "v_fma_f32 v101, v11, v11, v112\n"
    "v_mul_f32 v98, v98, v96\n"
    "v_mfma_f32_16x16x32_bf16 v[36:39], a[44:47], v[72:75], v[36:39]\n"
    "buffer_store_dwordx4 v[136:139], v4, s[20:23], s90 offen sc1\n"
    "v_mul_f32 v99, v99, v97\n"
    "v_mfma_f32_16x16x32_bf16 v[36:39], a[60:63], v[76:79], v[36:39]\n"
    "v_cvt_pk_bf16_f32 v88, v98, v99\n"
    "v_mul_f32 v100, v100, v10\n"
    "v_mfma_f32_16x16x32_bf16 v[36:39], a[76:79], v[80:83], v[36:39]\n"
    "v_mul_f32 v101, v101, v11\n"
    "v_mfma_f32_16x16x32_bf16 v[36:39], a[92:95], v[84:87], v[36:39]\n"
    "s_waitcnt lgkmcnt(0)\n"
    "v_mfma_f32_16x16x32_bf16 v[24:27], a[96:99], v[40:43], v[24:27]\n"
    "v_exp_f32 v100, -v100\n"
    "v_mfma_f32_16x16x32_bf16 v[24:27], a[112:115], v[44:47], v[24:27]\n"
    "ds_read_b128 v[64:67], v4 offset:30720\n"
    "v_mfma_f32_16x16x32_bf16 v[24:27], a[128:131], v[48:51], v[24:27]\n"
    "buffer_store_dwordx4 v[140:143], v4, s[20:23], s91 offen sc1\n"
    "v_mfma_f32_16x16x32_bf16 v[24:27], a[144:147], v[52:55], v[24:27]\n"
    "v_exp_f32 v101, -v101\n"
    "v_mfma_f32_16x16x32_bf16 v[24:27], a[160:163], v[56:59], v[24:27]\n"
    "ds_read_b128 v[68:71], v4 offset:31744\n"
    "v_mul_f32 v102, v10, v14\n"
    "v_mfma_f32_16x16x32_bf16 v[24:27], a[176:179], v[60:63], v[24:27]\n"
    "v_mul_f32 v103, v11, v15\n"
    "v_add_f32 v100, 1.0, v100\n"
    "v_mfma_f32_16x16x32_bf16 v[28:31], a[100:103], v[40:43], v[28:31]\n"
    "v_add_f32 v101, 1.0, v101\n"
    "v_mfma_f32_16x16x32_bf16 v[28:31], a[116:119], v[44:47], v[28:31]\n"
    "ds_read_b128 v[72:75], v4 offset:32768\n"
    "v_mfma_f32_16x16x32_bf16 v[28:31], a[132:135], v[48:51], v[28:31]\n"
    "v_rcp_f32 v100, v100\n"
    "v_mfma_f32_16x16x32_bf16 v[28:31], a[148:151], v[52:55], v[28:31]\n"
    "v_rcp_f32 v101, v101\n"
    "v_mfma_f32_16x16x32_bf16 v[28:31], a[164:167], v[56:59], v[28:31]\n"
    "ds_read_b128 v[76:79], v4 offset:33792\n"
    "v_fma_f32 v96, v16, v16, v112\n"
    "v_mfma_f32_16x16x32_bf16 v[28:31], a[180:183], v[60:63], v[28:31]\n"
    "buffer_store_dwordx4 v[144:147], v4, s[4:7], s92 offen sc1\n"
    "v_fma_f32 v97, v17, v17, v112\n"
    "v_mfma_f32_16x16x32_bf16 v[32:35], a[104:107], v[40:43], v[32:35]\n"
    "v_mul_f32 v102, v102, v100\n"
    "v_mul_f32 v103, v103, v101\n"
    "v_mfma_f32_16x16x32_bf16 v[32:35], a[120:123], v[44:47], v[32:35]\n"
    "ds_read_b128 v[80:83], v4 offset:34816\n"
    "v_cvt_pk_bf16_f32 v89, v102, v103\n"
    "v_mfma_f32_16x16x32_bf16 v[32:35], a[136:139], v[48:51], v[32:35]\n"
    "v_mul_f32 v96, v96, v16\n"
    "v_mul_f32 v97, v97, v17\n"
    "v_mfma_f32_16x16x32_bf16 v[32:35], a[152:155], v[52:55], v[32:35]\n"
    "v_exp_f32 v96, -v96\n"
    "v_mfma_f32_16x16x32_bf16 v[32:35], a[168:171], v[56:59], v[32:35]\n"
    "ds_read_b128 v[84:87], v4 offset:35840\n"
    "v_mfma_f32_16x16x32_bf16 v[32:35], a[184:187], v[60:63], v[32:35]\n"
    "v_exp_f32 v97, -v97\n"
    "v_mfma_f32_16x16x32_bf16 v[36:39], a[108:111], v[40:43], v[36:39]\n"
    "v_mul_f32 v98, v16, v20\n"
    "v_mul_f32 v99, v17, v21\n"
    "v_mfma_f32_16x16x32_bf16 v[36:39], a[124:127], v[44:47], v[36:39]\n"
    "v_add_f32 v96, 1.0, v96\n"
    "v_add_f32 v97, 1.0, v97\n"
    "v_mfma_f32_16x16x32_bf16 v[36:39], a[140:143], v[48:51], v[36:39]\n"
    "buffer_store_dwordx4 v[148:151], v4, s[4:7], s93 offen sc1\n"
    "v_mfma_f32_16x16x32_bf16 v[36:39], a[156:159], v[52:55], v[36:39]\n"
    "v_rcp_f32 v96, v96\n"
    "v_mfma_f32_16x16x32_bf16 v[36:39], a[172:175], v[56:59], v[36:39]\n"
    "v_rcp_f32 v97, v97\n"
    "v_mfma_f32_16x16x32_bf16 v[36:39], a[188:191], v[60:63], v[36:39]\n"
    "s_waitcnt vmcnt(8) lgkmcnt(0)\n"
    "s_barrier\n"
    "v_mfma_f32_16x16x32_bf16 v[24:27], a[192:195], v[64:67], v[24:27]\n"
    "v_fma_f32 v100, v18, v18, v112\n"
    "v_fma_f32 v101, v19, v19, v112\n"
    "v_mfma_f32_16x16x32_bf16 v[24:27], a[208:211], v[68:71], v[24:27]\n"
    "ds_read_b128 v[40:43], v120 offset:0\n"
    "v_mul_f32 v98, v98, v96\n"
    "v_mfma_f32_16x16x32_bf16 v[24:27], a[224:227], v[72:75], v[24:27]\n"
    "s_add_u32 s77, s71, 0\n"
    "s_add_u32 m0, s70, 0\n"
    "v_mfma_f32_16x16x32_bf16 v[24:27], a[240:243], v[76:79], v[24:27]\n"
    "buffer_load_dwordx4 v4, s[20:23], s77 offen offset:0 sc1 lds\n"
    "v_mul_f32 v99, v99, v97\n"
    "v_mfma_f32_16x16x32_bf16 v[24:27], v[224:227], v[80:83], v[24:27]\n"
    "ds_read_b128 v[44:47], v120 offset:1024\n"
    "v_cvt_pk_bf16_f32 v90, v98, v99\n"
    "v_mfma_f32_16x16x32_bf16 v[24:27], v[240:243], v[84:87], v[24:27]\n"
    "buffer_store_dwordx4 v[152:155], v4, s[4:7], s94 offen sc1\n"
    "v_mfma_f32_16x16x32_bf16 v[28:31], a[196:199], v[64:67], v[28:31]\n"
    "v_mul_f32 v100, v100, v18\n"
    "v_mul_f32 v101, v101, v19\n"
    "v_mfma_f32_16x16x32_bf16 v[28:31], a[212:215], v[68:71], v[28:31]\n"
    "ds_read_b128 v[48:51], v120 offset:2048\n"
    "v_mfma_f32_16x16x32_bf16 v[28:31], a[228:231], v[72:75], v[28:31]\n"
    "buffer_load_dwordx4 v4, s[20:23], s77 offen offset:1024 sc1 lds\n"
    "v_mfma_f32_16x16x32_bf16 v[28:31], a[244:247], v[76:79], v[28:31]\n"
    "v_exp_f32 v100, -v100\n"
    "v_mfma_f32_16x16x32_bf16 v[28:31], v[228:231], v[80:83], v[28:31]\n"
    "ds_read_b128 v[52:55], v120 offset:3072\n"
    "v_mfma_f32_16x16x32_bf16 v[28:31], v[244:247], v[84:87], v[28:31]\n"
    "v_exp_f32 v101, -v101\n"
    "v_mfma_f32_16x16x32_bf16 v[32:35], a[200:203], v[64:67], v[32:35]\n"
    "v_mul_f32 v102, v18, v22\n"
    "v_mul_f32 v103, v19, v23\n"
    "v_mfma_f32_16x16x32_bf16 v[32:35], a[216:219], v[68:71], v[32:35]\n"
    "ds_read_b128 v[56:59], v120 offset:4096\n"
    "v_add_f32 v100, 1.0, v100\n"
    "v_mfma_f32_16x16x32_bf16 v[32:35], a[232:235], v[72:75], v[32:35]\n"
    "buffer_store_dwordx4 v[156:159], v4, s[4:7], s95 offen sc1\n"
    "v_add_f32 v101, 1.0, v101\n"
    "v_mfma_f32_16x16x32_bf16 v[32:35], a[248:251], v[76:79], v[32:35]\n"
    "buffer_load_dwordx4 v4, s[20:23], s77 offen offset:2048 sc1 lds\n"
    "v_mfma_f32_16x16x32_bf16 v[32:35], v[232:235], v[80:83], v[32:35]\n"
    "ds_read_b128 v[60:63], v120 offset:5120\n"
    "v_mfma_f32_16x16x32_bf16 v[32:35], v[248:251], v[84:87], v[32:35]\n"
    "v_rcp_f32 v100, v100\n"
    "v_mfma_f32_16x16x32_bf16 v[36:39], a[204:207], v[64:67], v[36:39]\n"
    "v_rcp_f32 v101, v101\n"
    "v_mfma_f32_16x16x32_bf16 v[36:39], a[220:223], v[68:71], v[36:39]\n"
    "s_nop 0\n"
    "v_mul_f32 v102, v102, v100\n"
    "v_mfma_f32_16x16x32_bf16 v[36:39], a[236:239], v[72:75], v[36:39]\n"
    "buffer_load_dwordx4 v4, s[20:23], s77 offen offset:3072 sc1 lds\n"
    "v_mul_f32 v103, v103, v101\n"
    "v_mfma_f32_16x16x32_bf16 v[36:39], a[252:255], v[76:79], v[36:39]\n"
    "v_cvt_pk_bf16_f32 v91, v102, v103\n"
    "v_mfma_f32_16x16x32_bf16 v[36:39], v[236:239], v[80:83], v[36:39]\n"
    "v_mfma_f32_16x16x32_bf16 v[36:39], v[252:255], v[84:87], v[36:39]\n"
    "s_waitcnt lgkmcnt(0)\n"
    "v_mfma_f32_16x16x32_bf16 v[8:11], a[0:3], v[40:43], 0\n"
    "v_fma_f32 v96, v24, v24, v112\n"
    "v_fma_f32 v97, v25, v25, v112\n"
    "v_mfma_f32_16x16x32_bf16 v[8:11], a[16:19], v[44:47], v[8:11]\n"
    "ds_read_b128 v[64:67], v120 offset:6144\n"
    "v_mul_f32 v96, v96, v24\n"
    "v_mfma_f32_16x16x32_bf16 v[8:11], a[32:35], v[48:51], v[8:11]\n"
    "s_add_u32 s77, s71, 4096\n"
    "s_add_u32 m0, s70, 4096\n"
    "v_mfma_f32_16x16x32_bf16 v[8:11], a[48:51], v[52:55], v[8:11]\n"
    "buffer_load_dwordx4 v4, s[20:23], s77 offen offset:0 sc1 lds\n"
    "v_mul_f32 v97, v97, v25\n"
    "v_mfma_f32_16x16x32_bf16 v[8:11], a[64:67], v[56:59], v[8:11]\n"
    "ds_read_b128 v[68:71], v120 offset:7168\n"
    "v_mfma_f32_16x16x32_bf16 v[8:11], a[80:83], v[60:63], v[8:11]\n"
    "v_exp_f32 v96, -v96\n"
    "v_mfma_f32_16x16x32_bf16 v[12:15], a[4:7], v[40:43], 0\n"
    "v_exp_f32 v97, -v97\n"
    "v_mfma_f32_16x16x32_bf16 v[12:15], a[20:23], v[44:47], v[12:15]\n"
    "ds_read_b128 v[72:75], v120 offset:8192\n"
    "v_mul_f32 v98, v24, v28\n"
    "v_mfma_f32_16x16x32_bf16 v[12:15], a[36:39], v[48:51], v[12:15]\n"
    "buffer_load_dwordx4 v4, s[20:23], s77 offen offset:1024 sc1 lds\n"
    "v_mul_f32 v99, v25, v29\n"
    "v_mfma_f32_16x16x32_bf16 v[12:15], a[52:55], v[52:55], v[12:15]\n"
    "v_add_f32 v96, 1.0, v96\n"
    "v_add_f32 v97, 1.0, v97\n"
    "v_mfma_f32_16x16x32_bf16 v[12:15], a[68:71], v[56:59], v[12:15]\n"
    "ds_read_b128 v[76:79], v120 offset:9216\n"
    "v_mfma_f32_16x16x32_bf16 v[12:15], a[84:87], v[60:63], v[12:15]\n"
    "v_rcp_f32 v96, v96\n"
    "v_mfma_f32_16x16x32_bf16 v[16:19], a[8:11], v[40:43], 0\n"
    "v_rcp_f32 v97, v97\n"
    "v_mfma_f32_16x16x32_bf16 v[16:19], a[24:27], v[44:47], v[16:19]\n"
    "ds_read_b128 v[80:83], v120 offset:10240\n"
    "v_mfma_f32_16x16x32_bf16 v[16:19], a[40:43], v[48:51], v[16:19]\n"
    "buffer_load_dwordx4 v4, s[20:23], s77 offen offset:2048 sc1 lds\n"
    "v_fma_f32 v100, v26, v26, v112\n"
    "v_mfma_f32_16x16x32_bf16 v[16:19], a[56:59], v[52:55], v[16:19]\n"
    "v_fma_f32 v101, v27, v27, v112\n"
    "v_mfma_f32_16x16x32_bf16 v[16:19], a[72:75], v[56:59], v[16:19]\n"
    "ds_read_b128 v[84:87], v120 offset:11264\n"
    "v_mul_f32 v98, v98, v96\n"
    "v_mfma_f32_16x16x32_bf16 v[16:19], a[88:91], v[60:63], v[16:19]\n"
    "v_mul_f32 v99, v99, v97\n"
    "v_cvt_pk_bf16_f32 v92, v98, v99\n"
    "v_mfma_f32_16x16x32_bf16 v[20:23], a[12:15], v[40:43], 0\n"
    "v_mul_f32 v100, v100, v26\n"
    "v_mfma_f32_16x16x32_bf16 v[20:23], a[28:31], v[44:47], v[20:23]\n"
    "buffer_load_dwordx4 v4, s[20:23], s77 offen offset:3072 sc1 lds\n"
    "s_add_u32 s77, s71, 8192\n"
    "v_mfma_f32_16x16x32_bf16 v[20:23], a[44:47], v[48:51], v[20:23]\n"
    "s_add_u32 m0, s70, 8192\n"
    "v_mul_f32 v101, v101, v27\n"
    "v_mfma_f32_16x16x32_bf16 v[20:23], a[60:63], v[52:55], v[20:23]\n"
    "v_mfma_f32_16x16x32_bf16 v[20:23], a[76:79], v[56:59], v[20:23]\n"
    "v_exp_f32 v100, -v100\n"
    "v_mfma_f32_16x16x32_bf16 v[20:23], a[92:95], v[60:63], v[20:23]\n"
    "s_waitcnt lgkmcnt(0)\n"
    "v_mfma_f32_16x16x32_bf16 v[8:11], a[96:99], v[64:67], v[8:11]\n"
    "buffer_load_dwordx4 v4, s[20:23], s77 offen offset:0 sc1 lds\n"
    "v_readlane_b32 s71, v214, 5\n"
    "v_mfma_f32_16x16x32_bf16 v[8:11], a[112:115], v[68:71], v[8:11]\n"
    "ds_read_b128 v[40:43], v120 offset:12288\n"
    "v_mfma_f32_16x16x32_bf16 v[8:11], a[128:131], v[72:75], v[8:11]\n"
    "v_exp_f32 v101, -v101\n"
    "v_mfma_f32_16x16x32_bf16 v[8:11], a[144:147], v[76:79], v[8:11]\n"
    "v_mul_f32 v102, v26, v30\n"
    "v_mul_f32 v103, v27, v31\n"
    "v_mfma_f32_16x16x32_bf16 v[8:11], a[160:163], v[80:83], v[8:11]\n"
    "ds_read_b128 v[44:47], v120 offset:13312\n"
    "v_add_f32 v100, 1.0, v100\n"
    "v_mfma_f32_16x16x32_bf16 v[8:11], a[176:179], v[84:87], v[8:11]\n"
    "v_add_f32 v101, 1.0, v101\n"
    "v_mfma_f32_16x16x32_bf16 v[12:15], a[100:103], v[64:67], v[12:15]\n"
    "buffer_store_dwordx4 v[88:91], v4, s[24:27], s74 offen sc1\n"
    "v_readlane_b32 s74, v214, 22\n"
    "v_mfma_f32_16x16x32_bf16 v[12:15], a[116:119], v[68:71], v[12:15]\n"
    "ds_read_b128 v[48:51], v120 offset:14336\n"
    "v_mfma_f32_16x16x32_bf16 v[12:15], a[132:135], v[72:75], v[12:15]\n"
    "v_rcp_f32 v100, v100\n"
    "v_mfma_f32_16x16x32_bf16 v[12:15], a[148:151], v[76:79], v[12:15]\n"
    "v_rcp_f32 v101, v101\n"
    "v_mfma_f32_16x16x32_bf16 v[12:15], a[164:167], v[80:83], v[12:15]\n"
    "ds_read_b128 v[52:55], v120 offset:15360\n"
    "v_fma_f32 v96, v32, v32, v112\n"
    "v_mfma_f32_16x16x32_bf16 v[12:15], a[180:183], v[84:87], v[12:15]\n"
    "v_fma_f32 v97, v33, v33, v112\n"
    "v_mul_f32 v102, v102, v100\n"
    "v_mfma_f32_16x16x32_bf16 v[16:19], a[104:107], v[64:67], v[16:19]\n"
    "v_mul_f32 v103, v103, v101\n"
    "v_cvt_pk_bf16_f32 v93, v102, v103\n"
    "v_mfma_f32_16x16x32_bf16 v[16:19], a[120:123], v[68:71], v[16:19]\n"
    "ds_read_b128 v[56:59], v120 offset:16384\n"
    "v_mfma_f32_16x16x32_bf16 v[16:19], a[136:139], v[72:75], v[16:19]\n"
    "v_mul_f32 v96, v96, v32\n"
    "v_mul_f32 v97, v97, v33\n"
    "v_mfma_f32_16x16x32_bf16 v[16:19], a[152:155], v[76:79], v[16:19]\n"
    "v_mfma_f32_16x16x32_bf16 v[16:19], a[168:171], v[80:83], v[16:19]\n"
    "ds_read_b128 v[60:63], v120 offset:17408\n"
    "v_mfma_f32_16x16x32_bf16 v[16:19], a[184:187], v[84:87], v[16:19]\n"
    "v_exp_f32 v96, -v96\n"
    "v_mfma_f32_16x16x32_bf16 v[20:23], a[108:111], v[64:67], v[20:23]\n"
    "v_exp_f32 v97, -v97\n"
    "v_mfma_f32_16x16x32_bf16 v[20:23], a[124:127], v[68:71], v[20:23]\n"
    "v_mul_f32 v98, v32, v36\n"
    "v_mul_f32 v99, v33, v37\n"
    "v_mfma_f32_16x16x32_bf16 v[20:23], a[140:143], v[72:75], v[20:23]\n"
    "v_add_f32 v96, 1.0, v96\n"
    "v_add_f32 v97, 1.0, v97\n"
    "v_mfma_f32_16x16x32_bf16 v[20:23], a[156:159], v[76:79], v[20:23]\n"
    "v_mfma_f32_16x16x32_bf16 v[20:23], a[172:175], v[80:83], v[20:23]\n"
    "v_rcp_f32 v96, v96\n"
    "v_mfma_f32_16x16x32_bf16 v[20:23], a[188:191], v[84:87], v[20:23]\n"
    "s_waitcnt lgkmcnt(0)\n"
    "v_mfma_f32_16x16x32_bf16 v[8:11], a[192:195], v[40:43], v[8:11]\n"
    "v_rcp_f32 v97, v97\n"
    "v_mfma_f32_16x16x32_bf16 v[8:11], a[208:211], v[44:47], v[8:11]\n"
    "ds_read_b128 v[64:67], v120 offset:18432\n"
    "v_fma_f32 v100, v34, v34, v112\n"
    "v_mfma_f32_16x16x32_bf16 v[8:11], a[224:227], v[48:51], v[8:11]\n"
    "buffer_store_dwordx4 v[192:195], v4, s[52:55], s56 offen sc1\n"
    "s_add_u32 s56, s56, 4096\n"
    "v_mfma_f32_16x16x32_bf16 v[8:11], a[240:243], v[52:55], v[8:11]\n"
    "v_fma_f32 v101, v35, v35, v112\n"
    "v_mul_f32 v98, v98, v96\n"
    "v_mfma_f32_16x16x32_bf16 v[8:11], v[224:227], v[56:59], v[8:11]\n"
    "ds_read_b128 v[68:71], v120 offset:19456\n"
    "v_mul_f32 v99, v99, v97\n"
    "v_mfma_f32_16x16x32_bf16 v[8:11], v[240:243], v[60:63], v[8:11]\n"
    "v_cvt_pk_bf16_f32 v94, v98, v99\n"
    "v_mfma_f32_16x16x32_bf16 v[12:15], a[196:199], v[40:43], v[12:15]\n"
    "v_mul_f32 v100, v100, v34\n"
    "v_mul_f32 v101, v101, v35\n"
    "v_mfma_f32_16x16x32_bf16 v[12:15], a[212:215], v[44:47], v[12:15]\n"
    "ds_read_b128 v[72:75], v120 offset:20480\n"
    "v_mfma_f32_16x16x32_bf16 v[12:15], a[228:231], v[48:51], v[12:15]\n"
    "v_exp_f32 v100, -v100\n"
    "v_mfma_f32_16x16x32_bf16 v[12:15], a[244:247], v[52:55], v[12:15]\n"
    "v_mfma_f32_16x16x32_bf16 v[12:15], v[228:231], v[56:59], v[12:15]\n"
    "ds_read_b128 v[76:79], v120 offset:21504\n"
    "v_mfma_f32_16x16x32_bf16 v[12:15], v[244:247], v[60:63], v[12:15]\n"
    "v_exp_f32 v101, -v101\n"
    "v_mfma_f32_16x16x32_bf16 v[16:19], a[200:203], v[40:43], v[16:19]\n"
    "v_mul_f32 v102, v34, v38\n"
    "v_mul_f32 v103, v35, v39\n"
    "v_mfma_f32_16x16x32_bf16 v[16:19], a[216:219], v[44:47], v[16:19]\n"
    "ds_read_b128 v[80:83], v120 offset:22528\n"
    "v_add_f32 v100, 1.0, v100\n"
    "v_mfma_f32_16x16x32_bf16 v[16:19], a[232:235], v[48:51], v[16:19]\n"
    "v_add_f32 v101, 1.0, v101\n"
    "v_mfma_f32_16x16x32_bf16 v[16:19], a[248:251], v[52:55], v[16:19]\n"
    "v_rcp_f32 v100, v100\n"
    "v_mfma_f32_16x16x32_bf16 v[16:19], v[232:235], v[56:59], v[16:19]\n"
    "ds_read_b128 v[84:87], v120 offset:23552\n"
    "v_mfma_f32_16x16x32_bf16 v[16:19], v[248:251], v[60:63], v[16:19]\n"
    "buffer_store_dwordx4 v[196:199], v4, s[52:55], s56 offen sc1\n"
    "s_add_u32 s56, s56, 4096\n"
    "v_mfma_f32_16x16x32_bf16 v[20:23], a[204:207], v[40:43], v[20:23]\n"
    "v_rcp_f32 v101, v101\n"
    "v_mfma_f32_16x16x32_bf16 v[20:23], a[220:223], v[44:47], v[20:23]\n"
    "s_nop 0\n"
    "v_mul_f32 v102, v102, v100\n"
    "v_mfma_f32_16x16x32_bf16 v[20:23], a[236:239], v[48:51], v[20:23]\n"
    "v_mul_f32 v103, v103, v101\n"
    "v_cvt_pk_bf16_f32 v95, v102, v103\n"
    "v_mfma_f32_16x16x32_bf16 v[20:23], a[252:255], v[52:55], v[20:23]\n"
    "v_mfma_f32_16x16x32_bf16 v[20:23], v[236:239], v[56:59], v[20:23]\n"
    "v_mfma_f32_16x16x32_bf16 v[20:23], v[252:255], v[60:63], v[20:23]\n"
    "buffer_store_dwordx4 v[92:95], v4, s[24:27], s74 offen sc1\n"
    "s_waitcnt lgkmcnt(0)\n"
    "v_mfma_f32_16x16x32_bf16 v[24:27], a[0:3], v[64:67], 0\n"
    "v_readlane_b32 s74, v214, 23\n"
    "v_fma_f32 v96, v8, v8, v112\n"
    "v_mfma_f32_16x16x32_bf16 v[24:27], a[16:19], v[68:71], v[24:27]\n"
    "ds_read_b128 v[40:43], v120 offset:24576\n"
    "v_fma_f32 v97, v9, v9, v112\n"
    "v_mfma_f32_16x16x32_bf16 v[24:27], a[32:35], v[72:75], v[24:27]\n"
    "v_mul_f32 v96, v96, v8\n"
    "v_mul_f32 v97, v97, v9\n"
    "v_mfma_f32_16x16x32_bf16 v[24:27], a[48:51], v[76:79], v[24:27]\n"
    "v_exp_f32 v96, -v96\n"
    "v_mfma_f32_16x16x32_bf16 v[24:27], a[64:67], v[80:83], v[24:27]\n"
    "ds_read_b128 v[44:47], v120 offset:25600\n"
    "v_mfma_f32_16x16x32_bf16 v[24:27], a[80:83], v[84:87], v[24:27]\n"
    "v_exp_f32 v97, -v97\n"
    "v_mfma_f32_16x16x32_bf16 v[28:31], a[4:7], v[64:67], 0\n"
    "v_mul_f32 v98, v8, v12\n"
    "v_mul_f32 v99, v9, v13\n"
    "v_mfma_f32_16x16x32_bf16 v[28:31], a[20:23], v[68:71], v[28:31]\n"
    "ds_read_b128 v[48:51], v120 offset:26624\n"
    "v_add_f32 v96, 1.0, v96\n"
    "v_mfma_f32_16x16x32_bf16 v[28:31], a[36:39], v[72:75], v[28:31]\n"
    "buffer_store_dwordx4 v[200:203], v4, s[52:55], s56 offen sc1\n"
    "s_add_u32 s56, s56, 4096\n"
    "v_mfma_f32_16x16x32_bf16 v[28:31], a[52:55], v[76:79], v[28:31]\n"
    "v_add_f32 v97, 1.0, v97\n"
    "v_mfma_f32_16x16x32_bf16 v[28:31], a[68:71], v[80:83], v[28:31]\n"
    "ds_read_b128 v[52:55], v120 offset:27648\n"
    "v_mfma_f32_16x16x32_bf16 v[28:31], a[84:87], v[84:87], v[28:31]\n"
    "v_rcp_f32 v96, v96\n"
    "v_mfma_f32_16x16x32_bf16 v[32:35], a[8:11], v[64:67], 0\n"
    "v_rcp_f32 v97, v97\n"
    "v_mfma_f32_16x16x32_bf16 v[32:35], a[24:27], v[68:71], v[32:35]\n"
    "ds_read_b128 v[56:59], v120 offset:28672\n"
    "v_fma_f32 v100, v10, v10, v112\n"
    "v_mfma_f32_16x16x32_bf16 v[32:35], a[40:43], v[72:75], v[32:35]\n"
    "v_fma_f32 v101, v11, v11, v112\n"
    "v_mfma_f32_16x16x32_bf16 v[32:35], a[56:59], v[76:79], v[32:35]\n"
    "v_mul_f32 v98, v98, v96\n"
    "v_mul_f32 v99, v99, v97\n"
    "v_mfma_f32_16x16x32_bf16 v[32:35], a[72:75], v[80:83], v[32:35]\n"
    "ds_read_b128 v[60:63], v120 offset:29696\n"
    "v_cvt_pk_bf16_f32 v88, v98, v99\n"
    "v_mfma_f32_16x16x32_bf16 v[32:35], a[88:91], v[84:87], v[32:35]\n"
    "v_mul_f32 v100, v100, v10\n"
    "v_mfma_f32_16x16x32_bf16 v[36:39], a[12:15], v[64:67], 0\n"
    "v_mul_f32 v101, v101, v11\n"
    "v_mfma_f32_16x16x32_bf16 v[36:39], a[28:31], v[68:71], v[36:39]\n"
    "v_exp_f32 v100, -v100\n"
    "v_mfma_f32_16x16x32_bf16 v[36:39], a[44:47], v[72:75], v[36:39]\n"
    "v_mfma_f32_16x16x32_bf16 v[36:39], a[60:63], v[76:79], v[36:39]\n"
    "v_exp_f32 v101, -v101\n"
    "v_mfma_f32_16x16x32_bf16 v[36:39], a[76:79], v[80:83], v[36:39]\n"
    "v_mul_f32 v102, v10, v14\n"
    "v_mul_f32 v103, v11, v15\n"
    "v_mfma_f32_16x16x32_bf16 v[36:39], a[92:95], v[84:87], v[36:39]\n"
    "buffer_store_dwordx4 v[204:207], v4, s[52:55], s56 offen sc1\n"
    "s_waitcnt lgkmcnt(0)\n"
    "v_mfma_f32_16x16x32_bf16 v[24:27], a[96:99], v[40:43], v[24:27]\n"
    "s_add_u32 s56, s56, 4096\n"
    "v_add_f32 v100, 1.0, v100\n"
    "v_mfma_f32_16x16x32_bf16 v[24:27], a[112:115], v[44:47], v[24:27]\n"
    "ds_read_b128 v[64:67], v120 offset:30720\n"
    "v_add_f32 v101, 1.0, v101\n"
    "v_mfma_f32_16x16x32_bf16 v[24:27], a[128:131], v[48:51], v[24:27]\n"
    "v_mfma_f32_16x16x32_bf16 v[24:27], a[144:147], v[52:55], v[24:27]\n"
    "v_rcp_f32 v100, v100\n"
    "v_mfma_f32_16x16x32_bf16 v[24:27], a[160:163], v[56:59], v[24:27]\n"
    "ds_read_b128 v[68:71], v120 offset:31744\n"
    "v_mfma_f32_16x16x32_bf16 v[24:27], a[176:179], v[60:63], v[24:27]\n"
    "v_rcp_f32 v101, v101\n"
    "v_mfma_f32_16x16x32_bf16 v[28:31], a[100:103], v[40:43], v[28:31]\n"
    "v_fma_f32 v96, v16, v16, v112\n"
    "v_fma_f32 v97, v17, v17, v112\n"
    "v_mfma_f32_16x16x32_bf16 v[28:31], a[116:119], v[44:47], v[28:31]\n"
    "ds_read_b128 v[72:75], v120 offset:32768\n"
    "v_mul_f32 v102, v102, v100\n"
    "v_mfma_f32_16x16x32_bf16 v[28:31], a[132:135], v[48:51], v[28:31]\n"
    "v_mul_f32 v103, v103, v101\n"
    "v_cvt_pk_bf16_f32 v89, v102, v103\n"
    "v_mfma_f32_16x16x32_bf16 v[28:31], a[148:151], v[52:55], v[28:31]\n"
    "v_mul_f32 v96, v96, v16\n"
    "v_mfma_f32_16x16x32_bf16 v[28:31], a[164:167], v[56:59], v[28:31]\n"
    "ds_read_b128 v[76:79], v120 offset:33792\n"
    "v_mul_f32 v97, v97, v17\n"
    "v_mfma_f32_16x16x32_bf16 v[28:31], a[180:183], v[60:63], v[28:31]\n"
    "v_mfma_f32_16x16x32_bf16 v[32:35], a[104:107], v[40:43], v[32:35]\n"
    "v_exp_f32 v96, -v96\n"
    "v_mfma_f32_16x16x32_bf16 v[32:35], a[120:123], v[44:47], v[32:35]\n"
    "ds_read_b128 v[80:83], v120 offset:34816\n"
    "v_mfma_f32_16x16x32_bf16 v[32:35], a[136:139], v[48:51], v[32:35]\n"
    "buffer_store_dwordx4 v[208:211], v125, s[52:55], s57 offen sc1\n"
    "v_mfma_f32_16x16x32_bf16 v[32:35], a[152:155], v[52:55], v[32:35]\n"
    "v_exp_f32 v97, -v97\n"
    "v_mfma_f32_16x16x32_bf16 v[32:35], a[168:171], v[56:59], v[32:35]\n"
    "ds_read_b128 v[84:87], v120 offset:35840\n"
    "v_mul_f32 v98, v16, v20\n"
    "v_mfma_f32_16x16x32_bf16 v[32:35], a[184:187], v[60:63], v[32:35]\n"
    "v_mul_f32 v99, v17, v21\n"
    "v_add_f32 v96, 1.0, v96\n"
    "v_mfma_f32_16x16x32_bf16 v[36:39], a[108:111], v[40:43], v[36:39]\n"
    "v_add_f32 v97, 1.0, v97\n"
    "v_mfma_f32_16x16x32_bf16 v[36:39], a[124:127], v[44:47], v[36:39]\n"
    "v_rcp_f32 v96, v96\n"
    "v_mfma_f32_16x16x32_bf16 v[36:39], a[140:143], v[48:51], v[36:39]\n"
    "v_rcp_f32 v97, v97\n"
    "v_mfma_f32_16x16x32_bf16 v[36:39], a[156:159], v[52:55], v[36:39]\n"
    "v_fma_f32 v100, v18, v18, v112\n"
    "v_fma_f32 v101, v19, v19, v112\n"
    "v_mfma_f32_16x16x32_bf16 v[36:39], a[172:175], v[56:59], v[36:39]\n"
    "v_mul_f32 v98, v98, v96\n"
    "v_mfma_f32_16x16x32_bf16 v[36:39], a[188:191], v[60:63], v[36:39]\n"
    "s_waitcnt vmcnt(7) lgkmcnt(0)\n"
    "s_barrier\n"
    "v_mfma_f32_16x16x32_bf16 v[24:27], a[192:195], v[64:67], v[24:27]\n"
    "s_lshl_b32 s85, s2, 7\n"
    "s_add_u32 s85, s85, 335872\n"
    "v_mfma_f32_16x16x32_bf16 v[24:27], a[208:211], v[68:71], v[24:27]\n"
    "ds_read_b128 v[40:43], v4 offset:0\n"
    "v_mul_f32 v99, v99, v97\n"
    "v_mfma_f32_16x16x32_bf16 v[24:27], a[224:227], v[72:75], v[24:27]\n"
    "buffer_store_dwordx4 v[218:221], v215, s[20:23], s85 offen sc1\n"
    "s_add_u32 s77, s71, 0\n"
    "v_mfma_f32_16x16x32_bf16 v[24:27], a[240:243], v[76:79], v[24:27]\n"
    "s_add_u32 m0, s70, 36864\n"
    "v_mfma_f32_16x16x32_bf16 v[24:27], v[224:227], v[80:83], v[24:27]\n"
    "ds_read_b128 v[44:47], v4 offset:1024\n"
    "v_cvt_pk_bf16_f32 v90, v98, v99\n"
    "v_mfma_f32_16x16x32_bf16 v[24:27], v[240:243], v[84:87], v[24:27]\n"
    "buffer_load_dwordx4 v4, s[20:23], s77 offen offset:0 sc1 lds\n"
    "v_mul_f32 v100, v100, v18\n"
    "v_mfma_f32_16x16x32_bf16 v[28:31], a[196:199], v[64:67], v[28:31]\n"
    "v_mul_f32 v101, v101, v19\n"
    "v_mfma_f32_16x16x32_bf16 v[28:31], a[212:215], v[68:71], v[28:31]\n"
    "ds_read_b128 v[48:51], v4 offset:2048\n"
    "v_mfma_f32_16x16x32_bf16 v[28:31], a[228:231], v[72:75], v[28:31]\n"
    "v_exp_f32 v100, -v100\n"
    "v_mfma_f32_16x16x32_bf16 v[28:31], a[244:247], v[76:79], v[28:31]\n"
    "v_exp_f32 v101, -v101\n"
    "v_mfma_f32_16x16x32_bf16 v[28:31], v[228:231], v[80:83], v[28:31]\n"
    "ds_read_b128 v[52:55], v4 offset:3072\n"
    "LS_6:\n"
    "s_cbranch_execz LS_10\n"
    "v_mul_f32 v102, v18, v22\n"
    "v_mfma_f32_16x16x32_bf16 v[28:31], v[244:247], v[84:87], v[28:31]\n"
    "buffer_load_dwordx4 v4, s[20:23], s77 offen offset:1024 sc1 lds\n"
    "v_mfma_f32_16x16x32_bf16 v[32:35], a[200:203], v[64:67], v[32:35]\n"
    "v_mul_f32 v103, v19, v23\n"
    "v_add_f32 v100, 1.0, v100\n"
    "v_mfma_f32_16x16x32_bf16 v[32:35], a[216:219], v[68:71], v[32:35]\n"
    "ds_read_b128 v[56:59], v4 offset:4096\n"
    "v_add_f32 v101, 1.0, v101\n"
    "v_mfma_f32_16x16x32_bf16 v[32:35], a[232:235], v[72:75], v[32:35]\n"
    "v_mfma_f32_16x16x32_bf16 v[32:35], a[248:251], v[76:79], v[32:35]\n"
    "v_rcp_f32 v100, v100\n"
    "v_mfma_f32_16x16x32_bf16 v[32:35], v[232:235], v[80:83], v[32:35]\n"
    "ds_read_b128 v[60:63], v4 offset:5120\n"
    "v_mfma_f32_16x16x32_bf16 v[32:35], v[248:251], v[84:87], v[32:35]\n"
    "buffer_load_dwordx4 v4, s[20:23], s77 offen offset:2048 sc1 lds\n"
    "v_mfma_f32_16x16x32_bf16 v[36:39], a[204:207], v[64:67], v[36:39]\n"
    "v_rcp_f32 v101, v101\n"
    "v_mfma_f32_16x16x32_bf16 v[36:39], a[220:223], v[68:71], v[36:39]\n"
    "s_nop 0\n"
    "v_mul_f32 v102, v102, v100\n"
    "v_mfma_f32_16x16x32_bf16 v[36:39], a[236:239], v[72:75], v[36:39]\n"
    "v_mul_f32 v103, v103, v101\n"
    "v_cvt_pk_bf16_f32 v91, v102, v103\n"
    "v_mfma_f32_16x16x32_bf16 v[36:39], a[252:255], v[76:79], v[36:39]\n"
    "v_mfma_f32_16x16x32_bf16 v[36:39], v[236:239], v[80:83], v[36:39]\n"
    "buffer_load_dwordx4 v4, s[20:23], s77 offen offset:3072 sc1 lds\n"
    "v_mfma_f32_16x16x32_bf16 v[36:39], v[252:255], v[84:87], v[36:39]\n"
    "s_waitcnt lgkmcnt(0)\n"
    "v_mfma_f32_16x16x32_bf16 v[8:11], a[0:3], v[40:43], 0\n"
    "v_fma_f32 v96, v24, v24, v112\n"
    "v_fma_f32 v97, v25, v25, v112\n"
    "v_mfma_f32_16x16x32_bf16 v[8:11], a[16:19], v[44:47], v[8:11]\n"
    "ds_read_b128 v[64:67], v4 offset:6144\n"
    "v_mul_f32 v96, v96, v24\n"
    "v_mfma_f32_16x16x32_bf16 v[8:11], a[32:35], v[48:51], v[8:11]\n"
    "s_add_u32 s77, s71, 4096\n"
    "s_add_u32 m0, s70, 40960\n"
    "v_mfma_f32_16x16x32_bf16 v[8:11], a[48:51], v[52:55], v[8:11]\n"
    "buffer_load_dwordx4 v4, s[20:23], s77 offen offset:0 sc1 lds\n"
    "v_mul_f32 v97, v97, v25\n"
    "v_mfma_f32_16x16x32_bf16 v[8:11], a[64:67], v[56:59], v[8:11]\n"
    "ds_read_b128 v[68:71], v4 offset:7168\n"
    "v_mfma_f32_16x16x32_bf16 v[8:11], a[80:83], v[60:63], v[8:11]\n"
    "v_exp_f32 v96, -v96\n"
    "v_mfma_f32_16x16x32_bf16 v[12:15], a[4:7], v[40:43], 0\n"
    "v_exp_f32 v97, -v97\n"
    "v_mfma_f32_16x16x32_bf16 v[12:15], a[20:23], v[44:47], v[12:15]\n"
    "ds_read_b128 v[72:75], v4 offset:8192\n"
    "v_mul_f32 v98, v24, v28\n"
    "v_mfma_f32_16x16x32_bf16 v[12:15], a[36:39], v[48:51], v[12:15]\n"
    "buffer_load_dwordx4 v4, s[20:23], s77 offen offset:1024 sc1 lds\n"
    "v_mul_f32 v99, v25, v29\n"
    "v_mfma_f32_16x16x32_bf16 v[12:15], a[52:55], v[52:55], v[12:15]\n"
    "v_add_f32 v96, 1.0, v96\n"
    "v_add_f32 v97, 1.0, v97\n"
    "v_mfma_f32_16x16x32_bf16 v[12:15], a[68:71], v[56:59], v[12:15]\n"
    "ds_read_b128 v[76:79], v4 offset:9216\n"
    "v_mfma_f32_16x16x32_bf16 v[12:15], a[84:87], v[60:63], v[12:15]\n"
    "v_rcp_f32 v96, v96\n"
    "v_mfma_f32_16x16x32_bf16 v[16:19], a[8:11], v[40:43], 0\n"
    "v_rcp_f32 v97, v97\n"
    "v_mfma_f32_16x16x32_bf16 v[16:19], a[24:27], v[44:47], v[16:19]\n"
    "ds_read_b128 v[80:83], v4 offset:10240\n"
    "v_mfma_f32_16x16x32_bf16 v[16:19], a[40:43], v[48:51], v[16:19]\n"
    "buffer_load_dwordx4 v4, s[20:23], s77 offen offset:2048 sc1 lds\n"
    "v_fma_f32 v100, v26, v26, v112\n"
    "v_mfma_f32_16x16x32_bf16 v[16:19], a[56:59], v[52:55], v[16:19]\n"
    "v_fma_f32 v101, v27, v27, v112\n"
    "v_mul_f32 v98, v98, v96\n"
    "v_mfma_f32_16x16x32_bf16 v[16:19], a[72:75], v[56:59], v[16:19]\n"
    "ds_read_b128 v[84:87], v4 offset:11264\n"
    "v_mfma_f32_16x16x32_bf16 v[16:19], a[88:91], v[60:63], v[16:19]\n"
    "v_mul_f32 v99, v99, v97\n"
    "v_cvt_pk_bf16_f32 v92, v98, v99\n"
    "v_mfma_f32_16x16x32_bf16 v[20:23], a[12:15], v[40:43], 0\n"
    "v_mul_f32 v100, v100, v26\n"
    "v_mfma_f32_16x16x32_bf16 v[20:23], a[28:31], v[44:47], v[20:23]\n"
    "buffer_load_dwordx4 v4, s[20:23], s77 offen offset:3072 sc1 lds\n"
    "s_add_u32 s77, s71, 8192\n"
    "v_mfma_f32_16x16x32_bf16 v[20:23], a[44:47], v[48:51], v[20:23]\n"
    "s_add_u32 m0, s70, 45056\n"
    "v_mul_f32 v101, v101, v27\n"
    "v_mfma_f32_16x16x32_bf16 v[20:23], a[60:63], v[52:55], v[20:23]\n"
    "v_exp_f32 v100, -v100\n"
    "v_mfma_f32_16x16x32_bf16 v[20:23], a[76:79], v[56:59], v[20:23]\n"
    "v_mfma_f32_16x16x32_bf16 v[20:23], a[92:95], v[60:63], v[20:23]\n"
    "s_waitcnt lgkmcnt(0)\n"
    "v_mfma_f32_16x16x32_bf16 v[8:11], a[96:99], v[64:67], v[8:11]\n"
    "buffer_load_dwordx4 v4, s[20:23], s77 offen offset:0 sc1 lds\n"
    "v_readlane_b32 s71, v214, 6\n"
    "v_mfma_f32_16x16x32_bf16 v[8:11], a[112:115], v[68:71], v[8:11]\n"
    "ds_read_b128 v[40:43], v4 offset:12288\n"
    "v_mfma_f32_16x16x32_bf16 v[8:11], a[128:131], v[72:75], v[8:11]\n"
    "v_exp_f32 v101, -v101\n"
    "v_mfma_f32_16x16x32_bf16 v[8:11], a[144:147], v[76:79], v[8:11]\n"
    "v_mul_f32 v102, v26, v30\n"
    "v_mul_f32 v103, v27, v31\n"
    "v_mfma_f32_16x16x32_bf16 v[8:11], a[160:163], v[80:83], v[8:11]\n"
    "ds_read_b128 v[44:47], v4 offset:13312\n"
    "v_add_f32 v100, 1.0, v100\n"
    "v_mfma_f32_16x16x32_bf16 v[8:11], a[176:179], v[84:87], v[8:11]\n"
    "v_add_f32 v101, 1.0, v101\n"
    "v_mfma_f32_16x16x32_bf16 v[12:15], a[100:103], v[64:67], v[12:15]\n"
    "buffer_store_dwordx4 v[88:91], v4, s[24:27], s74 offen sc1\n"
    "v_readlane_b32 s74, v214, 24\n"
    "v_mfma_f32_16x16x32_bf16 v[12:15], a[116:119], v[68:71], v[12:15]\n"
    "ds_read_b128 v[48:51], v4 offset:14336\n"
    "v_mfma_f32_16x16x32_bf16 v[12:15], a[132:135], v[72:75], v[12:15]\n"
    "s_add_u32 s50, s50, LM_end-32768-LG1_e0\n"
    "s_addc_u32 s51, s51, 0\n"
    "v_mfma_f32_16x16x32_bf16 v[12:15], a[148:151], v[76:79], v[12:15]\n"
    "global_load_dword v124, v123, s[50:51]\n"
    "v_mfma_f32_16x16x32_bf16 v[12:15], a[164:167], v[80:83], v[12:15]\n"
    "ds_read_b128 v[52:55], v4 offset:15360\n"
    "v_mfma_f32_16x16x32_bf16 v[12:15], a[180:183], v[84:87], v[12:15]\n"
    "v_rcp_f32 v100, v100\n"
    "v_mfma_f32_16x16x32_bf16 v[16:19], a[104:107], v[64:67], v[16:19]\n"
    "v_rcp_f32 v101, v101\n"
    "v_mfma_f32_16x16x32_bf16 v[16:19], a[120:123], v[68:71], v[16:19]\n"
    "ds_read_b128 v[56:59], v4 offset:16384\n"
    "v_fma_f32 v96, v32, v32, v112\n"
    "v_mfma_f32_16x16x32_bf16 v[16:19], a[136:139], v[72:75], v[16:19]\n"
    "v_fma_f32 v97, v33, v33, v112\n"
    "v_mul_f32 v102, v102, v100\n"
    "v_mfma_f32_16x16x32_bf16 v[16:19], a[152:155], v[76:79], v[16:19]\n"
    "v_mul_f32 v103, v103, v101\n"
    "v_cvt_pk_bf16_f32 v93, v102, v103\n"
    "v_mfma_f32_16x16x32_bf16 v[16:19], a[168:171], v[80:83], v[16:19]\n"
    "ds_read_b128 v[60:63], v4 offset:17408\n"
    "v_mul_f32 v96, v96, v32\n"
    "v_mfma_f32_16x16x32_bf16 v[16:19], a[184:187], v[84:87], v[16:19]\n"
    "v_mul_f32 v97, v97, v33\n"
    "v_mfma_f32_16x16x32_bf16 v[20:23], a[108:111], v[64:67], v[20:23]\n"
    "v_exp_f32 v96, -v96\n"
    "v_mfma_f32_16x16x32_bf16 v[20:23], a[124:127], v[68:71], v[20:23]\n"
    "v_exp_f32 v97, -v97\n"
    "v_mfma_f32_16x16x32_bf16 v[20:23], a[140:143], v[72:75], v[20:23]\n"
    "v_mul_f32 v98, v32, v36\n"
    "v_mul_f32 v99, v33, v37\n"
    "v_mfma_f32_16x16x32_bf16 v[20:23], a[156:159], v[76:79], v[20:23]\n"
    "v_add_f32 v96, 1.0, v96\n"
    "v_mfma_f32_16x16x32_bf16 v[20:23], a[172:175], v[80:83], v[20:23]\n"
    "v_add_f32 v97, 1.0, v97\n"
    "v_mfma_f32_16x16x32_bf16 v[20:23], a[188:191], v[84:87], v[20:23]\n"
    "s_waitcnt lgkmcnt(0)\n"
    "v_mfma_f32_16x16x32_bf16 v[8:11], a[192:195], v[40:43], v[8:11]\n"
    "v_rcp_f32 v96, v96\n"
    "v_mfma_f32_16x16x32_bf16 v[8:11], a[208:211], v[44:47], v[8:11]\n"
    "ds_read_b128 v[64:67], v4 offset:18432\n"
    "v_mfma_f32_16x16x32_bf16 v[8:11], a[224:227], v[48:51], v[8:11]\n"
    "buffer_store_dwordx4 v[116:119], v4, s[20:23], s13 offen offset:0 sc1\n"
    "v_mfma_f32_16x16x32_bf16 v[8:11], a[240:243], v[52:55], v[8:11]\n"
    "v_rcp_f32 v97, v97\n"
    "v_mfma_f32_16x16x32_bf16 v[8:11], v[224:227], v[56:59], v[8:11]\n"
    "ds_read_b128 v[68:71], v4 offset:19456\n"
    "v_fma_f32 v100, v34, v34, v112\n"
    "v_mfma_f32_16x16x32_bf16 v[8:11], v[240:243], v[60:63], v[8:11]\n"
    "v_fma_f32 v101, v35, v35, v112\n"
    "v_mul_f32 v98, v98, v96\n"
    "v_mfma_f32_16x16x32_bf16 v[12:15], a[196:199], v[40:43], v[12:15]\n"
    "v_mul_f32 v99, v99, v97\n"
    "v_cvt_pk_bf16_f32 v94, v98, v99\n"
    "v_mfma_f32_16x16x32_bf16 v[12:15], a[212:215], v[44:47], v[12:15]\n"
    "ds_read_b128 v[72:75], v4 offset:20480\n"
    "v_mul_f32 v100, v100, v34\n"
    "v_mfma_f32_16x16x32_bf16 v[12:15], a[228:231], v[48:51], v[12:15]\n"
    "v_mul_f32 v101, v101, v35\n"
    "v_mfma_f32_16x16x32_bf16 v[12:15], a[244:247], v[52:55], v[12:15]\n"
    "v_exp_f32 v100, -v100\n"
    "v_mfma_f32_16x16x32_bf16 v[12:15], v[228:231], v[56:59], v[12:15]\n"
    "ds_read_b128 v[76:79], v4 offset:21504\n"
    "v_mfma_f32_16x16x32_bf16 v[12:15], v[244:247], v[60:63], v[12:15]\n"
    "v_exp_f32 v101, -v101\n"
    "v_mfma_f32_16x16x32_bf16 v[16:19], a[200:203], v[40:43], v[16:19]\n"
    "v_mul_f32 v102, v34, v38\n"
    "v_mul_f32 v103, v35, v39\n"
    "v_mfma_f32_16x16x32_bf16 v[16:19], a[216:219], v[44:47], v[16:19]\n"
    "ds_read_b128 v[80:83], v4 offset:22528\n"
    "v_add_f32 v100, 1.0, v100\n"
    "v_mfma_f32_16x16x32_bf16 v[16:19], a[232:235], v[48:51], v[16:19]\n"
    "v_add_f32 v101, 1.0, v101\n"
    "v_mfma_f32_16x16x32_bf16 v[16:19], a[248:251], v[52:55], v[16:19]\n"
    "v_rcp_f32 v100, v100\n"
    "v_mfma_f32_16x16x32_bf16 v[16:19], v[232:235], v[56:59], v[16:19]\n"
    "ds_read_b128 v[84:87], v4 offset:23552\n"
    "v_mfma_f32_16x16x32_bf16 v[16:19], v[248:251], v[60:63], v[16:19]\n"
    "v_rcp_f32 v101, v101\n"
    "v_mfma_f32_16x16x32_bf16 v[20:23], a[204:207], v[40:43], v[20:23]\n"
    "s_nop 0\n"
    "v_mfma_f32_16x16x32_bf16 v[20:23], a[220:223], v[44:47], v[20:23]\n"
    "v_mul_f32 v102, v102, v100\n"
    "v_mul_f32 v103, v103, v101\n"
    "v_mfma_f32_16x16x32_bf16 v[20:23], a[236:239], v[48:51], v[20:23]\n"
    "v_cvt_pk_bf16_f32 v95, v102, v103\n"
    "v_mfma_f32_16x16x32_bf16 v[20:23], a[252:255], v[52:55], v[20:23]\n"
    "v_mfma_f32_16x16x32_bf16 v[20:23], v[236:239], v[56:59], v[20:23]\n"
    "buffer_store_dwordx4 v[116:119], v4, s[20:23], s13 offen offset:1024 sc1\n"
    "v_mfma_f32_16x16x32_bf16 v[20:23], v[252:255], v[60:63], v[20:23]\n"
    "buffer_store_dwordx4 v[92:95], v4, s[24:27], s74 offen sc1\n"
    "s_waitcnt lgkmcnt(0)\n"
    "v_mfma_f32_16x16x32_bf16 v[24:27], a[0:3], v[64:67], 0\n"
    "v_readlane_b32 s74, v214, 25\n"
    "v_fma_f32 v96, v8, v8, v112\n"
    "v_mfma_f32_16x16x32_bf16 v[24:27], a[16:19], v[68:71], v[24:27]\n"
    "ds_read_b128 v[40:43], v4 offset:24576\n"
    "v_fma_f32 v97, v9, v9, v112\n"
    "v_mfma_f32_16x16x32_bf16 v[24:27], a[32:35], v[72:75], v[24:27]\n"
    "v_mul_f32 v96, v96, v8\n"
    "v_mul_f32 v97, v97, v9\n"
    "v_mfma_f32_16x16x32_bf16 v[24:27], a[48:51], v[76:79], v[24:27]\n"
    "v_exp_f32 v96, -v96\n"
    "v_mfma_f32_16x16x32_bf16 v[24:27], a[64:67], v[80:83], v[24:27]\n"
    "ds_read_b128 v[44:47], v4 offset:25600\n"
    "v_mfma_f32_16x16x32_bf16 v[24:27], a[80:83], v[84:87], v[24:27]\n"
    "v_exp_f32 v97, -v97\n"
    "v_mfma_f32_16x16x32_bf16 v[28:31], a[4:7], v[64:67], 0\n"
    "v_mul_f32 v98, v8, v12\n"
    "v_mul_f32 v99, v9, v13\n"
    "v_mfma_f32_16x16x32_bf16 v[28:31], a[20:23], v[68:71], v[28:31]\n"
    "ds_read_b128 v[48:51], v4 offset:26624\n"
    "v_add_f32 v96, 1.0, v96\n"
    "v_mfma_f32_16x16x32_bf16 v[28:31], a[36:39], v[72:75], v[28:31]\n"
    "v_add_f32 v97, 1.0, v97\n"
    "v_mfma_f32_16x16x32_bf16 v[28:31], a[52:55], v[76:79], v[28:31]\n"
    "v_rcp_f32 v96, v96\n"
    "v_mfma_f32_16x16x32_bf16 v[28:31], a[68:71], v[80:83], v[28:31]\n"
    "ds_read_b128 v[52:55], v4 offset:27648\n"
    "v_mfma_f32_16x16x32_bf16 v[28:31], a[84:87], v[84:87], v[28:31]\n"
    "v_rcp_f32 v97, v97\n"
    "v_mfma_f32_16x16x32_bf16 v[32:35], a[8:11], v[64:67], 0\n"
    "v_fma_f32 v100, v10, v10, v112\n"
    "v_fma_f32 v101, v11, v11, v112\n"
    "v_mfma_f32_16x16x32_bf16 v[32:35], a[24:27], v[68:71], v[32:35]\n"
    "ds_read_b128 v[56:59], v4 offset:28672\n"
    "v_mfma_f32_16x16x32_bf16 v[32:35], a[40:43], v[72:75], v[32:35]\n"
    "v_mul_f32 v98, v98, v96\n"
    "v_mul_f32 v99, v99, v97\n"
    "v_mfma_f32_16x16x32_bf16 v[32:35], a[56:59], v[76:79], v[32:35]\n"
    "v_cvt_pk_bf16_f32 v88, v98, v99\n"
    "v_mfma_f32_16x16x32_bf16 v[32:35], a[72:75], v[80:83], v[32:35]\n"
    "ds_read_b128 v[60:63], v4 offset:29696\n"
    "v_mul_f32 v100, v100, v10\n"
    "v_mfma_f32_16x16x32_bf16 v[32:35], a[88:91], v[84:87], v[32:35]\n"
    "v_mul_f32 v101, v101, v11\n"
    "v_mfma_f32_16x16x32_bf16 v[36:39], a[12:15], v[64:67], 0\n"
    "buffer_store_dwordx4 v[116:119], v4, s[20:23], s13 offen offset:2048 sc1\n"
    "v_mfma_f32_16x16x32_bf16 v[36:39], a[28:31], v[68:71], v[36:39]\n"
    "v_exp_f32 v100, -v100\n"
    "v_mfma_f32_16x16x32_bf16 v[36:39], a[44:47], v[72:75], v[36:39]\n"
    "v_exp_f32 v101, -v101\n"
    "v_mfma_f32_16x16x32_bf16 v[36:39], a[60:63], v[76:79], v[36:39]\n"
    "v_mul_f32 v102, v10, v14\n"
    "v_mfma_f32_16x16x32_bf16 v[36:39], a[76:79], v[80:83], v[36:39]\n"
    "v_mul_f32 v103, v11, v15\n"
    "v_add_f32 v100, 1.0, v100\n"
    "v_mfma_f32_16x16x32_bf16 v[36:39], a[92:95], v[84:87], v[36:39]\n"
    "v_add_f32 v101, 1.0, v101\n"
    "s_waitcnt lgkmcnt(0)\n"
    "v_mfma_f32_16x16x32_bf16 v[24:27], a[96:99], v[40:43], v[24:27]\n"
    "v_mfma_f32_16x16x32_bf16 v[24:27], a[112:115], v[44:47], v[24:27]\n"
    "ds_read_b128 v[64:67], v4 offset:30720\n"
    "v_mfma_f32_16x16x32_bf16 v[24:27], a[128:131], v[48:51], v[24:27]\n"
    "v_rcp_f32 v100, v100\n"
    "v_mfma_f32_16x16x32_bf16 v[24:27], a[144:147], v[52:55], v[24:27]\n"
    "v_rcp_f32 v101, v101\n"
    "v_mfma_f32_16x16x32_bf16 v[24:27], a[160:163], v[56:59], v[24:27]\n"
    "ds_read_b128 v[68:71], v4 offset:31744\n"
    "v_fma_f32 v96, v16, v16, v112\n"
    "v_mfma_f32_16x16x32_bf16 v[24:27], a[176:179], v[60:63], v[24:27]\n"
    "v_fma_f32 v97, v17, v17, v112\n"
    "v_mul_f32 v102, v102, v100\n"
    "v_mfma_f32_16x16x32_bf16 v[28:31], a[100:103], v[40:43], v[28:31]\n"
    "v_mul_f32 v103, v103, v101\n"
    "v_mfma_f32_16x16x32_bf16 v[28:31], a[116:119], v[44:47], v[28:31]\n"
    "ds_read_b128 v[72:75], v4 offset:32768\n"
    "v_cvt_pk_bf16_f32 v89, v102, v103\n"
    "v_mfma_f32_16x16x32_bf16 v[28:31], a[132:135], v[48:51], v[28:31]\n"
    "v_mul_f32 v96, v96, v16\n"
    "v_mfma_f32_16x16x32_bf16 v[28:31], a[148:151], v[52:55], v[28:31]\n"
    "v_mul_f32 v97, v97, v17\n"
    "v_mfma_f32_16x16x32_bf16 v[28:31], a[164:167], v[56:59], v[28:31]\n"
    "ds_read_b128 v[76:79], v4 offset:33792\n"
    "v_mfma_f32_16x16x32_bf16 v[28:31], a[180:183], v[60:63], v[28:31]\n"
    "v_exp_f32 v96, -v96\n"
    "v_mfma_f32_16x16x32_bf16 v[32:35], a[104:107], v[40:43], v[32:35]\n"
    "v_exp_f32 v97, -v97\n"
    "v_mfma_f32_16x16x32_bf16 v[32:35], a[120:123], v[44:47], v[32:35]\n"
    "ds_read_b128 v[80:83], v4 offset:34816\n"
    "v_mul_f32 v98, v16, v20\n"
    "v_mfma_f32_16x16x32_bf16 v[32:35], a[136:139], v[48:51], v[32:35]\n"
    "buffer_store_dwordx4 v[116:119], v4, s[20:23], s13 offen offset:3072 sc1\n"
    "v_mul_f32 v99, v17, v21\n"
    "v_mfma_f32_16x16x32_bf16 v[32:35], a[152:155], v[52:55], v[32:35]\n"
    "v_add_f32 v96, 1.0, v96\n"
    "v_mfma_f32_16x16x32_bf16 v[32:35], a[168:171], v[56:59], v[32:35]\n"
    "ds_read_b128 v[84:87], v4 offset:35840\n"
    "v_add_f32 v97, 1.0, v97\n"
    "v_mfma_f32_16x16x32_bf16 v[32:35], a[184:187], v[60:63], v[32:35]\n"
    "v_mfma_f32_16x16x32_bf16 v[36:39], a[108:111], v[40:43], v[36:39]\n"
    "v_rcp_f32 v96, v96\n"
    "v_mfma_f32_16x16x32_bf16 v[36:39], a[124:127], v[44:47], v[36:39]\n"
    "v_rcp_f32 v97, v97\n"
    "v_mfma_f32_16x16x32_bf16 v[36:39], a[140:143], v[48:51], v[36:39]\n"
    "v_fma_f32 v100, v18, v18, v112\n"
    "v_fma_f32 v101, v19, v19, v112\n"
    "v_mfma_f32_16x16x32_bf16 v[36:39], a[156:159], v[52:55], v[36:39]\n"
    "v_mul_f32 v98, v98, v96\n"
    "v_mfma_f32_16x16x32_bf16 v[36:39], a[172:175], v[56:59], v[36:39]\n"
    "v_mul_f32 v99, v99, v97\n"
    "v_cvt_pk_bf16_f32 v90, v98, v99\n"
    "v_mfma_f32_16x16x32_bf16 v[36:39], a[188:191], v[60:63], v[36:39]\n"
    "s_waitcnt vmcnt(7) lgkmcnt(0)\n"
    "s_barrier\n"
    "v_mfma_f32_16x16x32_bf16 v[24:27], a[192:195], v[64:67], v[24:27]\n"
    "s_mul_i32 s78, s34, 7\n"
    "s_add_u32 s33, s78, s35\n"
    "v_mfma_f32_16x16x32_bf16 v[24:27], a[208:211], v[68:71], v[24:27]\n"
    "ds_read_b128 v[40:43], v120 offset:0\n"
    "s_lshl_b32 s85, s33, 7\n"
    "v_mfma_f32_16x16x32_bf16 v[24:27], a[224:227], v[72:75], v[24:27]\n"
    "s_add_u32 s85, s85, 303104\n"
    "s_add_u32 s77, s71, 0\n"
    "v_mfma_f32_16x16x32_bf16 v[24:27], a[240:243], v[76:79], v[24:27]\n"
    "buffer_store_dwordx4 v[218:221], v215, s[20:23], s85 offen sc1\n"
    "s_add_u32 m0, s70, 0\n"
    "v_mfma_f32_16x16x32_bf16 v[24:27], v[224:227], v[80:83], v[24:27]\n"
    "ds_read_b128 v[44:47], v120 offset:1024\n"
    "v_mfma_f32_16x16x32_bf16 v[24:27], v[240:243], v[84:87], v[24:27]\n"
    "buffer_load_dwordx4 v4, s[20:23], s77 offen offset:0 sc1 lds\n"
    "v_mul_f32 v100, v100, v18\n"
    "v_mfma_f32_16x16x32_bf16 v[28:31], a[196:199], v[64:67], v[28:31]\n"
    "v_mul_f32 v101, v101, v19\n"
    "v_mfma_f32_16x16x32_bf16 v[28:31], a[212:215], v[68:71], v[28:31]\n"
    "ds_read_b128 v[48:51], v120 offset:2048\n"
    "v_mfma_f32_16x16x32_bf16 v[28:31], a[228:231], v[72:75], v[28:31]\n"
    "v_exp_f32 v100, -v100\n"
    "v_mfma_f32_16x16x32_bf16 v[28:31], a[244:247], v[76:79], v[28:31]\n"
    "v_exp_f32 v101, -v101\n"
    "v_mfma_f32_16x16x32_bf16 v[28:31], v[228:231], v[80:83], v[28:31]\n"
    "ds_read_b128 v[52:55], v120 offset:3072\n"
    "v_mul_f32 v102, v18, v22\n"
    "v_mfma_f32_16x16x32_bf16 v[28:31], v[244:247], v[84:87], v[28:31]\n"
    "buffer_load_dwordx4 v4, s[20:23], s77 offen offset:1024 sc1 lds\n"
    "v_mul_f32 v103, v19, v23\n"
    "v_mfma_f32_16x16x32_bf16 v[32:35], a[200:203], v[64:67], v[32:35]\n"
    "v_add_f32 v100, 1.0, v100\n"
    "v_mfma_f32_16x16x32_bf16 v[32:35], a[216:219], v[68:71], v[32:35]\n"
    "ds_read_b128 v[56:59], v120 offset:4096\n"
    "v_add_f32 v101, 1.0, v101\n"
    "v_mfma_f32_16x16x32_bf16 v[32:35], a[232:235], v[72:75], v[32:35]\n"
    "v_mfma_f32_16x16x32_bf16 v[32:35], a[248:251], v[76:79], v[32:35]\n"
    "v_rcp_f32 v100, v100\n"
    "v_mfma_f32_16x16x32_bf16 v[32:35], v[232:235], v[80:83], v[32:35]\n"
    "ds_read_b128 v[60:63], v120 offset:5120\n"
    "v_mfma_f32_16x16x32_bf16 v[32:35], v[248:251], v[84:87], v[32:35]\n"
    "buffer_load_dwordx4 v4, s[20:23], s77 offen offset:2048 sc1 lds\n"
    "v_mfma_f32_16x16x32_bf16 v[36:39], a[204:207], v[64:67], v[36:39]\n"
    "v_rcp_f32 v101, v101\n"
    "v_mfma_f32_16x16x32_bf16 v[36:39], a[220:223], v[68:71], v[36:39]\n"
    "s_nop 0\n"
    "v_mul_f32 v102, v102, v100\n"
    "v_mfma_f32_16x16x32_bf16 v[36:39], a[236:239], v[72:75], v[36:39]\n"
    "v_mul_f32 v103, v103, v101\n"
    "v_cvt_pk_bf16_f32 v91, v102, v103\n"
    "v_mfma_f32_16x16x32_bf16 v[36:39], a[252:255], v[76:79], v[36:39]\n"
    "v_mfma_f32_16x16x32_bf16 v[36:39], v[236:239], v[80:83], v[36:39]\n"
    "buffer_load_dwordx4 v4, s[20:23], s77 offen offset:3072 sc1 lds\n"
    "v_mfma_f32_16x16x32_bf16 v[36:39], v[252:255], v[84:87], v[36:39]\n"
    "s_waitcnt lgkmcnt(0)\n"
    "v_mfma_f32_16x16x32_bf16 v[8:11], a[0:3], v[40:43], 0\n"
    "v_fma_f32 v96, v24, v24, v112\n"
    "v_fma_f32 v97, v25, v25, v112\n"
    "v_mfma_f32_16x16x32_bf16 v[8:11], a[16:19], v[44:47], v[8:11]\n"
    "ds_read_b128 v[64:67], v120 offset:6144\n"
    "v_mul_f32 v96, v96, v24\n"
    "v_mfma_f32_16x16x32_bf16 v[8:11], a[32:35], v[48:51], v[8:11]\n"
    "s_add_u32 s77, s71, 4096\n"
    "s_add_u32 m0, s70, 4096\n"
    "v_mfma_f32_16x16x32_bf16 v[8:11], a[48:51], v[52:55], v[8:11]\n"
    "buffer_load_dwordx4 v4, s[20:23], s77 offen offset:0 sc1 lds\n"
    "v_mul_f32 v97, v97, v25\n"
    "v_mfma_f32_16x16x32_bf16 v[8:11], a[64:67], v[56:59], v[8:11]\n"
    "ds_read_b128 v[68:71], v120 offset:7168\n"
    "v_mfma_f32_16x16x32_bf16 v[8:11], a[80:83], v[60:63], v[8:11]\n"
    "v_exp_f32 v96, -v96\n"
    "v_mfma_f32_16x16x32_bf16 v[12:15], a[4:7], v[40:43], 0\n"
    "v_exp_f32 v97, -v97\n"
    "v_mfma_f32_16x16x32_bf16 v[12:15], a[20:23], v[44:47], v[12:15]\n"
    "ds_read_b128 v[72:75], v120 offset:8192\n"
    "v_mul_f32 v98, v24, v28\n"
    "v_mfma_f32_16x16x32_bf16 v[12:15], a[36:39], v[48:51], v[12:15]\n"
    "buffer_load_dwordx4 v4, s[20:23], s77 offen offset:1024 sc1 lds\n"
    "v_mul_f32 v99, v25, v29\n"
    "v_mfma_f32_16x16x32_bf16 v[12:15], a[52:55], v[52:55], v[12:15]\n"
    "v_add_f32 v96, 1.0, v96\n"
    "v_add_f32 v97, 1.0, v97\n"
    "v_mfma_f32_16x16x32_bf16 v[12:15], a[68:71], v[56:59], v[12:15]\n"
    "ds_read_b128 v[76:79], v120 offset:9216\n"
    "v_mfma_f32_16x16x32_bf16 v[12:15], a[84:87], v[60:63], v[12:15]\n"
    "v_rcp_f32 v96, v96\n"
    "v_mfma_f32_16x16x32_bf16 v[16:19], a[8:11], v[40:43], 0\n"
    "v_mfma_f32_16x16x32_bf16 v[16:19], a[24:27], v[44:47], v[16:19]\n"
    "ds_read_b128 v[80:83], v120 offset:10240\n"
    "v_mfma_f32_16x16x32_bf16 v[16:19], a[40:43], v[48:51], v[16:19]\n"
    "buffer_load_dwordx4 v4, s[20:23], s77 offen offset:2048 sc1 lds\n"
    "v_mfma_f32_16x16x32_bf16 v[16:19], a[56:59], v[52:55], v[16:19]\n"
    "v_rcp_f32 v97, v97\n"
    "v_mfma_f32_16x16x32_bf16 v[16:19], a[72:75], v[56:59], v[16:19]\n"
    "ds_read_b128 v[84:87], v120 offset:11264\n"
    "v_fma_f32 v100, v26, v26, v112\n"
    "v_mfma_f32_16x16x32_bf16 v[16:19], a[88:91], v[60:63], v[16:19]\n"
    "v_fma_f32 v101, v27, v27, v112\n"
    "v_mul_f32 v98, v98, v96\n"
    "v_mfma_f32_16x16x32_bf16 v[20:23], a[12:15], v[40:43], 0\n"
    "v_mul_f32 v99, v99, v97\n"
    "v_cvt_pk_bf16_f32 v92, v98, v99\n"
    "v_mfma_f32_16x16x32_bf16 v[20:23], a[28:31], v[44:47], v[20:23]\n"
    "buffer_load_dwordx4 v4, s[20:23], s77 offen offset:3072 sc1 lds\n"
    "s_add_u32 s77, s71, 8192\n"
    "v_mfma_f32_16x16x32_bf16 v[20:23], a[44:47], v[48:51], v[20:23]\n"
    "s_add_u32 m0, s70, 8192\n"
    "v_mul_f32 v100, v100, v26\n"
    "v_mfma_f32_16x16x32_bf16 v[20:23], a[60:63], v[52:55], v[20:23]\n"
    "v_mul_f32 v101, v101, v27\n"
    "v_mfma_f32_16x16x32_bf16 v[20:23], a[76:79], v[56:59], v[20:23]\n"
    "v_exp_f32 v100, -v100\n"
    "v_mfma_f32_16x16x32_bf16 v[20:23], a[92:95], v[60:63], v[20:23]\n"
    "s_waitcnt lgkmcnt(0)\n"
    "v_mfma_f32_16x16x32_bf16 v[8:11], a[96:99], v[64:67], v[8:11]\n"
    "buffer_load_dwordx4 v4, s[20:23], s77 offen offset:0 sc1 lds\n"
    "v_readlane_b32 s71, v214, 7\n"
    "v_mfma_f32_16x16x32_bf16 v[8:11], a[112:115], v[68:71], v[8:11]\n"
    "ds_read_b128 v[40:43], v120 offset:12288\n"
    "v_mfma_f32_16x16x32_bf16 v[8:11], a[128:131], v[72:75], v[8:11]\n"
    "v_exp_f32 v101, -v101\n"
    "v_mfma_f32_16x16x32_bf16 v[8:11], a[144:147], v[76:79], v[8:11]\n"
    "v_mul_f32 v102, v26, v30\n"
    "v_mul_f32 v103, v27, v31\n"
    "v_mfma_f32_16x16x32_bf16 v[8:11], a[160:163], v[80:83], v[8:11]\n"
    "ds_read_b128 v[44:47], v120 offset:13312\n"
    "v_add_f32 v100, 1.0, v100\n"
    "v_mfma_f32_16x16x32_bf16 v[8:11], a[176:179], v[84:87], v[8:11]\n"
    "v_add_f32 v101, 1.0, v101\n"
    "v_mfma_f32_16x16x32_bf16 v[12:15], a[100:103], v[64:67], v[12:15]\n"
    "buffer_store_dwordx4 v[88:91], v4, s[24:27], s74 offen sc1\n"
    "v_readlane_b32 s74, v214, 26\n"
    "v_mfma_f32_16x16x32_bf16 v[12:15], a[116:119], v[68:71], v[12:15]\n"
    "ds_read_b128 v[48:51], v120 offset:14336\n"
    "s_mov_b32 s85, 335872\n"
    "v_mfma_f32_16x16x32_bf16 v[12:15], a[132:135], v[72:75], v[12:15]\n"
    "buffer_load_dwordx2 v[104:105], v109, s[20:23], s85 offen sc1\n"
    "v_mfma_f32_16x16x32_bf16 v[12:15], a[148:151], v[76:79], v[12:15]\n"
    "v_rcp_f32 v100, v100\n"
    "v_mfma_f32_16x16x32_bf16 v[12:15], a[164:167], v[80:83], v[12:15]\n"
    "ds_read_b128 v[52:55], v120 offset:15360\n"
    "v_mfma_f32_16x16x32_bf16 v[12:15], a[180:183], v[84:87], v[12:15]\n"
    "v_rcp_f32 v101, v101\n"
    "v_mfma_f32_16x16x32_bf16 v[16:19], a[104:107], v[64:67], v[16:19]\n"
    "v_fma_f32 v96, v32, v32, v112\n"
    "v_fma_f32 v97, v33, v33, v112\n"
    "v_mfma_f32_16x16x32_bf16 v[16:19], a[120:123], v[68:71], v[16:19]\n"
    "ds_read_b128 v[56:59], v120 offset:16384\n"
    "v_mul_f32 v102, v102, v100\n"
    "v_mfma_f32_16x16x32_bf16 v[16:19], a[136:139], v[72:75], v[16:19]\n"
    "v_mul_f32 v103, v103, v101\n"
    "v_cvt_pk_bf16_f32 v93, v102, v103\n"
    "v_mfma_f32_16x16x32_bf16 v[16:19], a[152:155], v[76:79], v[16:19]\n"
    "v_mul_f32 v96, v96, v32\n"
    "v_mfma_f32_16x16x32_bf16 v[16:19], a[168:171], v[80:83], v[16:19]\n"
    "ds_read_b128 v[60:63], v120 offset:17408\n"
    "v_mul_f32 v97, v97, v33\n"
    "v_mfma_f32_16x16x32_bf16 v[16:19], a[184:187], v[84:87], v[16:19]\n"
    "v_exp_f32 v96, -v96\n"
    "v_mfma_f32_16x16x32_bf16 v[20:23], a[108:111], v[64:67], v[20:23]\n"
    "v_mfma_f32_16x16x32_bf16 v[20:23], a[124:127], v[68:71], v[20:23]\n"
    "v_exp_f32 v97, -v97\n"
    "v_mfma_f32_16x16x32_bf16 v[20:23], a[140:143], v[72:75], v[20:23]\n"
    "v_mul_f32 v98, v32, v36\n"
    "v_mul_f32 v99, v33, v37\n"
    "v_mfma_f32_16x16x32_bf16 v[20:23], a[156:159], v[76:79], v[20:23]\n"
    "v_add_f32 v96, 1.0, v96\n"
    "v_mfma_f32_16x16x32_bf16 v[20:23], a[172:175], v[80:83], v[20:23]\n"
    "v_add_f32 v97, 1.0, v97\n"
    "v_mfma_f32_16x16x32_bf16 v[20:23], a[188:191], v[84:87], v[20:23]\n"
    "s_waitcnt lgkmcnt(0)\n"
    "v_mfma_f32_16x16x32_bf16 v[8:11], a[192:195], v[40:43], v[8:11]\n"
    "v_rcp_f32 v96, v96\n"
    "v_mfma_f32_16x16x32_bf16 v[8:11], a[208:211], v[44:47], v[8:11]\n"
    "ds_read_b128 v[64:67], v120 offset:18432\n"
    "v_mfma_f32_16x16x32_bf16 v[8:11], a[224:227], v[48:51], v[8:11]\n"
    "v_rcp_f32 v97, v97\n"
    "v_mfma_f32_16x16x32_bf16 v[8:11], a[240:243], v[52:55], v[8:11]\n"
    "v_fma_f32 v100, v34, v34, v112\n"
    "v_fma_f32 v101, v35, v35, v112\n"
    "v_mfma_f32_16x16x32_bf16 v[8:11], v[224:227], v[56:59], v[8:11]\n"
    "ds_read_b128 v[68:71], v120 offset:19456\n"
    "v_mul_f32 v98, v98, v96\n"
    "v_mfma_f32_16x16x32_bf16 v[8:11], v[240:243], v[60:63], v[8:11]\n"
    "v_mul_f32 v99, v99, v97\n"
    "v_cvt_pk_bf16_f32 v94, v98, v99\n"
    "v_mfma_f32_16x16x32_bf16 v[12:15], a[196:199], v[40:43], v[12:15]\n"
    "buffer_store_dwordx4 v[116:119], v4, s[20:23], s15 offen offset:0 sc1\n"
    "v_mfma_f32_16x16x32_bf16 v[12:15], a[212:215], v[44:47], v[12:15]\n"
    "ds_read_b128 v[72:75], v120 offset:20480\n"
    "v_mul_f32 v100, v100, v34\n"
    "v_mfma_f32_16x16x32_bf16 v[12:15], a[228:231], v[48:51], v[12:15]\n"
    "v_mul_f32 v101, v101, v35\n"
    "v_mfma_f32_16x16x32_bf16 v[12:15], a[244:247], v[52:55], v[12:15]\n"
    "v_exp_f32 v100, -v100\n"
    "v_mfma_f32_16x16x32_bf16 v[12:15], v[228:231], v[56:59], v[12:15]\n"
    "ds_read_b128 v[76:79], v120 offset:21504\n"
    "v_mfma_f32_16x16x32_bf16 v[12:15], v[244:247], v[60:63], v[12:15]\n"
    "v_exp_f32 v101, -v101\n"
    "v_mfma_f32_16x16x32_bf16 v[16:19], a[200:203], v[40:43], v[16:19]\n"
    "v_mul_f32 v102, v34, v38\n"
    "v_mul_f32 v103, v35, v39\n"
    "v_mfma_f32_16x16x32_bf16 v[16:19], a[216:219], v[44:47], v[16:19]\n"
    "ds_read_b128 v[80:83], v120 offset:22528\n"
    "v_mfma_f32_16x16x32_bf16 v[16:19], a[232:235], v[48:51], v[16:19]\n"
    "v_add_f32 v100, 1.0, v100\n"
    "v_add_f32 v101, 1.0, v101\n"
    "v_mfma_f32_16x16x32_bf16 v[16:19], a[248:251], v[52:55], v[16:19]\n"
    "v_mfma_f32_16x16x32_bf16 v[16:19], v[232:235], v[56:59], v[16:19]\n"
    "ds_read_b128 v[84:87], v120 offset:23552\n"
    "v_mfma_f32_16x16x32_bf16 v[16:19], v[248:251], v[60:63], v[16:19]\n"
    "v_rcp_f32 v100, v100\n"
    "v_mfma_f32_16x16x32_bf16 v[20:23], a[204:207], v[40:43], v[20:23]\n"
    "v_rcp_f32 v101, v101\n"
    "v_mfma_f32_16x16x32_bf16 v[20:23], a[220:223], v[44:47], v[20:23]\n"
    "s_nop 0\n"
    "v_mul_f32 v102, v102, v100\n"
    "v_mfma_f32_16x16x32_bf16 v[20:23], a[236:239], v[48:51], v[20:23]\n"
    "v_mul_f32 v103, v103, v101\n"
    "v_cvt_pk_bf16_f32 v95, v102, v103\n"
    "v_mfma_f32_16x16x32_bf16 v[20:23], a[252:255], v[52:55], v[20:23]\n"
    "v_mfma_f32_16x16x32_bf16 v[20:23], v[236:239], v[56:59], v[20:23]\n"
    "v_mfma_f32_16x16x32_bf16 v[20:23], v[252:255], v[60:63], v[20:23]\n"
    "buffer_store_dwordx4 v[92:95], v4, s[24:27], s74 offen sc1\n"
    "s_waitcnt lgkmcnt(0)\n"
    "v_mfma_f32_16x16x32_bf16 v[24:27], a[0:3], v[64:67], 0\n"
    "v_readlane_b32 s74, v214, 27\n"
    "v_fma_f32 v96, v8, v8, v112\n"
    "v_mfma_f32_16x16x32_bf16 v[24:27], a[16:19], v[68:71], v[24:27]\n"
    "ds_read_b128 v[40:43], v120 offset:24576\n"
    "v_fma_f32 v97, v9, v9, v112\n"
    "v_mfma_f32_16x16x32_bf16 v[24:27], a[32:35], v[72:75], v[24:27]\n"
    "buffer_store_dwordx4 v[116:119], v4, s[20:23], s15 offen offset:1024 sc1\n"
    "v_mul_f32 v96, v96, v8\n"
    "v_mfma_f32_16x16x32_bf16 v[24:27], a[48:51], v[76:79], v[24:27]\n"
    "v_mul_f32 v97, v97, v9\n"
    "v_mfma_f32_16x16x32_bf16 v[24:27], a[64:67], v[80:83], v[24:27]\n"
    "ds_read_b128 v[44:47], v120 offset:25600\n"
    "v_mfma_f32_16x16x32_bf16 v[24:27], a[80:83], v[84:87], v[24:27]\n"
    "v_exp_f32 v96, -v96\n"
    "v_mfma_f32_16x16x32_bf16 v[28:31], a[4:7], v[64:67], 0\n"
    "v_exp_f32 v97, -v97\n"
    "v_mfma_f32_16x16x32_bf16 v[28:31], a[20:23], v[68:71], v[28:31]\n"
    "ds_read_b128 v[48:51], v120 offset:26624\n"
    "v_mul_f32 v98, v8, v12\n"
    "v_mfma_f32_16x16x32_bf16 v[28:31], a[36:39], v[72:75], v[28:31]\n"
    "v_mul_f32 v99, v9, v13\n"
    "v_add_f32 v96, 1.0, v96\n"
    "v_mfma_f32_16x16x32_bf16 v[28:31], a[52:55], v[76:79], v[28:31]\n"
    "v_add_f32 v97, 1.0, v97\n"
    "v_mfma_f32_16x16x32_bf16 v[28:31], a[68:71], v[80:83], v[28:31]\n"
    "ds_read_b128 v[52:55], v120 offset:27648\n"
    "v_mfma_f32_16x16x32_bf16 v[28:31], a[84:87], v[84:87], v[28:31]\n"
    "v_rcp_f32 v96, v96\n"
    "v_mfma_f32_16x16x32_bf16 v[32:35], a[8:11], v[64:67], 0\n"
    "v_rcp_f32 v97, v97\n"
    "v_mfma_f32_16x16x32_bf16 v[32:35], a[24:27], v[68:71], v[32:35]\n"
    "ds_read_b128 v[56:59], v120 offset:28672\n"
    "v_fma_f32 v100, v10, v10, v112\n"
    "v_mfma_f32_16x16x32_bf16 v[32:35], a[40:43], v[72:75], v[32:35]\n"
    "v_fma_f32 v101, v11, v11, v112\n"
    "v_mul_f32 v98, v98, v96\n"
    "v_mfma_f32_16x16x32_bf16 v[32:35], a[56:59], v[76:79], v[32:35]\n"
    "v_mul_f32 v99, v99, v97\n"
    "v_cvt_pk_bf16_f32 v88, v98, v99\n"
    "v_mfma_f32_16x16x32_bf16 v[32:35], a[72:75], v[80:83], v[32:35]\n"
    "ds_read_b128 v[60:63], v120 offset:29696\n"
    "v_mul_f32 v100, v100, v10\n"
    "v_mfma_f32_16x16x32_bf16 v[32:35], a[88:91], v[84:87], v[32:35]\n"
    "v_mul_f32 v101, v101, v11\n"
    "v_mfma_f32_16x16x32_bf16 v[36:39], a[12:15], v[64:67], 0\n"
    "v_exp_f32 v100, -v100\n"
    "v_mfma_f32_16x16x32_bf16 v[36:39], a[28:31], v[68:71], v[36:39]\n"
    "v_mfma_f32_16x16x32_bf16 v[36:39], a[44:47], v[72:75], v[36:39]\n"
    "v_exp_f32 v101, -v101\n"
    "v_mfma_f32_16x16x32_bf16 v[36:39], a[60:63], v[76:79], v[36:39]\n"
    "v_mul_f32 v102, v10, v14\n"
    "v_mul_f32 v103, v11, v15\n"
    "LS_7:\n"
    "s_cbranch_execz LS_11\n"
    "v_mfma_f32_16x16x32_bf16 v[36:39], a[76:79], v[80:83], v[36:39]\n"
    "buffer_store_dwordx4 v[116:119], v4, s[20:23], s15 offen offset:2048 sc1\n"
    "v_add_f32 v100, 1.0, v100\n"
    "v_mfma_f32_16x16x32_bf16 v[36:39], a[92:95], v[84:87], v[36:39]\n"
    "s_waitcnt lgkmcnt(0)\n"
    "v_mfma_f32_16x16x32_bf16 v[24:27], a[96:99], v[40:43], v[24:27]\n"
    "v_add_f32 v101, 1.0, v101\n"
    "v_mfma_f32_16x16x32_bf16 v[24:27], a[112:115], v[44:47], v[24:27]\n"
    "ds_read_b128 v[64:67], v120 offset:30720\n"
    "v_mfma_f32_16x16x32_bf16 v[24:27], a[128:131], v[48:51], v[24:27]\n"
    "v_rcp_f32 v100, v100\n"
    "v_mfma_f32_16x16x32_bf16 v[24:27], a[144:147], v[52:55], v[24:27]\n"
    "v_rcp_f32 v101, v101\n"
    "v_mfma_f32_16x16x32_bf16 v[24:27], a[160:163], v[56:59], v[24:27]\n"
    "ds_read_b128 v[68:71], v120 offset:31744\n"
    "s_waitcnt vmcnt(4)\n"
    "v_mfma_f32_16x16x32_bf16 v[24:27], a[176:179], v[60:63], v[24:27]\n"
    "v_xor_b32 v106, s40, v104\n"
    "v_xor_b32 v107, s41, v105\n"
    "v_mfma_f32_16x16x32_bf16 v[28:31], a[100:103], v[40:43], v[28:31]\n"
    "v_or_b32 v106, v106, v107\n"
    "v_cmp_ne_u32 vcc, 0, v106\n"
    "v_mfma_f32_16x16x32_bf16 v[28:31], a[116:119], v[44:47], v[28:31]\n"
    "ds_read_b128 v[72:75], v120 offset:32768\n"
    "s_nop 1\n"
    "v_mfma_f32_16x16x32_bf16 v[28:31], a[132:135], v[48:51], v[28:31]\n"
    "s_cbranch_vccnz LG1_fr_s\n"
    "LG1_fr_r:\n"
    "v_fma_f32 v96, v16, v16, v112\n"
    "v_mfma_f32_16x16x32_bf16 v[28:31], a[148:151], v[52:55], v[28:31]\n"
    "v_fma_f32 v97, v17, v17, v112\n"
    "v_mul_f32 v102, v102, v100\n"
    "v_mfma_f32_16x16x32_bf16 v[28:31], a[164:167], v[56:59], v[28:31]\n"
    "ds_read_b128 v[76:79], v120 offset:33792\n"
    "v_mfma_f32_16x16x32_bf16 v[28:31], a[180:183], v[60:63], v[28:31]\n"
    "v_mul_f32 v103, v103, v101\n"
    "v_cvt_pk_bf16_f32 v89, v102, v103\n"
    "v_mfma_f32_16x16x32_bf16 v[32:35], a[104:107], v[40:43], v[32:35]\n"
    "v_mul_f32 v96, v96, v16\n"
    "v_mfma_f32_16x16x32_bf16 v[32:35], a[120:123], v[44:47], v[32:35]\n"
    "ds_read_b128 v[80:83], v120 offset:34816\n"
    "v_mul_f32 v97, v97, v17\n"
    "v_mfma_f32_16x16x32_bf16 v[32:35], a[136:139], v[48:51], v[32:35]\n"
    "v_exp_f32 v96, -v96\n"
    "v_mfma_f32_16x16x32_bf16 v[32:35], a[152:155], v[52:55], v[32:35]\n"
    "v_mfma_f32_16x16x32_bf16 v[32:35], a[168:171], v[56:59], v[32:35]\n"
    "ds_read_b128 v[84:87], v120 offset:35840\n"
    "v_mfma_f32_16x16x32_bf16 v[32:35], a[184:187], v[60:63], v[32:35]\n"
    "v_exp_f32 v97, -v97\n"
    "v_mfma_f32_16x16x32_bf16 v[36:39], a[108:111], v[40:43], v[36:39]\n"
    "v_mul_f32 v98, v16, v20\n"
    "v_mul_f32 v99, v17, v21\n"
    "v_mfma_f32_16x16x32_bf16 v[36:39], a[124:127], v[44:47], v[36:39]\n"
    "v_add_f32 v96, 1.0, v96\n"
    "v_add_f32 v97, 1.0, v97\n"
    "v_mfma_f32_16x16x32_bf16 v[36:39], a[140:143], v[48:51], v[36:39]\n"
    "v_rcp_f32 v96, v96\n"
    "v_mfma_f32_16x16x32_bf16 v[36:39], a[156:159], v[52:55], v[36:39]\n"
    "v_mfma_f32_16x16x32_bf16 v[36:39], a[172:175], v[56:59], v[36:39]\n"
    "v_rcp_f32 v97, v97\n"
    "v_mfma_f32_16x16x32_bf16 v[36:39], a[188:191], v[60:63], v[36:39]\n"
    "s_waitcnt vmcnt(6) lgkmcnt(0)\n"
    "s_barrier\n"
    "v_mfma_f32_16x16x32_bf16 v[24:27], a[192:195], v[64:67], v[24:27]\n"
    "v_fma_f32 v100, v18, v18, v112\n"
    "v_fma_f32 v101, v19, v19, v112\n"
    "v_mfma_f32_16x16x32_bf16 v[24:27], a[208:211], v[68:71], v[24:27]\n"
    "ds_read_b128 v[40:43], v4 offset:0\n"
    "v_mul_f32 v98, v98, v96\n"
    "v_mfma_f32_16x16x32_bf16 v[24:27], a[224:227], v[72:75], v[24:27]\n"
    "s_add_u32 s77, s71, 0\n"
    "s_add_u32 m0, s70, 36864\n"
    "v_mfma_f32_16x16x32_bf16 v[24:27], a[240:243], v[76:79], v[24:27]\n"
    "buffer_load_dwordx4 v4, s[20:23], s77 offen offset:0 sc1 lds\n"
    "v_mul_f32 v99, v99, v97\n"
    "v_mfma_f32_16x16x32_bf16 v[24:27], v[224:227], v[80:83], v[24:27]\n"
    "ds_read_b128 v[44:47], v4 offset:1024\n"
    "v_mfma_f32_16x16x32_bf16 v[24:27], v[240:243], v[84:87], v[24:27]\n"
    "v_cvt_pk_bf16_f32 v90, v98, v99\n"
    "v_mul_f32 v100, v100, v18\n"
    "v_mfma_f32_16x16x32_bf16 v[28:31], a[196:199], v[64:67], v[28:31]\n"
    "v_mul_f32 v101, v101, v19\n"
    "v_mfma_f32_16x16x32_bf16 v[28:31], a[212:215], v[68:71], v[28:31]\n"
    "ds_read_b128 v[48:51], v4 offset:2048\n"
    "v_mfma_f32_16x16x32_bf16 v[28:31], a[228:231], v[72:75], v[28:31]\n"
    "buffer_load_dwordx4 v4, s[20:23], s77 offen offset:1024 sc1 lds\n"
    "v_mfma_f32_16x16x32_bf16 v[28:31], a[244:247], v[76:79], v[28:31]\n"
    "v_exp_f32 v100, -v100\n"
    "v_mfma_f32_16x16x32_bf16 v[28:31], v[228:231], v[80:83], v[28:31]\n"
    "ds_read_b128 v[52:55], v4 offset:3072\n"
    "v_mfma_f32_16x16x32_bf16 v[28:31], v[244:247], v[84:87], v[28:31]\n"
    "v_exp_f32 v101, -v101\n"
    "v_mfma_f32_16x16x32_bf16 v[32:35], a[200:203], v[64:67], v[32:35]\n"
    "v_mul_f32 v102, v18, v22\n"
    "v_mul_f32 v103, v19, v23\n"
    "v_mfma_f32_16x16x32_bf16 v[32:35], a[216:219], v[68:71], v[32:35]\n"
    "ds_read_b128 v[56:59], v4 offset:4096\n"
    "v_add_f32 v100, 1.0, v100\n"
    "v_mfma_f32_16x16x32_bf16 v[32:35], a[232:235], v[72:75], v[32:35]\n"
    "buffer_load_dwordx4 v4, s[20:23], s77 offen offset:2048 sc1 lds\n"
    "v_add_f32 v101, 1.0, v101\n"
    "v_mfma_f32_16x16x32_bf16 v[32:35], a[248:251], v[76:79], v[32:35]\n"
    "v_rcp_f32 v100, v100\n"
    "v_mfma_f32_16x16x32_bf16 v[32:35], v[232:235], v[80:83], v[32:35]\n"
    "ds_read_b128 v[60:63], v4 offset:5120\n"
    "v_mfma_f32_16x16x32_bf16 v[32:35], v[248:251], v[84:87], v[32:35]\n"
    "v_rcp_f32 v101, v101\n"
    "v_mfma_f32_16x16x32_bf16 v[36:39], a[204:207], v[64:67], v[36:39]\n"
    "s_nop 0\n"
    "v_mfma_f32_16x16x32_bf16 v[36:39], a[220:223], v[68:71], v[36:39]\n"
    "buffer_load_dwordx4 v4, s[20:23], s77 offen offset:3072 sc1 lds\n"
    "v_mul_f32 v102, v102, v100\n"
    "v_mfma_f32_16x16x32_bf16 v[36:39], a[236:239], v[72:75], v[36:39]\n"
    "v_mul_f32 v103, v103, v101\n"
    "v_cvt_pk_bf16_f32 v91, v102, v103\n"
    "v_mfma_f32_16x16x32_bf16 v[36:39], a[252:255], v[76:79], v[36:39]\n"
    "v_mfma_f32_16x16x32_bf16 v[36:39], v[236:239], v[80:83], v[36:39]\n"
    "v_mfma_f32_16x16x32_bf16 v[36:39], v[252:255], v[84:87], v[36:39]\n"
    "s_waitcnt lgkmcnt(0)\n"
    "v_mfma_f32_16x16x32_bf16 v[8:11], a[0:3], v[40:43], 0\n"
    "v_fma_f32 v96, v24, v24, v112\n"
    "v_fma_f32 v97, v25, v25, v112\n"
    "v_mfma_f32_16x16x32_bf16 v[8:11], a[16:19], v[44:47], v[8:11]\n"
    "ds_read_b128 v[64:67], v4 offset:6144\n"
    "v_mul_f32 v96, v96, v24\n"
    "v_mfma_f32_16x16x32_bf16 v[8:11], a[32:35], v[48:51], v[8:11]\n"
    "s_add_u32 s77, s71, 4096\n"
    "s_add_u32 m0, s70, 40960\n"
    "v_mfma_f32_16x16x32_bf16 v[8:11], a[48:51], v[52:55], v[8:11]\n"
    "buffer_load_dwordx4 v4, s[20:23], s77 offen offset:0 sc1 lds\n"
    "v_mul_f32 v97, v97, v25\n"
    "v_mfma_f32_16x16x32_bf16 v[8:11], a[64:67], v[56:59], v[8:11]\n"
    "ds_read_b128 v[68:71], v4 offset:7168\n"
    "v_mfma_f32_16x16x32_bf16 v[8:11], a[80:83], v[60:63], v[8:11]\n"
    "s_or_b32 s85, s2, s32\n"
    "s_cbranch_scc0 LG1_ep\n"
    "LG1_ep_r:\n"
    "v_mfma_f32_16x16x32_bf16 v[12:15], a[4:7], v[40:43], 0\n"
    "v_exp_f32 v96, -v96\n"
    "v_mfma_f32_16x16x32_bf16 v[12:15], a[20:23], v[44:47], v[12:15]\n"
    "ds_read_b128 v[72:75], v4 offset:8192\n"
    "v_mfma_f32_16x16x32_bf16 v[12:15], a[36:39], v[48:51], v[12:15]\n"
    "buffer_load_dwordx4 v4, s[20:23], s77 offen offset:1024 sc1 lds\n"
    "v_mfma_f32_16x16x32_bf16 v[12:15], a[52:55], v[52:55], v[12:15]\n"
    "v_exp_f32 v97, -v97\n"
    "v_mfma_f32_16x16x32_bf16 v[12:15], a[68:71], v[56:59], v[12:15]\n"
    "ds_read_b128 v[76:79], v4 offset:9216\n"
    "v_mul_f32 v98, v24, v28\n"
    "v_mfma_f32_16x16x32_bf16 v[12:15], a[84:87], v[60:63], v[12:15]\n"
    "v_mul_f32 v99, v25, v29\n"
    "v_add_f32 v96, 1.0, v96\n"
    "v_mfma_f32_16x16x32_bf16 v[16:19], a[8:11], v[40:43], 0\n"
    "v_add_f32 v97, 1.0, v97\n"
    "v_mfma_f32_16x16x32_bf16 v[16:19], a[24:27], v[44:47], v[16:19]\n"
    "ds_read_b128 v[80:83], v4 offset:10240\n"
    "v_mfma_f32_16x16x32_bf16 v[16:19], a[40:43], v[48:51], v[16:19]\n"
    "buffer_load_dwordx4 v4, s[20:23], s77 offen offset:2048 sc1 lds\n"
    "v_mfma_f32_16x16x32_bf16 v[16:19], a[56:59], v[52:55], v[16:19]\n"
    "v_rcp_f32 v96, v96\n"
    "v_mfma_f32_16x16x32_bf16 v[16:19], a[72:75], v[56:59], v[16:19]\n"
    "ds_read_b128 v[84:87], v4 offset:11264\n"
    "v_mfma_f32_16x16x32_bf16 v[16:19], a[88:91], v[60:63], v[16:19]\n"
    "v_rcp_f32 v97, v97\n"
    "v_mfma_f32_16x16x32_bf16 v[20:23], a[12:15], v[40:43], 0\n"
    "v_fma_f32 v100, v26, v26, v112\n"
    "v_fma_f32 v101, v27, v27, v112\n"
    "v_mfma_f32_16x16x32_bf16 v[20:23], a[28:31], v[44:47], v[20:23]\n"
    "buffer_load_dwordx4 v4, s[20:23], s77 offen offset:3072 sc1 lds\n"
    "s_add_u32 s77, s71, 8192\n"
    "v_mfma_f32_16x16x32_bf16 v[20:23], a[44:47], v[48:51], v[20:23]\n"
    "s_add_u32 m0, s70, 45056\n"
    "v_mul_f32 v98, v98, v96\n"
    "v_mfma_f32_16x16x32_bf16 v[20:23], a[60:63], v[52:55], v[20:23]\n"
    "v_mul_f32 v99, v99, v97\n"
    "v_cvt_pk_bf16_f32 v92, v98, v99\n"
    "v_mfma_f32_16x16x32_bf16 v[20:23], a[76:79], v[56:59], v[20:23]\n"
    "v_mul_f32 v100, v100, v26\n"
    "v_mul_f32 v101, v101, v27\n"
    "v_mfma_f32_16x16x32_bf16 v[20:23], a[92:95], v[60:63], v[20:23]\n"
    "s_waitcnt lgkmcnt(0)\n"
    "v_mfma_f32_16x16x32_bf16 v[8:11], a[96:99], v[64:67], v[8:11]\n"
    "buffer_load_dwordx4 v4, s[20:23], s77 offen offset:0 sc1 lds\n"
    "v_readlane_b32 s71, v214, 8\n"
    "v_mfma_f32_16x16x32_bf16 v[8:11], a[112:115], v[68:71], v[8:11]\n"
    "ds_read_b128 v[40:43], v4 offset:12288\n"
    "v_mfma_f32_16x16x32_bf16 v[8:11], a[128:131], v[72:75], v[8:11]\n"
    "v_exp_f32 v100, -v100\n"
    "v_mfma_f32_16x16x32_bf16 v[8:11], a[144:147], v[76:79], v[8:11]\n"
    "v_exp_f32 v101, -v101\n"
    "v_mfma_f32_16x16x32_bf16 v[8:11], a[160:163], v[80:83], v[8:11]\n"
    "ds_read_b128 v[44:47], v4 offset:13312\n"
    "v_mul_f32 v102, v26, v30\n"
    "v_mfma_f32_16x16x32_bf16 v[8:11], a[176:179], v[84:87], v[8:11]\n"
    "v_mul_f32 v103, v27, v31\n"
    "v_add_f32 v100, 1.0, v100\n"
    "v_mfma_f32_16x16x32_bf16 v[12:15], a[100:103], v[64:67], v[12:15]\n"
    "buffer_store_dwordx4 v[88:91], v4, s[24:27], s74 offen sc1\n"
    "v_readlane_b32 s74, v214, 28\n"
    "v_mfma_f32_16x16x32_bf16 v[12:15], a[116:119], v[68:71], v[12:15]\n"
    "ds_read_b128 v[48:51], v4 offset:14336\n"
    "v_readlane_b32 s77, v5, 0\n"
    "v_mfma_f32_16x16x32_bf16 v[12:15], a[132:135], v[72:75], v[12:15]\n"
    "s_add_u32 m0, s83, 73728\n"
    "v_add_f32 v101, 1.0, v101\n"
    "v_mfma_f32_16x16x32_bf16 v[12:15], a[148:151], v[76:79], v[12:15]\n"
    "v_rcp_f32 v100, v100\n"
    "v_mfma_f32_16x16x32_bf16 v[12:15], a[164:167], v[80:83], v[12:15]\n"
    "ds_read_b128 v[52:55], v4 offset:15360\n"
    "v_mfma_f32_16x16x32_bf16 v[12:15], a[180:183], v[84:87], v[12:15]\n"
    "v_rcp_f32 v101, v101\n"
    "v_mfma_f32_16x16x32_bf16 v[16:19], a[104:107], v[64:67], v[16:19]\n"
    "buffer_load_dwordx4 v6, s[20:23], s77 offen offset:0 sc1 lds\n"
    "v_fma_f32 v96, v32, v32, v112\n"
    "v_mfma_f32_16x16x32_bf16 v[16:19], a[120:123], v[68:71], v[16:19]\n"
    "ds_read_b128 v[56:59], v4 offset:16384\n"
    "v_fma_f32 v97, v33, v33, v112\n"
    "v_mfma_f32_16x16x32_bf16 v[16:19], a[136:139], v[72:75], v[16:19]\n"
    "v_mul_f32 v102, v102, v100\n"
    "v_mul_f32 v103, v103, v101\n"
    "v_mfma_f32_16x16x32_bf16 v[16:19], a[152:155], v[76:79], v[16:19]\n"
    "v_cvt_pk_bf16_f32 v93, v102, v103\n"
    "v_mul_f32 v96, v96, v32\n"
    "v_mfma_f32_16x16x32_bf16 v[16:19], a[168:171], v[80:83], v[16:19]\n"
    "ds_read_b128 v[60:63], v4 offset:17408\n"
    "v_mul_f32 v97, v97, v33\n"
    "v_mfma_f32_16x16x32_bf16 v[16:19], a[184:187], v[84:87], v[16:19]\n"
    "buffer_load_dwordx4 v6, s[20:23], s77 offen offset:1024 sc1 lds\n"
    "v_mfma_f32_16x16x32_bf16 v[20:23], a[108:111], v[64:67], v[20:23]\n"
    "v_exp_f32 v96, -v96\n"
    "v_mfma_f32_16x16x32_bf16 v[20:23], a[124:127], v[68:71], v[20:23]\n"
    "v_exp_f32 v97, -v97\n"
    "v_mfma_f32_16x16x32_bf16 v[20:23], a[140:143], v[72:75], v[20:23]\n"
    "v_mul_f32 v98, v32, v36\n"
    "v_mul_f32 v99, v33, v37\n"
    "v_mfma_f32_16x16x32_bf16 v[20:23], a[156:159], v[76:79], v[20:23]\n"
    "v_add_f32 v96, 1.0, v96\n"
    "v_add_f32 v97, 1.0, v97\n"
    "v_mfma_f32_16x16x32_bf16 v[20:23], a[172:175], v[80:83], v[20:23]\n"
    "buffer_load_dwordx4 v6, s[20:23], s77 offen offset:2048 sc1 lds\n"
    "v_mfma_f32_16x16x32_bf16 v[20:23], a[188:191], v[84:87], v[20:23]\n"
    "s_waitcnt lgkmcnt(0)\n"
    "v_mfma_f32_16x16x32_bf16 v[8:11], a[192:195], v[40:43], v[8:11]\n"
    "v_rcp_f32 v96, v96\n"
    "v_mfma_f32_16x16x32_bf16 v[8:11], a[208:211], v[44:47], v[8:11]\n"
    "ds_read_b128 v[64:67], v4 offset:18432\n"
    "v_mfma_f32_16x16x32_bf16 v[8:11], a[224:227], v[48:51], v[8:11]\n"
    "v_rcp_f32 v97, v97\n"
    "v_mfma_f32_16x16x32_bf16 v[8:11], a[240:243], v[52:55], v[8:11]\n"
    "buffer_load_dwordx4 v6, s[20:23], s77 offen offset:3072 sc1 lds\n"
    "s_add_u32 m0, s83, 77824\n"
    "v_mfma_f32_16x16x32_bf16 v[8:11], v[224:227], v[56:59], v[8:11]\n"
    "ds_read_b128 v[68:71], v4 offset:19456\n"
    "v_fma_f32 v100, v34, v34, v112\n"
    "v_mfma_f32_16x16x32_bf16 v[8:11], v[240:243], v[60:63], v[8:11]\n"
    "v_fma_f32 v101, v35, v35, v112\n"
    "v_mul_f32 v98, v98, v96\n"
    "v_mfma_f32_16x16x32_bf16 v[12:15], a[196:199], v[40:43], v[12:15]\n"
    "v_mul_f32 v99, v99, v97\n"
    "v_cvt_pk_bf16_f32 v94, v98, v99\n"
    "v_mfma_f32_16x16x32_bf16 v[12:15], a[212:215], v[44:47], v[12:15]\n"
    "ds_read_b128 v[72:75], v4 offset:20480\n"
    "v_mul_f32 v100, v100, v34\n"
    "v_mfma_f32_16x16x32_bf16 v[12:15], a[228:231], v[48:51], v[12:15]\n"
    "buffer_load_dwordx4 v7, s[20:23], s77 offen offset:0 sc1 lds\n"
    "v_readlane_b32 s77, v5, 0\n"
    "v_mfma_f32_16x16x32_bf16 v[12:15], a[244:247], v[52:55], v[12:15]\n"
    "s_add_u32 m0, s72, 154624\n"
    "v_mul_f32 v101, v101, v35\n"
    "v_mfma_f32_16x16x32_bf16 v[12:15], v[228:231], v[56:59], v[12:15]\n"
    "ds_read_b128 v[76:79], v4 offset:21504\n"
    "v_mfma_f32_16x16x32_bf16 v[12:15], v[244:247], v[60:63], v[12:15]\n"
    "v_exp_f32 v100, -v100\n"
    "v_mfma_f32_16x16x32_bf16 v[16:19], a[200:203], v[40:43], v[16:19]\n"
    "v_exp_f32 v101, -v101\n"
    "v_mfma_f32_16x16x32_bf16 v[16:19], a[216:219], v[44:47], v[16:19]\n"
    "ds_read_b128 v[80:83], v4 offset:22528\n"
    "v_mul_f32 v102, v34, v38\n"
    "v_mfma_f32_16x16x32_bf16 v[16:19], a[232:235], v[48:51], v[16:19]\n"
    "buffer_load_dwordx4 v7, s[20:23], s77 offen offset:1024 sc1 lds\n"
    "v_mul_f32 v103, v35, v39\n"
    "v_mfma_f32_16x16x32_bf16 v[16:19], a[248:251], v[52:55], v[16:19]\n"
    "v_add_f32 v100, 1.0, v100\n"
    "v_add_f32 v101, 1.0, v101\n"
    "v_mfma_f32_16x16x32_bf16 v[16:19], v[232:235], v[56:59], v[16:19]\n"
    "ds_read_b128 v[84:87], v4 offset:23552\n"
    "v_mfma_f32_16x16x32_bf16 v[16:19], v[248:251], v[60:63], v[16:19]\n"
    "v_rcp_f32 v100, v100\n"
    "v_mfma_f32_16x16x32_bf16 v[20:23], a[204:207], v[40:43], v[20:23]\n"
    "v_rcp_f32 v101, v101\n"
    "v_mfma_f32_16x16x32_bf16 v[20:23], a[220:223], v[44:47], v[20:23]\n"
    "buffer_load_dwordx4 v7, s[20:23], s77 offen offset:2048 sc1 lds\n"
    "v_readlane_b32 s77, v5, 1\n"
    "v_mfma_f32_16x16x32_bf16 v[20:23], a[236:239], v[48:51], v[20:23]\n"
    "s_add_u32 m0, s83, 94208\n"
    "s_nop 0\n"
    "v_mfma_f32_16x16x32_bf16 v[20:23], a[252:255], v[52:55], v[20:23]\n"
    "v_mul_f32 v102, v102, v100\n"
    "v_mul_f32 v103, v103, v101\n"
    "v_mfma_f32_16x16x32_bf16 v[20:23], v[236:239], v[56:59], v[20:23]\n"
    "v_cvt_pk_bf16_f32 v95, v102, v103\n"
    "v_mfma_f32_16x16x32_bf16 v[20:23], v[252:255], v[60:63], v[20:23]\n"
    "buffer_store_dwordx4 v[92:95], v4, s[24:27], s74 offen sc1\n"
    "s_waitcnt lgkmcnt(0)\n"
    "v_mfma_f32_16x16x32_bf16 v[24:27], a[0:3], v[64:67], 0\n"
    "v_readlane_b32 s74, v214, 29\n"
    "v_fma_f32 v96, v8, v8, v112\n"
    "v_mfma_f32_16x16x32_bf16 v[24:27], a[16:19], v[68:71], v[24:27]\n"
    "ds_read_b128 v[40:43], v4 offset:24576\n"
    "v_fma_f32 v97, v9, v9, v112\n"
    "v_mfma_f32_16x16x32_bf16 v[24:27], a[32:35], v[72:75], v[24:27]\n"
    "buffer_load_dwordx4 v6, s[20:23], s77 offen offset:0 sc1 lds\n"
    "v_mul_f32 v96, v96, v8\n"
    "v_mfma_f32_16x16x32_bf16 v[24:27], a[48:51], v[76:79], v[24:27]\n"
    "v_mul_f32 v97, v97, v9\n"
    "v_mfma_f32_16x16x32_bf16 v[24:27], a[64:67], v[80:83], v[24:27]\n"
    "ds_read_b128 v[44:47], v4 offset:25600\n"
    "v_mfma_f32_16x16x32_bf16 v[24:27], a[80:83], v[84:87], v[24:27]\n"
    "v_exp_f32 v96, -v96\n"
    "v_mfma_f32_16x16x32_bf16 v[28:31], a[4:7], v[64:67], 0\n"
    "v_exp_f32 v97, -v97\n"
    "v_mfma_f32_16x16x32_bf16 v[28:31], a[20:23], v[68:71], v[28:31]\n"
    "ds_read_b128 v[48:51], v4 offset:26624\n"
    "v_mul_f32 v98, v8, v12\n"
    "v_mfma_f32_16x16x32_bf16 v[28:31], a[36:39], v[72:75], v[28:31]\n"
    "buffer_load_dwordx4 v6, s[20:23], s77 offen offset:1024 sc1 lds\n"
    "v_mul_f32 v99, v9, v13\n"
    "v_mfma_f32_16x16x32_bf16 v[28:31], a[52:55], v[76:79], v[28:31]\n"
    "v_add_f32 v96, 1.0, v96\n"
    "v_add_f32 v97, 1.0, v97\n"
    "v_mfma_f32_16x16x32_bf16 v[28:31], a[68:71], v[80:83], v[28:31]\n"
    "ds_read_b128 v[52:55], v4 offset:27648\n"
    "v_mfma_f32_16x16x32_bf16 v[28:31], a[84:87], v[84:87], v[28:31]\n"
    "v_rcp_f32 v96, v96\n"
    "v_mfma_f32_16x16x32_bf16 v[32:35], a[8:11], v[64:67], 0\n"
    "v_rcp_f32 v97, v97\n"
    "v_mfma_f32_16x16x32_bf16 v[32:35], a[24:27], v[68:71], v[32:35]\n"
    "ds_read_b128 v[56:59], v4 offset:28672\n"
    "v_fma_f32 v100, v10, v10, v112\n"
    "v_mfma_f32_16x16x32_bf16 v[32:35], a[40:43], v[72:75], v[32:35]\n"
    "buffer_load_dwordx4 v6, s[20:23], s77 offen offset:2048 sc1 lds\n"
    "v_fma_f32 v101, v11, v11, v112\n"
    "v_mfma_f32_16x16x32_bf16 v[32:35], a[56:59], v[76:79], v[32:35]\n"
    "v_mul_f32 v98, v98, v96\n"
    "v_mul_f32 v99, v99, v97\n"
    "v_mfma_f32_16x16x32_bf16 v[32:35], a[72:75], v[80:83], v[32:35]\n"
    "ds_read_b128 v[60:63], v4 offset:29696\n"
    "v_mfma_f32_16x16x32_bf16 v[32:35], a[88:91], v[84:87], v[32:35]\n"
    "v_cvt_pk_bf16_f32 v88, v98, v99\n"
    "v_mul_f32 v100, v100, v10\n"
    "v_mfma_f32_16x16x32_bf16 v[36:39], a[12:15], v[64:67], 0\n"
    "v_mul_f32 v101, v101, v11\n"
    "v_mfma_f32_16x16x32_bf16 v[36:39], a[28:31], v[68:71], v[36:39]\n"
    "buffer_load_dwordx4 v6, s[20:23], s77 offen offset:3072 sc1 lds\n"
    "s_add_u32 m0, s83, 98304\n"
    "v_mfma_f32_16x16x32_bf16 v[36:39], a[44:47], v[72:75], v[36:39]\n"
    "v_exp_f32 v100, -v100\n"
    "v_mfma_f32_16x16x32_bf16 v[36:39], a[60:63], v[76:79], v[36:39]\n"
    "v_mfma_f32_16x16x32_bf16 v[36:39], a[76:79], v[80:83], v[36:39]\n"
    "v_exp_f32 v101, -v101\n"
    "v_mfma_f32_16x16x32_bf16 v[36:39], a[92:95], v[84:87], v[36:39]\n"
    "v_mul_f32 v102, v10, v14\n"
    "s_waitcnt lgkmcnt(0)\n"
    "v_mfma_f32_16x16x32_bf16 v[24:27], a[96:99], v[40:43], v[24:27]\n"
    "buffer_load_dwordx4 v7, s[20:23], s77 offen offset:0 sc1 lds\n"
    "s_mov_b32 s85, 303104\n"
    "v_mfma_f32_16x16x32_bf16 v[24:27], a[112:115], v[44:47], v[24:27]\n"
    "ds_read_b128 v[64:67], v4 offset:30720\n"
    "v_mul_f32 v103, v11, v15\n"
    "v_mfma_f32_16x16x32_bf16 v[24:27], a[128:131], v[48:51], v[24:27]\n"
    "v_add_f32 v100, 1.0, v100\n"
    "v_add_f32 v101, 1.0, v101\n"
    "v_mfma_f32_16x16x32_bf16 v[24:27], a[144:147], v[52:55], v[24:27]\n"
    "v_mfma_f32_16x16x32_bf16 v[24:27], a[160:163], v[56:59], v[24:27]\n"
    "ds_read_b128 v[68:71], v4 offset:31744\n"
    "v_mfma_f32_16x16x32_bf16 v[24:27], a[176:179], v[60:63], v[24:27]\n"
    "buffer_load_dwordx2 v[104:105], v110, s[20:23], s85 offen sc1\n"
    "v_mfma_f32_16x16x32_bf16 v[28:31], a[100:103], v[40:43], v[28:31]\n"
    "v_rcp_f32 v100, v100\n"
    "v_mfma_f32_16x16x32_bf16 v[28:31], a[116:119], v[44:47], v[28:31]\n"
    "ds_read_b128 v[72:75], v4 offset:32768\n"
    "v_mfma_f32_16x16x32_bf16 v[28:31], a[132:135], v[48:51], v[28:31]\n"
    "v_rcp_f32 v101, v101\n"
    "v_mfma_f32_16x16x32_bf16 v[28:31], a[148:151], v[52:55], v[28:31]\n"
    "v_fma_f32 v96, v16, v16, v112\n"
    "v_fma_f32 v97, v17, v17, v112\n"
    "v_mfma_f32_16x16x32_bf16 v[28:31], a[164:167], v[56:59], v[28:31]\n"
    "ds_read_b128 v[76:79], v4 offset:33792\n"
    "v_mul_f32 v102, v102, v100\n"
    "v_mfma_f32_16x16x32_bf16 v[28:31], a[180:183], v[60:63], v[28:31]\n"
    "v_mul_f32 v103, v103, v101\n"
    "v_cvt_pk_bf16_f32 v89, v102, v103\n"
    "v_mfma_f32_16x16x32_bf16 v[32:35], a[104:107], v[40:43], v[32:35]\n"
    "v_mul_f32 v96, v96, v16\n"
    "v_mul_f32 v97, v97, v17\n"
    "v_mfma_f32_16x16x32_bf16 v[32:35], a[120:123], v[44:47], v[32:35]\n"
    "ds_read_b128 v[80:83], v4 offset:34816\n"
    "v_mfma_f32_16x16x32_bf16 v[32:35], a[136:139], v[48:51], v[32:35]\n"
    "v_exp_f32 v96, -v96\n"
    "v_mfma_f32_16x16x32_bf16 v[32:35], a[152:155], v[52:55], v[32:35]\n"
    "v_exp_f32 v97, -v97\n"
    "v_mfma_f32_16x16x32_bf16 v[32:35], a[168:171], v[56:59], v[32:35]\n"
    "ds_read_b128 v[84:87], v4 offset:35840\n"
    "v_mul_f32 v98, v16, v20\n"
    "v_mfma_f32_16x16x32_bf16 v[32:35], a[184:187], v[60:63], v[32:35]\n"
    "v_mul_f32 v99, v17, v21\n"
    "v_add_f32 v96, 1.0, v96\n"
    "v_mfma_f32_16x16x32_bf16 v[36:39], a[108:111], v[40:43], v[36:39]\n"
    "v_add_f32 v97, 1.0, v97\n"
    "v_mfma_f32_16x16x32_bf16 v[36:39], a[124:127], v[44:47], v[36:39]\n"
    "v_rcp_f32 v96, v96\n"
    "v_mfma_f32_16x16x32_bf16 v[36:39], a[140:143], v[48:51], v[36:39]\n"
    "v_mfma_f32_16x16x32_bf16 v[36:39], a[156:159], v[52:55], v[36:39]\n"
    "v_rcp_f32 v97, v97\n"
    "v_mfma_f32_16x16x32_bf16 v[36:39], a[172:175], v[56:59], v[36:39]\n"
    "v_fma_f32 v100, v18, v18, v112\n"
    "v_fma_f32 v101, v19, v19, v112\n"
    "v_mfma_f32_16x16x32_bf16 v[36:39], a[188:191], v[60:63], v[36:39]\n"
    "s_waitcnt vmcnt(15) lgkmcnt(0)\n"
    "s_barrier\n"
    "v_mfma_f32_16x16x32_bf16 v[24:27], a[192:195], v[64:67], v[24:27]\n"
    "s_lshl_b32 s85, s33, 7\n"
    "s_add_u32 s85, s85, 270336\n"
    "v_mfma_f32_16x16x32_bf16 v[24:27], a[208:211], v[68:71], v[24:27]\n"
    "ds_read_b128 v[40:43], v120 offset:0\n"
    "v_mul_f32 v98, v98, v96\n"
    "v_mfma_f32_16x16x32_bf16 v[24:27], a[224:227], v[72:75], v[24:27]\n"
    "buffer_store_dwordx4 v[218:221], v215, s[20:23], s85 offen sc1\n"
    "s_add_u32 s77, s71, 0\n"
    "v_mfma_f32_16x16x32_bf16 v[24:27], a[240:243], v[76:79], v[24:27]\n"
    "s_add_u32 m0, s70, 0\n"
    "v_mul_f32 v99, v99, v97\n"
    "v_mfma_f32_16x16x32_bf16 v[24:27], v[224:227], v[80:83], v[24:27]\n"
    "ds_read_b128 v[44:47], v120 offset:1024\n"
    "v_cvt_pk_bf16_f32 v90, v98, v99\n"
    "v_mfma_f32_16x16x32_bf16 v[24:27], v[240:243], v[84:87], v[24:27]\n"
    "buffer_load_dwordx4 v4, s[20:23], s77 offen offset:0 sc1 lds\n"
    "v_mfma_f32_16x16x32_bf16 v[28:31], a[196:199], v[64:67], v[28:31]\n"
    "v_mul_f32 v100, v100, v18\n"
    "v_mul_f32 v101, v101, v19\n"
    "v_mfma_f32_16x16x32_bf16 v[28:31], a[212:215], v[68:71], v[28:31]\n"
    "ds_read_b128 v[48:51], v120 offset:2048\n"
    "v_mfma_f32_16x16x32_bf16 v[28:31], a[228:231], v[72:75], v[28:31]\n"
    "v_exp_f32 v100, -v100\n"
    "v_mfma_f32_16x16x32_bf16 v[28:31], a[244:247], v[76:79], v[28:31]\n"
    "v_exp_f32 v101, -v101\n"
    "v_mfma_f32_16x16x32_bf16 v[28:31], v[228:231], v[80:83], v[28:31]\n"
    "ds_read_b128 v[52:55], v120 offset:3072\n"
    "v_mfma_f32_16x16x32_bf16 v[28:31], v[244:247], v[84:87], v[28:31]\n"
    "buffer_load_dwordx4 v4, s[20:23], s77 offen offset:1024 sc1 lds\n"
    "v_mul_f32 v102, v18, v22\n"
    "v_mfma_f32_16x16x32_bf16 v[32:35], a[200:203], v[64:67], v[32:35]\n"
    "v_mul_f32 v103, v19, v23\n"
    "v_add_f32 v100, 1.0, v100\n"
    "v_mfma_f32_16x16x32_bf16 v[32:35], a[216:219], v[68:71], v[32:35]\n"
    "ds_read_b128 v[56:59], v120 offset:4096\n"
    "v_mfma_f32_16x16x32_bf16 v[32:35], a[232:235], v[72:75], v[32:35]\n"
    "v_add_f32 v101, 1.0, v101\n"
    "v_mfma_f32_16x16x32_bf16 v[32:35], a[248:251], v[76:79], v[32:35]\n"
    "v_rcp_f32 v100, v100\n"
    "v_mfma_f32_16x16x32_bf16 v[32:35], v[232:235], v[80:83], v[32:35]\n"
    "ds_read_b128 v[60:63], v120 offset:5120\n"
    "v_mfma_f32_16x16x32_bf16 v[32:35], v[248:251], v[84:87], v[32:35]\n"
    "buffer_load_dwordx4 v4, s[20:23], s77 offen offset:2048 sc1 lds\n"
    "v_mfma_f32_16x16x32_bf16 v[36:39], a[204:207], v[64:67], v[36:39]\n"
    "v_rcp_f32 v101, v101\n"
    "v_mfma_f32_16x16x32_bf16 v[36:39], a[220:223], v[68:71], v[36:39]\n"
    "s_nop 0\n"
    "v_mul_f32 v102, v102, v100\n"
    "v_mfma_f32_16x16x32_bf16 v[36:39], a[236:239], v[72:75], v[36:39]\n"
    "v_mul_f32 v103, v103, v101\n"
    "v_cvt_pk_bf16_f32 v91, v102, v103\n"
    "v_mfma_f32_16x16x32_bf16 v[36:39], a[252:255], v[76:79], v[36:39]\n"
    "v_mfma_f32_16x16x32_bf16 v[36:39], v[236:239], v[80:83], v[36:39]\n"
    "buffer_load_dwordx4 v4, s[20:23], s77 offen offset:3072 sc1 lds\n"
    "v_mfma_f32_16x16x32_bf16 v[36:39], v[252:255], v[84:87], v[36:39]\n"
    "s_waitcnt lgkmcnt(0)\n"
    "v_mfma_f32_16x16x32_bf16 v[8:11], a[0:3], v[40:43], 0\n"
    "v_fma_f32 v96, v24, v24, v112\n"
    "v_fma_f32 v97, v25, v25, v112\n"
    "v_mfma_f32_16x16x32_bf16 v[8:11], a[16:19], v[44:47], v[8:11]\n"
    "ds_read_b128 v[64:67], v120 offset:6144\n"
    "v_mul_f32 v96, v96, v24\n"
    "v_mfma_f32_16x16x32_bf16 v[8:11], a[32:35], v[48:51], v[8:11]\n"
    "s_add_u32 s77, s71, 4096\n"
    "s_add_u32 m0, s70, 4096\n"
    "v_mfma_f32_16x16x32_bf16 v[8:11], a[48:51], v[52:55], v[8:11]\n"
    "buffer_load_dwordx4 v4, s[20:23], s77 offen offset:0 sc1 lds\n"
    "v_mul_f32 v97, v97, v25\n"
    "v_mfma_f32_16x16x32_bf16 v[8:11], a[64:67], v[56:59], v[8:11]\n"
    "ds_read_b128 v[68:71], v120 offset:7168\n"
    "v_mfma_f32_16x16x32_bf16 v[8:11], a[80:83], v[60:63], v[8:11]\n"
    "v_exp_f32 v96, -v96\n"
    "v_mfma_f32_16x16x32_bf16 v[12:15], a[4:7], v[40:43], 0\n"
    "v_exp_f32 v97, -v97\n"
    "v_mfma_f32_16x16x32_bf16 v[12:15], a[20:23], v[44:47], v[12:15]\n"
    "ds_read_b128 v[72:75], v120 offset:8192\n"
    "v_mul_f32 v98, v24, v28\n"
    "v_mfma_f32_16x16x32_bf16 v[12:15], a[36:39], v[48:51], v[12:15]\n"
    "buffer_load_dwordx4 v4, s[20:23], s77 offen offset:1024 sc1 lds\n"
    "v_mul_f32 v99, v25, v29\n"
    "v_mfma_f32_16x16x32_bf16 v[12:15], a[52:55], v[52:55], v[12:15]\n"
    "v_add_f32 v96, 1.0, v96\n"
    "v_add_f32 v97, 1.0, v97\n"
    "v_mfma_f32_16x16x32_bf16 v[12:15], a[68:71], v[56:59], v[12:15]\n"
    "ds_read_b128 v[76:79], v120 offset:9216\n"
    "v_mfma_f32_16x16x32_bf16 v[12:15], a[84:87], v[60:63], v[12:15]\n"
    "v_rcp_f32 v96, v96\n"
    "v_mfma_f32_16x16x32_bf16 v[16:19], a[8:11], v[40:43], 0\n"
    "v_rcp_f32 v97, v97\n"
    "v_mfma_f32_16x16x32_bf16 v[16:19], a[24:27], v[44:47], v[16:19]\n"
    "ds_read_b128 v[80:83], v120 offset:10240\n"
    "v_fma_f32 v100, v26, v26, v112\n"
    "v_mfma_f32_16x16x32_bf16 v[16:19], a[40:43], v[48:51], v[16:19]\n"
    "buffer_load_dwordx4 v4, s[20:23], s77 offen offset:2048 sc1 lds\n"
    "v_fma_f32 v101, v27, v27, v112\n"
    "v_mfma_f32_16x16x32_bf16 v[16:19], a[56:59], v[52:55], v[16:19]\n"
    "v_mul_f32 v98, v98, v96\n"
    "v_mul_f32 v99, v99, v97\n"
    "v_mfma_f32_16x16x32_bf16 v[16:19], a[72:75], v[56:59], v[16:19]\n"
    "ds_read_b128 v[84:87], v120 offset:11264\n"
    "v_cvt_pk_bf16_f32 v92, v98, v99\n"
    "v_mfma_f32_16x16x32_bf16 v[16:19], a[88:91], v[60:63], v[16:19]\n"
    "v_mul_f32 v100, v100, v26\n"
    "v_mul_f32 v101, v101, v27\n"
    "v_mfma_f32_16x16x32_bf16 v[20:23], a[12:15], v[40:43], 0\n"
    "v_exp_f32 v100, -v100\n"
    "v_mfma_f32_16x16x32_bf16 v[20:23], a[28:31], v[44:47], v[20:23]\n"
    "buffer_load_dwordx4 v4, s[20:23], s77 offen offset:3072 sc1 lds\n"
    "s_add_u32 s77, s71, 8192\n"
    "v_mfma_f32_16x16x32_bf16 v[20:23], a[44:47], v[48:51], v[20:23]\n"
    "s_add_u32 m0, s70, 8192\n"
    "v_mfma_f32_16x16x32_bf16 v[20:23], a[60:63], v[52:55], v[20:23]\n"
    "v_exp_f32 v101, -v101\n"
    "v_mfma_f32_16x16x32_bf16 v[20:23], a[76:79], v[56:59], v[20:23]\n"
    "v_mul_f32 v102, v26, v30\n"
    "v_mul_f32 v103, v27, v31\n"
    "v_mfma_f32_16x16x32_bf16 v[20:23], a[92:95], v[60:63], v[20:23]\n"
    "v_add_f32 v100, 1.0, v100\n"
    "s_waitcnt lgkmcnt(0)\n"
    "v_mfma_f32_16x16x32_bf16 v[8:11], a[96:99], v[64:67], v[8:11]\n"
    "buffer_load_dwordx4 v4, s[20:23], s77 offen offset:0 sc1 lds\n"
    "v_readlane_b32 s71, v214, 9\n"
    "v_mfma_f32_16x16x32_bf16 v[8:11], a[112:115], v[68:71], v[8:11]\n"
    "ds_read_b128 v[40:43], v120 offset:12288\n"
    "v_add_f32 v101, 1.0, v101\n"
    "v_mfma_f32_16x16x32_bf16 v[8:11], a[128:131], v[72:75], v[8:11]\n"
    "v_rcp_f32 v100, v100\n"
    "v_mfma_f32_16x16x32_bf16 v[8:11], a[144:147], v[76:79], v[8:11]\n"
    "v_mfma_f32_16x16x32_bf16 v[8:11], a[160:163], v[80:83], v[8:11]\n"
    "ds_read_b128 v[44:47], v120 offset:13312\n"
    "v_mfma_f32_16x16x32_bf16 v[8:11], a[176:179], v[84:87], v[8:11]\n"
    "v_rcp_f32 v101, v101\n"
    "v_mfma_f32_16x16x32_bf16 v[12:15], a[100:103], v[64:67], v[12:15]\n"
    "buffer_store_dwordx4 v[88:91], v4, s[24:27], s74 offen sc1\n"
    "v_readlane_b32 s74, v214, 30\n"
    "v_mfma_f32_16x16x32_bf16 v[12:15], a[116:119], v[68:71], v[12:15]\n"
    "ds_read_b128 v[48:51], v120 offset:14336\n"
    "s_waitcnt vmcnt(11)\n"
    "v_mfma_f32_16x16x32_bf16 v[12:15], a[132:135], v[72:75], v[12:15]\n"
    "v_xor_b32 v106, s40, v104\n"
    "v_xor_b32 v107, s41, v105\n"
    "v_mfma_f32_16x16x32_bf16 v[12:15], a[148:151], v[76:79], v[12:15]\n"
    "v_or_b32 v106, v106, v107\n"
    "v_cmp_ne_u32 vcc, 0, v106\n"
    "v_mfma_f32_16x16x32_bf16 v[12:15], a[164:167], v[80:83], v[12:15]\n"
    "ds_read_b128 v[52:55], v120 offset:15360\n"
    "s_nop 1\n"
    "v_mfma_f32_16x16x32_bf16 v[12:15], a[180:183], v[84:87], v[12:15]\n"
    "s_and_b64 vcc, vcc, 15\n"
    "s_cbranch_vccnz LG1_fk_s\n"
    "LG1_fk_r:\n"
    "v_mfma_f32_16x16x32_bf16 v[16:19], a[104:107], v[64:67], v[16:19]\n"
    "v_readlane_b32 s77, v5, 2\n"
    "s_add_u32 m0, s83, 114688\n"
    "v_mfma_f32_16x16x32_bf16 v[16:19], a[120:123], v[68:71], v[16:19]\n"
    "ds_read_b128 v[56:59], v120 offset:16384\n"
    "v_fma_f32 v96, v32, v32, v112\n"
    "v_mfma_f32_16x16x32_bf16 v[16:19], a[136:139], v[72:75], v[16:19]\n"
    "v_fma_f32 v97, v33, v33, v112\n"
    "v_mul_f32 v102, v102, v100\n"
    "v_mfma_f32_16x16x32_bf16 v[16:19], a[152:155], v[76:79], v[16:19]\n"
    "v_mul_f32 v103, v103, v101\n"
    "v_cvt_pk_bf16_f32 v93, v102, v103\n"
    "v_mfma_f32_16x16x32_bf16 v[16:19], a[168:171], v[80:83], v[16:19]\n"
    "ds_read_b128 v[60:63], v120 offset:17408\n"
    "v_mul_f32 v96, v96, v32\n"
    "v_mfma_f32_16x16x32_bf16 v[16:19], a[184:187], v[84:87], v[16:19]\n"
    "buffer_load_dwordx4 v6, s[20:23], s77 offen offset:0 sc1 lds\n"
    "v_mul_f32 v97, v97, v33\n"
    "v_mfma_f32_16x16x32_bf16 v[20:23], a[108:111], v[64:67], v[20:23]\n"
    "v_exp_f32 v96, -v96\n"
    "v_mfma_f32_16x16x32_bf16 v[20:23], a[124:127], v[68:71], v[20:23]\n"
    "v_exp_f32 v97, -v97\n"
    "v_mfma_f32_16x16x32_bf16 v[20:23], a[140:143], v[72:75], v[20:23]\n"
    "v_mul_f32 v98, v32, v36\n"
    "v_mul_f32 v99, v33, v37\n"
    "v_mfma_f32_16x16x32_bf16 v[20:23], a[156:159], v[76:79], v[20:23]\n"
    "v_add_f32 v96, 1.0, v96\n"
    "LS_8:\n"
    "s_cbranch_execz LS_12\n"
    "v_mfma_f32_16x16x32_bf16 v[20:23], a[172:175], v[80:83], v[20:23]\n"
    "buffer_load_dwordx4 v6, s[20:23], s77 offen offset:1024 sc1 lds\n"
    "v_add_f32 v97, 1.0, v97\n"
    "v_mfma_f32_16x16x32_bf16 v[20:23], a[188:191], v[84:87], v[20:23]\n"
    "s_waitcnt lgkmcnt(0)\n"
    "v_mfma_f32_16x16x32_bf16 v[8:11], a[192:195], v[40:43], v[8:11]\n"
    "v_rcp_f32 v96, v96\n"
    "v_mfma_f32_16x16x32_bf16 v[8:11], a[208:211], v[44:47], v[8:11]\n"
    "ds_read_b128 v[64:67], v120 offset:18432\n"
    "v_mfma_f32_16x16x32_bf16 v[8:11], a[224:227], v[48:51], v[8:11]\n"
    "v_rcp_f32 v97, v97\n"
    "v_mfma_f32_16x16x32_bf16 v[8:11], a[240:243], v[52:55], v[8:11]\n"
    "buffer_load_dwordx4 v6, s[20:23], s77 offen offset:2048 sc1 lds\n"
    "v_fma_f32 v100, v34, v34, v112\n"
    "v_mfma_f32_16x16x32_bf16 v[8:11], v[224:227], v[56:59], v[8:11]\n"
    "ds_read_b128 v[68:71], v120 offset:19456\n"
    "v_fma_f32 v101, v35, v35, v112\n"
    "v_mfma_f32_16x16x32_bf16 v[8:11], v[240:243], v[60:63], v[8:11]\n"
    "v_mul_f32 v98, v98, v96\n"
    "v_mul_f32 v99, v99, v97\n"
    "v_mfma_f32_16x16x32_bf16 v[12:15], a[196:199], v[40:43], v[12:15]\n"
    "v_cvt_pk_bf16_f32 v94, v98, v99\n"
    "v_mul_f32 v100, v100, v34\n"
    "v_mfma_f32_16x16x32_bf16 v[12:15], a[212:215], v[44:47], v[12:15]\n"
    "ds_read_b128 v[72:75], v120 offset:20480\n"
    "v_mul_f32 v101, v101, v35\n"
    "v_mfma_f32_16x16x32_bf16 v[12:15], a[228:231], v[48:51], v[12:15]\n"
    "buffer_load_dwordx4 v6, s[20:23], s77 offen offset:3072 sc1 lds\n"
    "s_add_u32 m0, s83, 118784\n"
    "v_mfma_f32_16x16x32_bf16 v[12:15], a[244:247], v[52:55], v[12:15]\n"
    "v_exp_f32 v100, -v100\n"
    "v_mfma_f32_16x16x32_bf16 v[12:15], v[228:231], v[56:59], v[12:15]\n"
    "ds_read_b128 v[76:79], v120 offset:21504\n"
    "v_mfma_f32_16x16x32_bf16 v[12:15], v[244:247], v[60:63], v[12:15]\n"
    "v_exp_f32 v101, -v101\n"
    "v_mfma_f32_16x16x32_bf16 v[16:19], a[200:203], v[40:43], v[16:19]\n"
    "v_mul_f32 v102, v34, v38\n"
    "v_mul_f32 v103, v35, v39\n"
    "v_mfma_f32_16x16x32_bf16 v[16:19], a[216:219], v[44:47], v[16:19]\n"
    "ds_read_b128 v[80:83], v120 offset:22528\n"
    "v_add_f32 v100, 1.0, v100\n"
    "v_mfma_f32_16x16x32_bf16 v[16:19], a[232:235], v[48:51], v[16:19]\n"
    "buffer_load_dwordx4 v7, s[20:23], s77 offen offset:0 sc1 lds\n"
    "v_readlane_b32 s77, v5, 16\n"
    "v_mfma_f32_16x16x32_bf16 v[16:19], a[248:251], v[52:55], v[16:19]\n"
    "v_add_f32 v101, 1.0, v101\n"
    "v_mfma_f32_16x16x32_bf16 v[16:19], v[232:235], v[56:59], v[16:19]\n"
    "ds_read_b128 v[84:87], v120 offset:23552\n"
    "v_mfma_f32_16x16x32_bf16 v[16:19], v[248:251], v[60:63], v[16:19]\n"
    "v_rcp_f32 v100, v100\n"
    "v_mfma_f32_16x16x32_bf16 v[20:23], a[204:207], v[40:43], v[20:23]\n"
    "v_rcp_f32 v101, v101\n"
    "v_mfma_f32_16x16x32_bf16 v[20:23], a[220:223], v[44:47], v[20:23]\n"
    "buffer_load_dwordx4 v[128:131], v222, s[20:23], s77 offen offset:0 sc1\n"
    "s_nop 0\n"
    "v_mfma_f32_16x16x32_bf16 v[20:23], a[236:239], v[48:51], v[20:23]\n"
    "v_mul_f32 v102, v102, v100\n"
    "v_mul_f32 v103, v103, v101\n"
    "v_mfma_f32_16x16x32_bf16 v[20:23], a[252:255], v[52:55], v[20:23]\n"
    "v_cvt_pk_bf16_f32 v95, v102, v103\n"
    "v_mfma_f32_16x16x32_bf16 v[20:23], v[236:239], v[56:59], v[20:23]\n"
    "v_mfma_f32_16x16x32_bf16 v[20:23], v[252:255], v[60:63], v[20:23]\n"
    "buffer_store_dwordx4 v[92:95], v4, s[24:27], s74 offen sc1\n"
    "s_waitcnt lgkmcnt(0)\n"
    "v_mfma_f32_16x16x32_bf16 v[24:27], a[0:3], v[64:67], 0\n"
    "v_readlane_b32 s74, v214, 31\n"
    "v_fma_f32 v96, v8, v8, v112\n"
    "v_mfma_f32_16x16x32_bf16 v[24:27], a[16:19], v[68:71], v[24:27]\n"
    "ds_read_b128 v[40:43], v120 offset:24576\n"
    "v_fma_f32 v97, v9, v9, v112\n"
    "v_mfma_f32_16x16x32_bf16 v[24:27], a[32:35], v[72:75], v[24:27]\n"
    "buffer_load_dwordx4 v[144:147], v223, s[20:23], s77 offen offset:0 sc1\n"
    "v_mul_f32 v96, v96, v8\n"
    "v_mfma_f32_16x16x32_bf16 v[24:27], a[48:51], v[76:79], v[24:27]\n"
    "v_mul_f32 v97, v97, v9\n"
    "v_mfma_f32_16x16x32_bf16 v[24:27], a[64:67], v[80:83], v[24:27]\n"
    "ds_read_b128 v[44:47], v120 offset:25600\n"
    "v_mfma_f32_16x16x32_bf16 v[24:27], a[80:83], v[84:87], v[24:27]\n"
    "v_exp_f32 v96, -v96\n"
    "v_mfma_f32_16x16x32_bf16 v[28:31], a[4:7], v[64:67], 0\n"
    "v_exp_f32 v97, -v97\n"
    "v_mfma_f32_16x16x32_bf16 v[28:31], a[20:23], v[68:71], v[28:31]\n"
    "ds_read_b128 v[48:51], v120 offset:26624\n"
    "v_mul_f32 v98, v8, v12\n"
    "v_mfma_f32_16x16x32_bf16 v[28:31], a[36:39], v[72:75], v[28:31]\n"
    "buffer_load_dwordx4 v[132:135], v222, s[20:23], s77 offen offset:1024 sc1\n"
    "v_mul_f32 v99, v9, v13\n"
    "v_mfma_f32_16x16x32_bf16 v[28:31], a[52:55], v[76:79], v[28:31]\n"
    "v_add_f32 v96, 1.0, v96\n"
    "v_add_f32 v97, 1.0, v97\n"
    "v_mfma_f32_16x16x32_bf16 v[28:31], a[68:71], v[80:83], v[28:31]\n"
    "ds_read_b128 v[52:55], v120 offset:27648\n"
    "v_mfma_f32_16x16x32_bf16 v[28:31], a[84:87], v[84:87], v[28:31]\n"
    "v_rcp_f32 v96, v96\n"
    "v_mfma_f32_16x16x32_bf16 v[32:35], a[8:11], v[64:67], 0\n"
    "v_rcp_f32 v97, v97\n"
    "v_mfma_f32_16x16x32_bf16 v[32:35], a[24:27], v[68:71], v[32:35]\n"
    "ds_read_b128 v[56:59], v120 offset:28672\n"
    "v_fma_f32 v100, v10, v10, v112\n"
    "v_mfma_f32_16x16x32_bf16 v[32:35], a[40:43], v[72:75], v[32:35]\n"
    "buffer_load_dwordx4 v[148:151], v223, s[20:23], s77 offen offset:1024 sc1\n"
    "v_fma_f32 v101, v11, v11, v112\n"
    "v_mfma_f32_16x16x32_bf16 v[32:35], a[56:59], v[76:79], v[32:35]\n"
    "v_mul_f32 v98, v98, v96\n"
    "v_mfma_f32_16x16x32_bf16 v[32:35], a[72:75], v[80:83], v[32:35]\n"
    "ds_read_b128 v[60:63], v120 offset:29696\n"
    "v_mul_f32 v99, v99, v97\n"
    "v_mfma_f32_16x16x32_bf16 v[32:35], a[88:91], v[84:87], v[32:35]\n"
    "v_cvt_pk_bf16_f32 v88, v98, v99\n"
    "v_mul_f32 v100, v100, v10\n"
    "v_mfma_f32_16x16x32_bf16 v[36:39], a[12:15], v[64:67], 0\n"
    "v_mul_f32 v101, v101, v11\n"
    "v_mfma_f32_16x16x32_bf16 v[36:39], a[28:31], v[68:71], v[36:39]\n"
    "buffer_load_dwordx4 v[136:139], v222, s[20:23], s77 offen offset:2048 sc1\n"
    "v_mfma_f32_16x16x32_bf16 v[36:39], a[44:47], v[72:75], v[36:39]\n"
    "v_exp_f32 v100, -v100\n"
    "v_mfma_f32_16x16x32_bf16 v[36:39], a[60:63], v[76:79], v[36:39]\n"
    "v_exp_f32 v101, -v101\n"
    "v_mfma_f32_16x16x32_bf16 v[36:39], a[76:79], v[80:83], v[36:39]\n"
    "v_mul_f32 v102, v10, v14\n"
    "v_mfma_f32_16x16x32_bf16 v[36:39], a[92:95], v[84:87], v[36:39]\n"
    "v_mul_f32 v103, v11, v15\n"
    "s_waitcnt lgkmcnt(0)\n"
    "v_mfma_f32_16x16x32_bf16 v[24:27], a[96:99], v[40:43], v[24:27]\n"
    "buffer_load_dwordx4 v[152:155], v223, s[20:23], s77 offen offset:2048 sc1\n"
    "v_add_f32 v100, 1.0, v100\n"
    "v_mfma_f32_16x16x32_bf16 v[24:27], a[112:115], v[44:47], v[24:27]\n"
    "ds_read_b128 v[64:67], v120 offset:30720\n"
    "v_mfma_f32_16x16x32_bf16 v[24:27], a[128:131], v[48:51], v[24:27]\n"
    "v_add_f32 v101, 1.0, v101\n"
    "v_mfma_f32_16x16x32_bf16 v[24:27], a[144:147], v[52:55], v[24:27]\n"
    "v_rcp_f32 v100, v100\n"
    "v_mfma_f32_16x16x32_bf16 v[24:27], a[160:163], v[56:59], v[24:27]\n"
    "ds_read_b128 v[68:71], v120 offset:31744\n"
    "v_mfma_f32_16x16x32_bf16 v[24:27], a[176:179], v[60:63], v[24:27]\n"
    "buffer_load_dwordx4 v[140:143], v222, s[20:23], s77 offen offset:3072 sc1\n"
    "v_mfma_f32_16x16x32_bf16 v[28:31], a[100:103], v[40:43], v[28:31]\n"
    "v_rcp_f32 v101, v101\n"
    "v_mfma_f32_16x16x32_bf16 v[28:31], a[116:119], v[44:47], v[28:31]\n"
    "ds_read_b128 v[72:75], v120 offset:32768\n"
    "v_fma_f32 v96, v16, v16, v112\n"
    "v_mfma_f32_16x16x32_bf16 v[28:31], a[132:135], v[48:51], v[28:31]\n"
    "v_fma_f32 v97, v17, v17, v112\n"
    "v_mul_f32 v102, v102, v100\n"
    "v_mfma_f32_16x16x32_bf16 v[28:31], a[148:151], v[52:55], v[28:31]\n"
    "v_mul_f32 v103, v103, v101\n"
    "v_cvt_pk_bf16_f32 v89, v102, v103\n"
    "v_mfma_f32_16x16x32_bf16 v[28:31], a[164:167], v[56:59], v[28:31]\n"
    "ds_read_b128 v[76:79], v120 offset:33792\n"
    "v_mul_f32 v96, v96, v16\n"
    "v_mfma_f32_16x16x32_bf16 v[28:31], a[180:183], v[60:63], v[28:31]\n"
    "buffer_load_dwordx4 v[156:159], v223, s[20:23], s77 offen offset:3072 sc1\n"
    "v_readlane_b32 s77, v5, 3\n"
    "v_mfma_f32_16x16x32_bf16 v[32:35], a[104:107], v[40:43], v[32:35]\n"
    "s_add_u32 m0, s83, 135168\n"
    "v_mfma_f32_16x16x32_bf16 v[32:35], a[120:123], v[44:47], v[32:35]\n"
    "ds_read_b128 v[80:83], v120 offset:34816\n"
    "v_mul_f32 v97, v97, v17\n"
    "v_mfma_f32_16x16x32_bf16 v[32:35], a[136:139], v[48:51], v[32:35]\n"
    "v_exp_f32 v96, -v96\n"
    "v_mfma_f32_16x16x32_bf16 v[32:35], a[152:155], v[52:55], v[32:35]\n"
    "v_mfma_f32_16x16x32_bf16 v[32:35], a[168:171], v[56:59], v[32:35]\n"
    "ds_read_b128 v[84:87], v120 offset:35840\n"
    "v_mfma_f32_16x16x32_bf16 v[32:35], a[184:187], v[60:63], v[32:35]\n"
    "buffer_load_dwordx4 v6, s[20:23], s77 offen offset:0 sc1 lds\n"
    "v_mfma_f32_16x16x32_bf16 v[36:39], a[108:111], v[40:43], v[36:39]\n"
    "v_exp_f32 v97, -v97\n"
    "v_mfma_f32_16x16x32_bf16 v[36:39], a[124:127], v[44:47], v[36:39]\n"
    "v_mul_f32 v98, v16, v20\n"
    "v_mul_f32 v99, v17, v21\n"
    "v_mfma_f32_16x16x32_bf16 v[36:39], a[140:143], v[48:51], v[36:39]\n"
    "v_add_f32 v96, 1.0, v96\n"
    "v_add_f32 v97, 1.0, v97\n"
    "v_mfma_f32_16x16x32_bf16 v[36:39], a[156:159], v[52:55], v[36:39]\n"
    "v_rcp_f32 v96, v96\n"
    "v_mfma_f32_16x16x32_bf16 v[36:39], a[172:175], v[56:59], v[36:39]\n"
    "buffer_load_dwordx4 v6, s[20:23], s77 offen offset:1024 sc1 lds\n"
    "v_mfma_f32_16x16x32_bf16 v[36:39], a[188:191], v[60:63], v[36:39]\n"
    "s_waitcnt vmcnt(17) lgkmcnt(0)\n"
    "s_barrier\n"
    "v_mfma_f32_16x16x32_bf16 v[24:27], a[192:195], v[64:67], v[24:27]\n"
    "v_rcp_f32 v97, v97\n"
    "v_mfma_f32_16x16x32_bf16 v[24:27], a[208:211], v[68:71], v[24:27]\n"
    "ds_read_b128 v[40:43], v4 offset:0\n"
    "v_fma_f32 v100, v18, v18, v112\n"
    "v_mfma_f32_16x16x32_bf16 v[24:27], a[224:227], v[72:75], v[24:27]\n"
    "v_fma_f32 v101, v19, v19, v112\n"
    "v_mul_f32 v98, v98, v96\n"
    "v_mfma_f32_16x16x32_bf16 v[24:27], a[240:243], v[76:79], v[24:27]\n"
    "buffer_load_dwordx4 v6, s[20:23], s77 offen offset:2048 sc1 lds\n"
    "v_mul_f32 v99, v99, v97\n"
    "v_mfma_f32_16x16x32_bf16 v[24:27], v[224:227], v[80:83], v[24:27]\n"
    "ds_read_b128 v[44:47], v4 offset:1024\n"
    "v_mfma_f32_16x16x32_bf16 v[24:27], v[240:243], v[84:87], v[24:27]\n"
    "v_cvt_pk_bf16_f32 v90, v98, v99\n"
    "v_mul_f32 v100, v100, v18\n"
    "v_mfma_f32_16x16x32_bf16 v[28:31], a[196:199], v[64:67], v[28:31]\n"
    "v_mul_f32 v101, v101, v19\n"
    "v_mfma_f32_16x16x32_bf16 v[28:31], a[212:215], v[68:71], v[28:31]\n"
    "ds_read_b128 v[48:51], v4 offset:2048\n"
    "v_mfma_f32_16x16x32_bf16 v[28:31], a[228:231], v[72:75], v[28:31]\n"
    "buffer_load_dwordx4 v6, s[20:23], s77 offen offset:3072 sc1 lds\n"
    "s_add_u32 m0, s83, 139264\n"
    "v_mfma_f32_16x16x32_bf16 v[28:31], a[244:247], v[76:79], v[28:31]\n"
    "v_exp_f32 v100, -v100\n"
    "v_mfma_f32_16x16x32_bf16 v[28:31], v[228:231], v[80:83], v[28:31]\n"
    "ds_read_b128 v[52:55], v4 offset:3072\n"
    "v_mfma_f32_16x16x32_bf16 v[28:31], v[244:247], v[84:87], v[28:31]\n"
    "v_exp_f32 v101, -v101\n"
    "v_mfma_f32_16x16x32_bf16 v[32:35], a[200:203], v[64:67], v[32:35]\n"
    "v_mul_f32 v102, v18, v22\n"
    "v_mul_f32 v103, v19, v23\n"
    "v_mfma_f32_16x16x32_bf16 v[32:35], a[216:219], v[68:71], v[32:35]\n"
    "ds_read_b128 v[56:59], v4 offset:4096\n"
    "v_add_f32 v100, 1.0, v100\n"
    "v_mfma_f32_16x16x32_bf16 v[32:35], a[232:235], v[72:75], v[32:35]\n"
    "buffer_load_dwordx4 v7, s[20:23], s77 offen offset:0 sc1 lds\n"
    "v_add_f32 v101, 1.0, v101\n"
    "v_mfma_f32_16x16x32_bf16 v[32:35], a[248:251], v[76:79], v[32:35]\n"
    "v_mfma_f32_16x16x32_bf16 v[32:35], v[232:235], v[80:83], v[32:35]\n"
    "ds_read_b128 v[60:63], v4 offset:5120\n"
    "v_mfma_f32_16x16x32_bf16 v[32:35], v[248:251], v[84:87], v[32:35]\n"
    "v_rcp_f32 v100, v100\n"
    "v_mfma_f32_16x16x32_bf16 v[36:39], a[204:207], v[64:67], v[36:39]\n"
    "v_rcp_f32 v101, v101\n"
    "v_mfma_f32_16x16x32_bf16 v[36:39], a[220:223], v[68:71], v[36:39]\n"
    "s_nop 0\n"
    "v_mul_f32 v102, v102, v100\n"
    "v_mfma_f32_16x16x32_bf16 v[36:39], a[236:239], v[72:75], v[36:39]\n"
    "v_mul_f32 v103, v103, v101\n"
    "v_cvt_pk_bf16_f32 v91, v102, v103\n"
    "v_mfma_f32_16x16x32_bf16 v[36:39], a[252:255], v[76:79], v[36:39]\n"
    "v_mfma_f32_16x16x32_bf16 v[36:39], v[236:239], v[80:83], v[36:39]\n"
    "v_mfma_f32_16x16x32_bf16 v[36:39], v[252:255], v[84:87], v[36:39]\n"
    "s_waitcnt lgkmcnt(0)\n"
    "v_mfma_f32_16x16x32_bf16 v[8:11], a[0:3], v[40:43], 0\n"
    "v_fma_f32 v96, v24, v24, v112\n"
    "v_fma_f32 v97, v25, v25, v112\n"
    "v_mfma_f32_16x16x32_bf16 v[8:11], a[16:19], v[44:47], v[8:11]\n"
    "ds_read_b128 v[64:67], v4 offset:6144\n"
    "v_readlane_b32 s77, v5, 17\n"
    "v_mfma_f32_16x16x32_bf16 v[8:11], a[32:35], v[48:51], v[8:11]\n"
    "v_mul_f32 v96, v96, v24\n"
    "v_mul_f32 v97, v97, v25\n"
    "v_mfma_f32_16x16x32_bf16 v[8:11], a[48:51], v[52:55], v[8:11]\n"
    "v_exp_f32 v96, -v96\n"
    "v_mfma_f32_16x16x32_bf16 v[8:11], a[64:67], v[56:59], v[8:11]\n"
    "ds_read_b128 v[68:71], v4 offset:7168\n"
    "v_mfma_f32_16x16x32_bf16 v[8:11], a[80:83], v[60:63], v[8:11]\n"
    "v_exp_f32 v97, -v97\n"
    "v_mfma_f32_16x16x32_bf16 v[12:15], a[4:7], v[40:43], 0\n"
    "buffer_load_dwordx4 v[160:163], v222, s[20:23], s77 offen offset:0 sc1\n"
    "v_mul_f32 v98, v24, v28\n"
    "v_mfma_f32_16x16x32_bf16 v[12:15], a[20:23], v[44:47], v[12:15]\n"
    "ds_read_b128 v[72:75], v4 offset:8192\n"
    "v_mul_f32 v99, v25, v29\n"
    "v_mfma_f32_16x16x32_bf16 v[12:15], a[36:39], v[48:51], v[12:15]\n"
    "v_add_f32 v96, 1.0, v96\n"
    "v_add_f32 v97, 1.0, v97\n"
    "v_mfma_f32_16x16x32_bf16 v[12:15], a[52:55], v[52:55], v[12:15]\n"
    "v_rcp_f32 v96, v96\n"
    "v_mfma_f32_16x16x32_bf16 v[12:15], a[68:71], v[56:59], v[12:15]\n"
    "ds_read_b128 v[76:79], v4 offset:9216\n"
    "v_mfma_f32_16x16x32_bf16 v[12:15], a[84:87], v[60:63], v[12:15]\n"
    "buffer_load_dwordx4 v[176:179], v223, s[20:23], s77 offen offset:0 sc1\n"
    "v_mfma_f32_16x16x32_bf16 v[16:19], a[8:11], v[40:43], 0\n"
    "v_rcp_f32 v97, v97\n"
    "v_mfma_f32_16x16x32_bf16 v[16:19], a[24:27], v[44:47], v[16:19]\n"
    "ds_read_b128 v[80:83], v4 offset:10240\n"
    "v_fma_f32 v100, v26, v26, v112\n"
    "v_mfma_f32_16x16x32_bf16 v[16:19], a[40:43], v[48:51], v[16:19]\n"
    "v_fma_f32 v101, v27, v27, v112\n"
    "v_mul_f32 v98, v98, v96\n"
    "v_mfma_f32_16x16x32_bf16 v[16:19], a[56:59], v[52:55], v[16:19]\n"
    "buffer_load_dwordx4 v[164:167], v222, s[20:23], s77 offen offset:1024 sc1\n"
    "v_mul_f32 v99, v99, v97\n"
    "v_mfma_f32_16x16x32_bf16 v[16:19], a[72:75], v[56:59], v[16:19]\n"
    "ds_read_b128 v[84:87], v4 offset:11264\n"
    "v_cvt_pk_bf16_f32 v92, v98, v99\n"
    "v_mfma_f32_16x16x32_bf16 v[16:19], a[88:91], v[60:63], v[16:19]\n"
    "v_mul_f32 v100, v100, v26\n"
    "v_mul_f32 v101, v101, v27\n"
    "v_mfma_f32_16x16x32_bf16 v[20:23], a[12:15], v[40:43], 0\n"
    "v_exp_f32 v100, -v100\n"
    "v_mfma_f32_16x16x32_bf16 v[20:23], a[28:31], v[44:47], v[20:23]\n"
    "buffer_load_dwordx4 v[180:183], v223, s[20:23], s77 offen offset:1024 sc1\n"
    "v_mfma_f32_16x16x32_bf16 v[20:23], a[44:47], v[48:51], v[20:23]\n"
    "v_exp_f32 v101, -v101\n"
    "v_mfma_f32_16x16x32_bf16 v[20:23], a[60:63], v[52:55], v[20:23]\n"
    "v_mul_f32 v102, v26, v30\n"
    "v_mfma_f32_16x16x32_bf16 v[20:23], a[76:79], v[56:59], v[20:23]\n"
    "v_mul_f32 v103, v27, v31\n"
    "v_add_f32 v100, 1.0, v100\n"
    "v_mfma_f32_16x16x32_bf16 v[20:23], a[92:95], v[60:63], v[20:23]\n"
    "buffer_load_dwordx4 v[168:171], v222, s[20:23], s77 offen offset:2048 sc1\n"
    "s_waitcnt lgkmcnt(0)\n"
    "v_mfma_f32_16x16x32_bf16 v[8:11], a[96:99], v[64:67], v[8:11]\n"
    "v_add_f32 v101, 1.0, v101\n"
    "v_mfma_f32_16x16x32_bf16 v[8:11], a[112:115], v[68:71], v[8:11]\n"
    "ds_read_b128 v[40:43], v4 offset:12288\n"
    "v_mfma_f32_16x16x32_bf16 v[8:11], a[128:131], v[72:75], v[8:11]\n"
    "v_rcp_f32 v100, v100\n"
    "v_mfma_f32_16x16x32_bf16 v[8:11], a[144:147], v[76:79], v[8:11]\n"
    "buffer_load_dwordx4 v[184:187], v223, s[20:23], s77 offen offset:2048 sc1\n"
    "v_mfma_f32_16x16x32_bf16 v[8:11], a[160:163], v[80:83], v[8:11]\n"
    "ds_read_b128 v[44:47], v4 offset:13312\n"
    "v_mfma_f32_16x16x32_bf16 v[8:11], a[176:179], v[84:87], v[8:11]\n"
    "v_rcp_f32 v101, v101\n"
    "v_mfma_f32_16x16x32_bf16 v[12:15], a[100:103], v[64:67], v[12:15]\n"
    "buffer_store_dwordx4 v[88:91], v4, s[24:27], s74 offen sc1\n"
    "v_readlane_b32 s74, v214, 32\n"
    "v_mfma_f32_16x16x32_bf16 v[12:15], a[116:119], v[68:71], v[12:15]\n"
    "ds_read_b128 v[48:51], v4 offset:14336\n"
    "v_fma_f32 v96, v32, v32, v112\n"
    "v_mfma_f32_16x16x32_bf16 v[12:15], a[132:135], v[72:75], v[12:15]\n"
    "buffer_load_dwordx4 v[172:175], v222, s[20:23], s77 offen offset:3072 sc1\n"
    "v_fma_f32 v97, v33, v33, v112\n"
    "v_mfma_f32_16x16x32_bf16 v[12:15], a[148:151], v[76:79], v[12:15]\n"
    "v_mul_f32 v102, v102, v100\n"
    "v_mul_f32 v103, v103, v101\n"
    "v_mfma_f32_16x16x32_bf16 v[12:15], a[164:167], v[80:83], v[12:15]\n"
    "ds_read_b128 v[52:55], v4 offset:15360\n"
    "v_cvt_pk_bf16_f32 v93, v102, v103\n"
    "v_mfma_f32_16x16x32_bf16 v[12:15], a[180:183], v[84:87], v[12:15]\n"
    "v_mul_f32 v96, v96, v32\n"
    "v_mul_f32 v97, v97, v33\n"
    "v_mfma_f32_16x16x32_bf16 v[16:19], a[104:107], v[64:67], v[16:19]\n"
    "buffer_load_dwordx4 v[188:191], v223, s[20:23], s77 offen offset:3072 sc1\n"
    "s_mov_b32 s85, 270336\n"
    "v_mfma_f32_16x16x32_bf16 v[16:19], a[120:123], v[68:71], v[16:19]\n"
    "ds_read_b128 v[56:59], v4 offset:16384\n"
    "v_mfma_f32_16x16x32_bf16 v[16:19], a[136:139], v[72:75], v[16:19]\n"
    "v_exp_f32 v96, -v96\n"
    "v_mfma_f32_16x16x32_bf16 v[16:19], a[152:155], v[76:79], v[16:19]\n"
    "v_exp_f32 v97, -v97\n"
    "v_mfma_f32_16x16x32_bf16 v[16:19], a[168:171], v[80:83], v[16:19]\n"
    "ds_read_b128 v[60:63], v4 offset:17408\n"
    "v_mul_f32 v98, v32, v36\n"
    "v_mfma_f32_16x16x32_bf16 v[16:19], a[184:187], v[84:87], v[16:19]\n"
    "buffer_load_dwordx2 v[104:105], v110, s[20:23], s85 offen sc1\n"
    "v_readlane_b32 s77, v5, 4\n"
    "v_mfma_f32_16x16x32_bf16 v[20:23], a[108:111], v[64:67], v[20:23]\n"
    "s_add_u32 m0, s83, 36864\n"
    "v_mul_f32 v99, v33, v37\n"
    "v_mfma_f32_16x16x32_bf16 v[20:23], a[124:127], v[68:71], v[20:23]\n"
    "v_add_f32 v96, 1.0, v96\n"
    "v_add_f32 v97, 1.0, v97\n"
    "v_mfma_f32_16x16x32_bf16 v[20:23], a[140:143], v[72:75], v[20:23]\n"
    "v_mfma_f32_16x16x32_bf16 v[20:23], a[156:159], v[76:79], v[20:23]\n"
    "v_rcp_f32 v96, v96\n"
    "v_mfma_f32_16x16x32_bf16 v[20:23], a[172:175], v[80:83], v[20:23]\n"
    "buffer_load_dwordx4 v6, s[20:23], s77 offen offset:0 sc1 lds\n"
    "v_mfma_f32_16x16x32_bf16 v[20:23], a[188:191], v[84:87], v[20:23]\n"
    "s_waitcnt lgkmcnt(0)\n"
    "v_mfma_f32_16x16x32_bf16 v[8:11], a[192:195], v[40:43], v[8:11]\n"
    "v_rcp_f32 v97, v97\n"
    "v_mfma_f32_16x16x32_bf16 v[8:11], a[208:211], v[44:47], v[8:11]\n"
    "ds_read_b128 v[64:67], v4 offset:18432\n"
    "v_fma_f32 v100, v34, v34, v112\n"
    "v_mfma_f32_16x16x32_bf16 v[8:11], a[224:227], v[48:51], v[8:11]\n"
    "buffer_load_dwordx4 v6, s[20:23], s77 offen offset:1024 sc1 lds\n"
    "v_fma_f32 v101, v35, v35, v112\n"
    "v_mfma_f32_16x16x32_bf16 v[8:11], a[240:243], v[52:55], v[8:11]\n"
    "v_mul_f32 v98, v98, v96\n"
    "v_mul_f32 v99, v99, v97\n"
    "v_mfma_f32_16x16x32_bf16 v[8:11], v[224:227], v[56:59], v[8:11]\n"
    "ds_read_b128 v[68:71], v4 offset:19456\n"
    "v_cvt_pk_bf16_f32 v94, v98, v99\n"
    "v_mfma_f32_16x16x32_bf16 v[8:11], v[240:243], v[60:63], v[8:11]\n"
    "v_mul_f32 v100, v100, v34\n"
    "v_mul_f32 v101, v101, v35\n"
    "v_mfma_f32_16x16x32_bf16 v[12:15], a[196:199], v[40:43], v[12:15]\n"
    "buffer_load_dwordx4 v6, s[20:23], s77 offen offset:2048 sc1 lds\n"
    "v_mfma_f32_16x16x32_bf16 v[12:15], a[212:215], v[44:47], v[12:15]\n"
    "ds_read_b128 v[72:75], v4 offset:20480\n"
    "v_mfma_f32_16x16x32_bf16 v[12:15], a[228:231], v[48:51], v[12:15]\n"
    "v_exp_f32 v100, -v100\n"
    "v_mfma_f32_16x16x32_bf16 v[12:15], a[244:247], v[52:55], v[12:15]\n"
    "v_exp_f32 v101, -v101\n"
    "v_mfma_f32_16x16x32_bf16 v[12:15], v[228:231], v[56:59], v[12:15]\n"
    "ds_read_b128 v[76:79], v4 offset:21504\n"
    "v_mul_f32 v102, v34, v38\n"
    "v_mfma_f32_16x16x32_bf16 v[12:15], v[244:247], v[60:63], v[12:15]\n"
    "buffer_load_dwordx4 v6, s[20:23], s77 offen offset:3072 sc1 lds\n"
    "s_add_u32 m0, s83, 40960\n"
    "v_mfma_f32_16x16x32_bf16 v[16:19], a[200:203], v[40:43], v[16:19]\n"
    "v_mul_f32 v103, v35, v39\n"
    "v_add_f32 v100, 1.0, v100\n"
    "v_mfma_f32_16x16x32_bf16 v[16:19], a[216:219], v[44:47], v[16:19]\n"
    "ds_read_b128 v[80:83], v4 offset:22528\n"
    "v_add_f32 v101, 1.0, v101\n"
    "v_mfma_f32_16x16x32_bf16 v[16:19], a[232:235], v[48:51], v[16:19]\n"
    "v_rcp_f32 v100, v100\n"
    "v_mfma_f32_16x16x32_bf16 v[16:19], a[248:251], v[52:55], v[16:19]\n"
    "buffer_load_dwordx4 v7, s[20:23], s77 offen offset:0 sc1 lds\n"
    "v_readlane_b32 s77, v5, 1\n"
    "v_mfma_f32_16x16x32_bf16 v[16:19], v[232:235], v[56:59], v[16:19]\n"
    "ds_read_b128 v[84:87], v4 offset:23552\n"
    "s_add_u32 m0, s72, 56320\n"
    "v_mfma_f32_16x16x32_bf16 v[16:19], v[248:251], v[60:63], v[16:19]\n"
    "v_mfma_f32_16x16x32_bf16 v[20:23], a[204:207], v[40:43], v[20:23]\n"
    "v_rcp_f32 v101, v101\n"
    "v_mfma_f32_16x16x32_bf16 v[20:23], a[220:223], v[44:47], v[20:23]\n"
    "s_nop 0\n"
    "v_mul_f32 v102, v102, v100\n"
    "v_mfma_f32_16x16x32_bf16 v[20:23], a[236:239], v[48:51], v[20:23]\n"
    "buffer_load_dwordx4 v7, s[20:23], s77 offen offset:1024 sc1 lds\n"
    "v_mul_f32 v103, v103, v101\n"
    "v_mfma_f32_16x16x32_bf16 v[20:23], a[252:255], v[52:55], v[20:23]\n"
    "v_cvt_pk_bf16_f32 v95, v102, v103\n"
    "v_mfma_f32_16x16x32_bf16 v[20:23], v[236:239], v[56:59], v[20:23]\n"
    "v_mfma_f32_16x16x32_bf16 v[20:23], v[252:255], v[60:63], v[20:23]\n"
    "buffer_store_dwordx4 v[92:95], v4, s[24:27], s74 offen sc1\n"
    "s_waitcnt lgkmcnt(0)\n"
    "v_mfma_f32_16x16x32_bf16 v[24:27], a[0:3], v[64:67], 0\n"
    "v_readlane_b32 s74, v214, 33\n"
    "v_fma_f32 v96, v8, v8, v112\n"
    "v_mfma_f32_16x16x32_bf16 v[24:27], a[16:19], v[68:71], v[24:27]\n"
    "ds_read_b128 v[40:43], v4 offset:24576\n"
    "v_fma_f32 v97, v9, v9, v112\n"
    "v_mfma_f32_16x16x32_bf16 v[24:27], a[32:35], v[72:75], v[24:27]\n"
    "buffer_load_dwordx4 v7, s[20:23], s77 offen offset:2048 sc1 lds\n"
    "v_readlane_b32 s77, v5, 2\n"
    "v_mfma_f32_16x16x32_bf16 v[24:27], a[48:51], v[76:79], v[24:27]\n"
    "s_add_u32 m0, s72, 64512\n"
    "v_mul_f32 v96, v96, v8\n"
    "v_mfma_f32_16x16x32_bf16 v[24:27], a[64:67], v[80:83], v[24:27]\n"
    "ds_read_b128 v[44:47], v4 offset:25600\n"
    "v_mul_f32 v97, v97, v9\n"
    "v_mfma_f32_16x16x32_bf16 v[24:27], a[80:83], v[84:87], v[24:27]\n"
    "v_exp_f32 v96, -v96\n"
    "v_mfma_f32_16x16x32_bf16 v[28:31], a[4:7], v[64:67], 0\n"
    "v_exp_f32 v97, -v97\n"
    "v_mfma_f32_16x16x32_bf16 v[28:31], a[20:23], v[68:71], v[28:31]\n"
    "ds_read_b128 v[48:51], v4 offset:26624\n"
    "v_mul_f32 v98, v8, v12\n"
    "v_mfma_f32_16x16x32_bf16 v[28:31], a[36:39], v[72:75], v[28:31]\n"
    "buffer_load_dwordx4 v7, s[20:23], s77 offen offset:1024 sc1 lds\n"
    "v_mul_f32 v99, v9, v13\n"
    "v_mfma_f32_16x16x32_bf16 v[28:31], a[52:55], v[76:79], v[28:31]\n"
    "v_add_f32 v96, 1.0, v96\n"
    "v_add_f32 v97, 1.0, v97\n"
    "v_mfma_f32_16x16x32_bf16 v[28:31], a[68:71], v[80:83], v[28:31]\n"
    "ds_read_b128 v[52:55], v4 offset:27648\n"
    "v_mfma_f32_16x16x32_bf16 v[28:31], a[84:87], v[84:87], v[28:31]\n"
    "v_rcp_f32 v96, v96\n"
    "v_mfma_f32_16x16x32_bf16 v[32:35], a[8:11], v[64:67], 0\n"
    "buffer_load_dwordx4 v7, s[20:23], s77 offen offset:2048 sc1 lds\n"
    "v_mfma_f32_16x16x32_bf16 v[32:35], a[24:27], v[68:71], v[32:35]\n"
    "ds_read_b128 v[56:59], v4 offset:28672\n"
    "s_waitcnt vmcnt(10)\n"
    "v_mfma_f32_16x16x32_bf16 v[32:35], a[40:43], v[72:75], v[32:35]\n"
    "v_xor_b32 v106, s40, v104\n"
    "v_xor_b32 v107, s41, v105\n"
    "v_mfma_f32_16x16x32_bf16 v[32:35], a[56:59], v[76:79], v[32:35]\n"
    "v_or_b32 v106, v106, v107\n"
    "v_cmp_ne_u32 vcc, 0, v106\n"
    "v_mfma_f32_16x16x32_bf16 v[32:35], a[72:75], v[80:83], v[32:35]\n"
    "ds_read_b128 v[60:63], v4 offset:29696\n"
    "s_nop 1\n"
    "v_mfma_f32_16x16x32_bf16 v[32:35], a[88:91], v[84:87], v[32:35]\n"
    "s_and_b64 vcc, vcc, 255\n"
    "s_cbranch_vccnz LG1_fm_s\n"
    "LG1_fm_r:\n"
    "v_mfma_f32_16x16x32_bf16 v[36:39], a[12:15], v[64:67], 0\n"
    "v_rcp_f32 v97, v97\n"
    "v_mfma_f32_16x16x32_bf16 v[36:39], a[28:31], v[68:71], v[36:39]\n"
    "v_fma_f32 v100, v10, v10, v112\n"
    "v_fma_f32 v101, v11, v11, v112\n"
    "v_mfma_f32_16x16x32_bf16 v[36:39], a[44:47], v[72:75], v[36:39]\n"
    "v_mul_f32 v98, v98, v96\n"
    "v_mul_f32 v99, v99, v97\n"
    "v_mfma_f32_16x16x32_bf16 v[36:39], a[60:63], v[76:79], v[36:39]\n"
    "v_cvt_pk_bf16_f32 v88, v98, v99\n"
    "v_mul_f32 v100, v100, v10\n"
    "v_mfma_f32_16x16x32_bf16 v[36:39], a[76:79], v[80:83], v[36:39]\n"
    "v_mul_f32 v101, v101, v11\n"
    "v_mfma_f32_16x16x32_bf16 v[36:39], a[92:95], v[84:87], v[36:39]\n"
    "v_exp_f32 v100, -v100\n"
    "s_waitcnt lgkmcnt(0)\n"
    "v_mfma_f32_16x16x32_bf16 v[24:27], a[96:99], v[40:43], v[24:27]\n"
    "v_exp_f32 v101, -v101\n"
    "v_mfma_f32_16x16x32_bf16 v[24:27], a[112:115], v[44:47], v[24:27]\n"
    "ds_read_b128 v[64:67], v4 offset:30720\n"
    "v_mul_f32 v102, v10, v14\n"
    "v_mfma_f32_16x16x32_bf16 v[24:27], a[128:131], v[48:51], v[24:27]\n"
    "v_readlane_b32 s77, v5, 18\n"
    "v_mul_f32 v103, v11, v15\n"
    "v_mfma_f32_16x16x32_bf16 v[24:27], a[144:147], v[52:55], v[24:27]\n"
    "v_add_f32 v100, 1.0, v100\n"
    "v_add_f32 v101, 1.0, v101\n"
    "v_mfma_f32_16x16x32_bf16 v[24:27], a[160:163], v[56:59], v[24:27]\n"
    "ds_read_b128 v[68:71], v4 offset:31744\n"
    "v_mfma_f32_16x16x32_bf16 v[24:27], a[176:179], v[60:63], v[24:27]\n"
    "v_rcp_f32 v100, v100\n"
    "v_mfma_f32_16x16x32_bf16 v[28:31], a[100:103], v[40:43], v[28:31]\n"
    "v_rcp_f32 v101, v101\n"
    "v_mfma_f32_16x16x32_bf16 v[28:31], a[116:119], v[44:47], v[28:31]\n"
    "ds_read_b128 v[72:75], v4 offset:32768\n"
    "v_fma_f32 v96, v16, v16, v112\n"
    "v_mfma_f32_16x16x32_bf16 v[28:31], a[132:135], v[48:51], v[28:31]\n"
    "buffer_load_dwordx4 a[0:3], v222, s[20:23], s77 offen offset:0 sc1\n"
    "v_mfma_f32_16x16x32_bf16 v[28:31], a[148:151], v[52:55], v[28:31]\n"
    "v_fma_f32 v97, v17, v17, v112\n"
    "v_mul_f32 v102, v102, v100\n"
    "v_mfma_f32_16x16x32_bf16 v[28:31], a[164:167], v[56:59], v[28:31]\n"
    "ds_read_b128 v[76:79], v4 offset:33792\n"
    "v_mul_f32 v103, v103, v101\n"
    "v_mfma_f32_16x16x32_bf16 v[28:31], a[180:183], v[60:63], v[28:31]\n"
    "buffer_load_dwordx4 a[16:19], v223, s[20:23], s77 offen offset:0 sc1\n"
    "v_cvt_pk_bf16_f32 v89, v102, v103\n"
    "v_mfma_f32_16x16x32_bf16 v[32:35], a[104:107], v[40:43], v[32:35]\n"
    "v_mul_f32 v96, v96, v16\n"
    "v_mfma_f32_16x16x32_bf16 v[32:35], a[120:123], v[44:47], v[32:35]\n"
    "ds_read_b128 v[80:83], v4 offset:34816\n"
    "v_mul_f32 v97, v97, v17\n"
    "v_mfma_f32_16x16x32_bf16 v[32:35], a[136:139], v[48:51], v[32:35]\n"
    "v_exp_f32 v96, -v96\n"
    "v_mfma_f32_16x16x32_bf16 v[32:35], a[152:155], v[52:55], v[32:35]\n"
    "v_exp_f32 v97, -v97\n"
    "v_mfma_f32_16x16x32_bf16 v[32:35], a[168:171], v[56:59], v[32:35]\n"
    "ds_read_b128 v[84:87], v4 offset:35840\n"
    "v_mfma_f32_16x16x32_bf16 v[32:35], a[184:187], v[60:63], v[32:35]\n"
    "buffer_load_dwordx4 a[4:7], v222, s[20:23], s77 offen offset:1024 sc1\n"
    "v_mul_f32 v98, v16, v20\n"
    "v_mfma_f32_16x16x32_bf16 v[36:39], a[108:111], v[40:43], v[36:39]\n"
    "v_mul_f32 v99, v17, v21\n"
    "v_add_f32 v96, 1.0, v96\n"
    "v_mfma_f32_16x16x32_bf16 v[36:39], a[124:127], v[44:47], v[36:39]\n"
    "v_add_f32 v97, 1.0, v97\n"
    "v_mfma_f32_16x16x32_bf16 v[36:39], a[140:143], v[48:51], v[36:39]\n"
    "buffer_load_dwordx4 a[20:23], v223, s[20:23], s77 offen offset:1024 sc1\n"
    "v_mfma_f32_16x16x32_bf16 v[36:39], a[156:159], v[52:55], v[36:39]\n"
    "v_rcp_f32 v96, v96\n"
    "v_mfma_f32_16x16x32_bf16 v[36:39], a[172:175], v[56:59], v[36:39]\n"
    "v_rcp_f32 v97, v97\n"
    "v_mfma_f32_16x16x32_bf16 v[36:39], a[188:191], v[60:63], v[36:39]\n"
    "v_fma_f32 v100, v18, v18, v112\n"
    "s_waitcnt vmcnt(17) lgkmcnt(0)\n"
    "s_barrier\n"
    "v_mfma_f32_16x16x32_bf16 v[24:27], a[192:195], v[64:67], v[24:27]\n"
    "s_lshl_b32 s85, s33, 7\n"
    "s_add_u32 s85, s85, 237568\n"
    "v_mfma_f32_16x16x32_bf16 v[24:27], a[208:211], v[68:71], v[24:27]\n"
    "ds_read_b128 v[40:43], v121 offset:8192\n"
    "v_fma_f32 v101, v19, v19, v112\n"
    "v_mfma_f32_16x16x32_bf16 v[24:27], a[224:227], v[72:75], v[24:27]\n"
    "buffer_store_dwordx4 v[218:221], v215, s[20:23], s85 offen sc1\n"
    "s_lshl_b32 s85, s2, 7\n"
    "v_mfma_f32_16x16x32_bf16 v[24:27], a[240:243], v[76:79], v[24:27]\n"
    "s_add_u32 s85, s85, 155648\n"
    "v_mul_f32 v98, v98, v96\n"
    "v_mfma_f32_16x16x32_bf16 v[24:27], v[224:227], v[80:83], v[24:27]\n"
    "ds_read_b128 v[44:47], v121 offset:13312\n"
    "v_mul_f32 v99, v99, v97\n"
    "v_mfma_f32_16x16x32_bf16 v[24:27], v[240:243], v[84:87], v[24:27]\n"
    "buffer_store_dwordx4 v[218:221], v215, s[20:23], s85 offen sc1\n"
    "s_add_u32 s85, s9, 114688\n"
    "v_mfma_f32_16x16x32_bf16 v[28:31], a[196:199], v[64:67], v[28:31]\n"
    "buffer_store_dwordx4 v[218:221], v215, s[52:55], s85 offen sc1\n"
    "v_cvt_pk_bf16_f32 v90, v98, v99\n"
    "v_mfma_f32_16x16x32_bf16 v[28:31], a[212:215], v[68:71], v[28:31]\n"
    "ds_read_b128 v[48:51], v121 offset:18432\n"
    "v_mul_f32 v100, v100, v18\n"
    "v_mfma_f32_16x16x32_bf16 v[28:31], a[228:231], v[72:75], v[28:31]\n"
    "buffer_load_dwordx4 a[8:11], v222, s[20:23], s77 offen offset:2048 sc1\n"
    "v_mfma_f32_16x16x32_bf16 v[28:31], a[244:247], v[76:79], v[28:31]\n"
    "buffer_load_dwordx4 a[24:27], v223, s[20:23], s77 offen offset:2048 sc1\n"
    "v_mul_f32 v101, v101, v19\n"
    "v_mfma_f32_16x16x32_bf16 v[28:31], v[228:231], v[80:83], v[28:31]\n"
    "ds_read_b128 v[52:55], v121 offset:23552\n"
    "v_mfma_f32_16x16x32_bf16 v[28:31], v[244:247], v[84:87], v[28:31]\n"
    "buffer_load_dwordx4 a[12:15], v222, s[20:23], s77 offen offset:3072 sc1\n"
    "v_mfma_f32_16x16x32_bf16 v[32:35], a[200:203], v[64:67], v[32:35]\n"
    "v_exp_f32 v100, -v100\n"
    "v_mfma_f32_16x16x32_bf16 v[32:35], a[216:219], v[68:71], v[32:35]\n"
    "v_exp_f32 v101, -v101\n"
    "v_mfma_f32_16x16x32_bf16 v[32:35], a[232:235], v[72:75], v[32:35]\n"
    "buffer_load_dwordx4 a[28:31], v223, s[20:23], s77 offen offset:3072 sc1\n"
    "v_readlane_b32 s77, v5, 19\n"
    "v_mfma_f32_16x16x32_bf16 v[32:35], a[248:251], v[76:79], v[32:35]\n"
    "v_mul_f32 v102, v18, v22\n"
    "v_mul_f32 v103, v19, v23\n"
    "v_mfma_f32_16x16x32_bf16 v[32:35], v[232:235], v[80:83], v[32:35]\n"
    "v_add_f32 v100, 1.0, v100\n"
    "v_add_f32 v101, 1.0, v101\n"
    "v_mfma_f32_16x16x32_bf16 v[32:35], v[248:251], v[84:87], v[32:35]\n"
    "v_rcp_f32 v100, v100\n"
    "v_mfma_f32_16x16x32_bf16 v[36:39], a[204:207], v[64:67], v[36:39]\n"
    "v_rcp_f32 v101, v101\n"
    "v_mfma_f32_16x16x32_bf16 v[36:39], a[220:223], v[68:71], v[36:39]\n"
    "LS_9:\n"
    "s_cbranch_execz LS_ret\n"
    "buffer_load_dwordx4 a[32:35], v222, s[20:23], s77 offen offset:0 sc1\n"
    "s_nop 0\n"
    "v_mfma_f32_16x16x32_bf16 v[36:39], a[236:239], v[72:75], v[36:39]\n"
    "v_mul_f32 v102, v102, v100\n"
    "v_mfma_f32_16x16x32_bf16 v[36:39], a[252:255], v[76:79], v[36:39]\n"
    "v_mul_f32 v103, v103, v101\n"
    "v_cvt_pk_bf16_f32 v91, v102, v103\n"
    "v_mfma_f32_16x16x32_bf16 v[36:39], v[236:239], v[80:83], v[36:39]\n"
    "v_mfma_f32_16x16x32_bf16 v[36:39], v[252:255], v[84:87], v[36:39]\n"
    "buffer_load_dwordx4 a[48:51], v223, s[20:23], s77 offen offset:0 sc1\n"
    "LG2_go:\n"
    "s_waitcnt vmcnt(38) lgkmcnt(0)\n"
    "v_mfma_f32_16x16x32_bf16 v[224:227], v[40:43], v[128:131], 0\n"
    "buffer_store_dwordx4 v[88:91], v4, s[24:27], s74 offen sc1\n"
    "v_readlane_b32 s74, v214, 34\n"
    "v_mfma_f32_16x16x32_bf16 v[224:227], v[44:47], v[132:135], v[224:227]\n"
    "ds_read_b128 v[56:59], v121 offset:9216\n"
    "v_fma_f32 v96, v24, v24, v112\n"
    "v_mfma_f32_16x16x32_bf16 v[224:227], v[48:51], v[136:139], v[224:227]\n"
    "ds_read_b128 v[60:63], v121 offset:14336\n"
    "v_fma_f32 v97, v25, v25, v112\n"
    "v_mul_f32 v96, v96, v24\n"
    "v_mfma_f32_16x16x32_bf16 v[224:227], v[52:55], v[140:143], v[224:227]\n"
    "ds_read_b128 v[64:67], v121 offset:19456\n"
    "v_mul_f32 v97, v97, v25\n"
    "v_mfma_f32_16x16x32_bf16 v[228:231], v[40:43], v[144:147], 0\n"
    "ds_read_b128 v[68:71], v121 offset:24576\n"
    "v_exp_f32 v96, -v96\n"
    "v_mfma_f32_16x16x32_bf16 v[228:231], v[44:47], v[148:151], v[228:231]\n"
    "ds_read_b128 v[72:75], v121 offset:10240\n"
    "v_mfma_f32_16x16x32_bf16 v[228:231], v[48:51], v[152:155], v[228:231]\n"
    "ds_read_b128 v[76:79], v121 offset:15360\n"
    "v_exp_f32 v97, -v97\n"
    "v_mfma_f32_16x16x32_bf16 v[228:231], v[52:55], v[156:159], v[228:231]\n"
    "ds_read_b128 v[80:83], v121 offset:20480\n"
    "s_waitcnt lgkmcnt(3)\n"
    "v_mfma_f32_16x16x32_bf16 v[232:235], v[56:59], v[128:131], 0\n"
    "ds_read_b128 v[84:87], v121 offset:25600\n"
    "v_mul_f32 v98, v24, v28\n"
    "v_mul_f32 v99, v25, v29\n"
    "v_mfma_f32_16x16x32_bf16 v[232:235], v[60:63], v[132:135], v[232:235]\n"
    "ds_read_b128 v[40:43], v121 offset:11264\n"
    "v_add_f32 v96, 1.0, v96\n"
    "v_mfma_f32_16x16x32_bf16 v[232:235], v[64:67], v[136:139], v[232:235]\n"
    "buffer_load_dwordx4 a[36:39], v222, s[20:23], s77 offen offset:1024 sc1\n"
    "v_add_f32 v97, 1.0, v97\n"
    "v_mfma_f32_16x16x32_bf16 v[232:235], v[68:71], v[140:143], v[232:235]\n"
    "ds_read_b128 v[44:47], v121 offset:16384\n"
    "v_mfma_f32_16x16x32_bf16 v[236:239], v[56:59], v[144:147], 0\n"
    "ds_read_b128 v[48:51], v121 offset:21504\n"
    "v_rcp_f32 v96, v96\n"
    "v_mfma_f32_16x16x32_bf16 v[236:239], v[60:63], v[148:151], v[236:239]\n"
    "ds_read_b128 v[52:55], v121 offset:26624\n"
    "v_mfma_f32_16x16x32_bf16 v[236:239], v[64:67], v[152:155], v[236:239]\n"
    "buffer_load_dwordx4 a[52:55], v223, s[20:23], s77 offen offset:1024 sc1\n"
    "v_rcp_f32 v97, v97\n"
    "v_mfma_f32_16x16x32_bf16 v[236:239], v[68:71], v[156:159], v[236:239]\n"
    "buffer_load_dwordx4 a[40:43], v222, s[20:23], s77 offen offset:2048 sc1\n"
    "s_waitcnt lgkmcnt(4)\n"
    "v_mfma_f32_16x16x32_bf16 v[240:243], v[72:75], v[128:131], 0\n"
    "ds_read_b128 v[56:59], v121 offset:12288\n"
    "v_fma_f32 v100, v26, v26, v112\n"
    "v_fma_f32 v101, v27, v27, v112\n"
    "v_mfma_f32_16x16x32_bf16 v[240:243], v[76:79], v[132:135], v[240:243]\n"
    "ds_read_b128 v[60:63], v121 offset:17408\n"
    "v_mul_f32 v98, v98, v96\n"
    "v_mfma_f32_16x16x32_bf16 v[240:243], v[80:83], v[136:139], v[240:243]\n"
    "buffer_load_dwordx4 a[56:59], v223, s[20:23], s77 offen offset:2048 sc1\n"
    "v_mul_f32 v99, v99, v97\n"
    "v_cvt_pk_bf16_f32 v92, v98, v99\n"
    "v_mfma_f32_16x16x32_bf16 v[240:243], v[84:87], v[140:143], v[240:243]\n"
    "v_mul_f32 v100, v100, v26\n"
    "v_mul_f32 v101, v101, v27\n"
    "v_mfma_f32_16x16x32_bf16 v[244:247], v[72:75], v[144:147], 0\n"
    "ds_read_b128 v[64:67], v121 offset:22528\n"
    "v_exp_f32 v100, -v100\n"
    "v_mfma_f32_16x16x32_bf16 v[244:247], v[76:79], v[148:151], v[244:247]\n"
    "ds_read_b128 v[68:71], v121 offset:27648\n"
    "v_exp_f32 v101, -v101\n"
    "v_mfma_f32_16x16x32_bf16 v[244:247], v[80:83], v[152:155], v[244:247]\n"
    "buffer_load_dwordx4 a[44:47], v222, s[20:23], s77 offen offset:3072 sc1\n"
    "v_mul_f32 v102, v26, v30\n"
    "v_mfma_f32_16x16x32_bf16 v[244:247], v[84:87], v[156:159], v[244:247]\n"
    "v_mul_f32 v103, v27, v31\n"
    "v_add_f32 v100, 1.0, v100\n"
    "s_waitcnt lgkmcnt(4)\n"
    "v_mfma_f32_16x16x32_bf16 v[248:251], v[40:43], v[128:131], 0\n"
    "ds_read_b128 v[72:75], v122 offset:24576\n"
    "v_add_f32 v101, 1.0, v101\n"
    "v_mfma_f32_16x16x32_bf16 v[248:251], v[44:47], v[132:135], v[248:251]\n"
    "ds_read_b128 v[76:79], v122 offset:26624\n"
    "v_rcp_f32 v100, v100\n"
    "v_mfma_f32_16x16x32_bf16 v[248:251], v[48:51], v[136:139], v[248:251]\n"
    "buffer_load_dwordx4 a[60:63], v223, s[20:23], s77 offen offset:3072 sc1\n"
    "v_readlane_b32 s77, v5, 3\n"
    "v_mfma_f32_16x16x32_bf16 v[248:251], v[52:55], v[140:143], v[248:251]\n"
    "s_add_u32 m0, s72, 3072\n"
    "v_rcp_f32 v101, v101\n"
    "v_mfma_f32_16x16x32_bf16 v[252:255], v[40:43], v[144:147], 0\n"
    "ds_read_b128 v[80:83], v122 offset:28672\n"
    "v_fma_f32 v96, v32, v32, v112\n"
    "v_mfma_f32_16x16x32_bf16 v[252:255], v[44:47], v[148:151], v[252:255]\n"
    "ds_read_b128 v[84:87], v122 offset:30720\n"
    "v_fma_f32 v97, v33, v33, v112\n"
    "v_mul_f32 v102, v102, v100\n"
    "v_mfma_f32_16x16x32_bf16 v[252:255], v[48:51], v[152:155], v[252:255]\n"
    "v_mul_f32 v103, v103, v101\n"
    "v_cvt_pk_bf16_f32 v93, v102, v103\n"
    "v_mfma_f32_16x16x32_bf16 v[252:255], v[52:55], v[156:159], v[252:255]\n"
    "buffer_load_dwordx4 v7, s[20:23], s77 offen offset:1024 sc1 lds\n"
    "v_mul_f32 v96, v96, v32\n"
    "s_waitcnt lgkmcnt(4)\n"
    "v_mfma_f32_16x16x32_bf16 v[192:195], v[56:59], v[128:131], 0\n"
    "ds_read_b128 v[40:43], v122 offset:25600\n"
    "v_mul_f32 v97, v97, v33\n"
    "v_mfma_f32_16x16x32_bf16 v[192:195], v[60:63], v[132:135], v[192:195]\n"
    "ds_read_b128 v[44:47], v122 offset:27648\n"
    "v_exp_f32 v96, -v96\n"
    "v_mfma_f32_16x16x32_bf16 v[192:195], v[64:67], v[136:139], v[192:195]\n"
    "v_exp_f32 v97, -v97\n"
    "v_mfma_f32_16x16x32_bf16 v[192:195], v[68:71], v[140:143], v[192:195]\n"
    "buffer_load_dwordx4 v7, s[20:23], s77 offen offset:2048 sc1 lds\n"
    "v_readlane_b32 s77, v5, 4\n"
    "v_mul_f32 v98, v32, v36\n"
    "v_mfma_f32_16x16x32_bf16 v[196:199], v[56:59], v[144:147], 0\n"
    "ds_read_b128 v[48:51], v122 offset:29696\n"
    "s_add_u32 m0, s72, 11264\n"
    "v_mfma_f32_16x16x32_bf16 v[196:199], v[60:63], v[148:151], v[196:199]\n"
    "ds_read_b128 v[52:55], v122 offset:31744\n"
    "v_mul_f32 v99, v33, v37\n"
    "v_mfma_f32_16x16x32_bf16 v[196:199], v[64:67], v[152:155], v[196:199]\n"
    "v_add_f32 v96, 1.0, v96\n"
    "v_add_f32 v97, 1.0, v97\n"
    "v_mfma_f32_16x16x32_bf16 v[196:199], v[68:71], v[156:159], v[196:199]\n"
    "s_waitcnt vmcnt(24) lgkmcnt(0)\n"
    "s_barrier\n"
    "v_mfma_f32_16x16x32_bf16 v[200:203], v[72:75], v[128:131], 0\n"
    "ds_read_b128 v[56:59], v121 offset:28672\n"
    "v_rcp_f32 v96, v96\n"
    "v_mfma_f32_16x16x32_bf16 v[200:203], v[76:79], v[132:135], v[200:203]\n"
    "ds_read_b128 v[60:63], v121 offset:33792\n"
    "v_mfma_f32_16x16x32_bf16 v[200:203], v[80:83], v[136:139], v[200:203]\n"
    "buffer_load_dwordx4 v7, s[20:23], s77 offen offset:1024 sc1 lds\n"
    "v_rcp_f32 v97, v97\n"
    "v_mfma_f32_16x16x32_bf16 v[200:203], v[84:87], v[140:143], v[200:203]\n"
    "v_fma_f32 v100, v34, v34, v112\n"
    "v_fma_f32 v101, v35, v35, v112\n"
    "v_mfma_f32_16x16x32_bf16 v[204:207], v[72:75], v[144:147], 0\n"
    "ds_read_b128 v[64:67], v121 offset:38912\n"
    "v_mul_f32 v98, v98, v96\n"
    "v_mul_f32 v99, v99, v97\n"
    "v_mfma_f32_16x16x32_bf16 v[204:207], v[76:79], v[148:151], v[204:207]\n"
    "ds_read_b128 v[68:71], v121 offset:44032\n"
    "v_cvt_pk_bf16_f32 v94, v98, v99\n"
    "v_mfma_f32_16x16x32_bf16 v[204:207], v[80:83], v[152:155], v[204:207]\n"
    "buffer_load_dwordx4 v7, s[20:23], s77 offen offset:2048 sc1 lds\n"
    "v_readlane_b32 s77, v5, 5\n"
    "v_mul_f32 v100, v100, v34\n"
    "v_mfma_f32_16x16x32_bf16 v[204:207], v[84:87], v[156:159], v[204:207]\n"
    "s_add_u32 m0, s83, 73728\n"
    "v_mul_f32 v101, v101, v35\n"
    "v_mfma_f32_16x16x32_bf16 v[208:211], v[40:43], v[128:131], 0\n"
    "ds_read_b128 v[72:75], v121 offset:29696\n"
    "v_exp_f32 v100, -v100\n"
    "v_mfma_f32_16x16x32_bf16 v[208:211], v[44:47], v[132:135], v[208:211]\n"
    "ds_read_b128 v[76:79], v121 offset:34816\n"
    "v_mfma_f32_16x16x32_bf16 v[208:211], v[48:51], v[136:139], v[208:211]\n"
    "v_exp_f32 v101, -v101\n"
    "v_mul_f32 v102, v34, v38\n"
    "v_mfma_f32_16x16x32_bf16 v[208:211], v[52:55], v[140:143], v[208:211]\n"
    "buffer_load_dwordx4 v6, s[20:23], s77 offen offset:0 sc1 lds\n"
    "v_mul_f32 v103, v35, v39\n"
    "v_mfma_f32_16x16x32_bf16 v[116:119], v[40:43], v[144:147], 0\n"
    "ds_read_b128 v[80:83], v121 offset:39936\n"
    "v_add_f32 v100, 1.0, v100\n"
    "v_add_f32 v101, 1.0, v101\n"
    "v_mfma_f32_16x16x32_bf16 v[116:119], v[44:47], v[148:151], v[116:119]\n"
    "ds_read_b128 v[84:87], v121 offset:45056\n"
    "v_mfma_f32_16x16x32_bf16 v[116:119], v[48:51], v[152:155], v[116:119]\n"
    "buffer_load_dwordx4 v6, s[20:23], s77 offen offset:1024 sc1 lds\n"
    "v_rcp_f32 v100, v100\n"
    "v_mfma_f32_16x16x32_bf16 v[116:119], v[52:55], v[156:159], v[116:119]\n"
    "s_waitcnt vmcnt(37) lgkmcnt(4)\n"
    "v_mfma_f32_16x16x32_bf16 v[224:227], v[56:59], v[160:163], v[224:227]\n"
    "ds_read_b128 v[40:43], v121 offset:30720\n"
    "v_rcp_f32 v101, v101\n"
    "v_mfma_f32_16x16x32_bf16 v[224:227], v[60:63], v[164:167], v[224:227]\n"
    "ds_read_b128 v[44:47], v121 offset:35840\n"
    "s_nop 0\n"
    "v_mfma_f32_16x16x32_bf16 v[224:227], v[64:67], v[168:171], v[224:227]\n"
    "buffer_load_dwordx4 v6, s[20:23], s77 offen offset:2048 sc1 lds\n"
    "v_mul_f32 v102, v102, v100\n"
    "v_mul_f32 v103, v103, v101\n"
    "v_mfma_f32_16x16x32_bf16 v[224:227], v[68:71], v[172:175], v[224:227]\n"
    "v_cvt_pk_bf16_f32 v95, v102, v103\n"
    "v_mfma_f32_16x16x32_bf16 v[228:231], v[56:59], v[176:179], v[228:231]\n"
    "ds_read_b128 v[48:51], v121 offset:40960\n"
    "v_mfma_f32_16x16x32_bf16 v[228:231], v[60:63], v[180:183], v[228:231]\n"
    "ds_read_b128 v[52:55], v121 offset:46080\n"
    "v_mfma_f32_16x16x32_bf16 v[228:231], v[64:67], v[184:187], v[228:231]\n"
    "buffer_store_dwordx4 v[92:95], v4, s[24:27], s74 offen sc1\n"
    "v_mfma_f32_16x16x32_bf16 v[228:231], v[68:71], v[188:191], v[228:231]\n"
    "buffer_load_dwordx4 v6, s[20:23], s77 offen offset:3072 sc1 lds\n"
    "s_waitcnt lgkmcnt(4)\n"
    "v_mfma_f32_16x16x32_bf16 v[232:235], v[72:75], v[160:163], v[232:235]\n"
    "ds_read_b128 v[56:59], v121 offset:31744\n"
    "s_add_u32 m0, s83, 77824\n"
    "v_mfma_f32_16x16x32_bf16 v[232:235], v[76:79], v[164:167], v[232:235]\n"
    "ds_read_b128 v[60:63], v121 offset:36864\n"
    "v_mfma_f32_16x16x32_bf16 v[232:235], v[80:83], v[168:171], v[232:235]\n"
    "v_mfma_f32_16x16x32_bf16 v[232:235], v[84:87], v[172:175], v[232:235]\n"
    "buffer_load_dwordx4 v7, s[20:23], s77 offen offset:0 sc1 lds\n"
    "v_readlane_b32 s77, v5, 5\n"
    "v_mfma_f32_16x16x32_bf16 v[236:239], v[72:75], v[176:179], v[236:239]\n"
    "ds_read_b128 v[64:67], v121 offset:41984\n"
    "s_add_u32 m0, s72, 19456\n"
    "v_mfma_f32_16x16x32_bf16 v[236:239], v[76:79], v[180:183], v[236:239]\n"
    "ds_read_b128 v[68:71], v121 offset:47104\n"
    "s_mov_b32 s85, 237568\n"
    "v_mfma_f32_16x16x32_bf16 v[236:239], v[80:83], v[184:187], v[236:239]\n"
    "buffer_load_dwordx2 v[104:105], v110, s[20:23], s85 offen sc1\n"
    "v_mfma_f32_16x16x32_bf16 v[236:239], v[84:87], v[188:191], v[236:239]\n"
    "s_waitcnt lgkmcnt(4)\n"
    "v_mfma_f32_16x16x32_bf16 v[240:243], v[40:43], v[160:163], v[240:243]\n"
    "ds_read_b128 v[72:75], v121 offset:32768\n"
    "v_mfma_f32_16x16x32_bf16 v[240:243], v[44:47], v[164:167], v[240:243]\n"
    "ds_read_b128 v[76:79], v121 offset:37888\n"
    "v_mfma_f32_16x16x32_bf16 v[240:243], v[48:51], v[168:171], v[240:243]\n"
    "buffer_load_dwordx4 v7, s[20:23], s77 offen offset:1024 sc1 lds\n"
    "v_mfma_f32_16x16x32_bf16 v[240:243], v[52:55], v[172:175], v[240:243]\n"
    "v_mfma_f32_16x16x32_bf16 v[244:247], v[40:43], v[176:179], v[244:247]\n"
    "ds_read_b128 v[80:83], v121 offset:43008\n"
    "v_mfma_f32_16x16x32_bf16 v[244:247], v[44:47], v[180:183], v[244:247]\n"
    "ds_read_b128 v[84:87], v121 offset:48128\n"
    "v_mfma_f32_16x16x32_bf16 v[244:247], v[48:51], v[184:187], v[244:247]\n"
    "buffer_load_dwordx4 v7, s[20:23], s77 offen offset:2048 sc1 lds\n"
    "v_readlane_b32 s77, v5, 6\n"
    "v_mfma_f32_16x16x32_bf16 v[244:247], v[52:55], v[188:191], v[244:247]\n"
    "s_add_u32 m0, s72, 27648\n"
    "s_waitcnt lgkmcnt(4)\n"
    "v_mfma_f32_16x16x32_bf16 v[248:251], v[56:59], v[160:163], v[248:251]\n"
    "ds_read_b128 v[40:43], v4 offset:57344\n"
    "v_mfma_f32_16x16x32_bf16 v[248:251], v[60:63], v[164:167], v[248:251]\n"
    "ds_read_b128 v[44:47], v4 offset:59392\n"
    "v_mfma_f32_16x16x32_bf16 v[248:251], v[64:67], v[168:171], v[248:251]\n"
    "v_mfma_f32_16x16x32_bf16 v[248:251], v[68:71], v[172:175], v[248:251]\n"
    "buffer_load_dwordx4 v7, s[20:23], s77 offen offset:1024 sc1 lds\n"
    "v_mfma_f32_16x16x32_bf16 v[252:255], v[56:59], v[176:179], v[252:255]\n"
    "ds_read_b128 v[48:51], v4 offset:61440\n"
    "v_mfma_f32_16x16x32_bf16 v[252:255], v[60:63], v[180:183], v[252:255]\n"
    "ds_read_b128 v[52:55], v4 offset:63488\n"
    "v_mfma_f32_16x16x32_bf16 v[252:255], v[64:67], v[184:187], v[252:255]\n"
    "buffer_load_dwordx4 v7, s[20:23], s77 offen offset:2048 sc1 lds\n"
    "v_readlane_b32 s77, v5, 6\n"
    "v_mfma_f32_16x16x32_bf16 v[252:255], v[68:71], v[188:191], v[252:255]\n"
    "s_add_u32 m0, s83, 94208\n"
    "s_waitcnt lgkmcnt(4)\n"
    "v_mfma_f32_16x16x32_bf16 v[192:195], v[72:75], v[160:163], v[192:195]\n"
    "ds_read_b128 v[56:59], v4 offset:58368\n"
    "v_mfma_f32_16x16x32_bf16 v[192:195], v[76:79], v[164:167], v[192:195]\n"
    "ds_read_b128 v[60:63], v4 offset:60416\n"
    "v_mfma_f32_16x16x32_bf16 v[192:195], v[80:83], v[168:171], v[192:195]\n"
    "v_mfma_f32_16x16x32_bf16 v[192:195], v[84:87], v[172:175], v[192:195]\n"
    "v_mfma_f32_16x16x32_bf16 v[196:199], v[72:75], v[176:179], v[196:199]\n"
    "ds_read_b128 v[64:67], v4 offset:62464\n"
    "v_mfma_f32_16x16x32_bf16 v[196:199], v[76:79], v[180:183], v[196:199]\n"
    "ds_read_b128 v[68:71], v4 offset:64512\n"
    "v_mfma_f32_16x16x32_bf16 v[196:199], v[80:83], v[184:187], v[196:199]\n"
    "v_mfma_f32_16x16x32_bf16 v[196:199], v[84:87], v[188:191], v[196:199]\n"
    "s_waitcnt vmcnt(7) lgkmcnt(0)\n"
    "s_barrier\n"
    "v_mfma_f32_16x16x32_bf16 v[200:203], v[40:43], v[160:163], v[200:203]\n"
    "ds_read_b128 v[72:75], v121 offset:49152\n"
    "s_lshl_b32 s85, s33, 7\n"
    "v_mfma_f32_16x16x32_bf16 v[200:203], v[44:47], v[164:167], v[200:203]\n"
    "ds_read_b128 v[76:79], v121 offset:54272\n"
    "s_add_u32 s85, s85, 122880\n"
    "v_mfma_f32_16x16x32_bf16 v[200:203], v[48:51], v[168:171], v[200:203]\n"
    "buffer_store_dwordx4 v[218:221], v215, s[20:23], s85 offen sc1\n"
    "v_mfma_f32_16x16x32_bf16 v[200:203], v[52:55], v[172:175], v[200:203]\n"
    "buffer_load_dwordx4 v6, s[20:23], s77 offen offset:0 sc1 lds\n"
    "v_mfma_f32_16x16x32_bf16 v[204:207], v[40:43], v[176:179], v[204:207]\n"
    "ds_read_b128 v[80:83], v121 offset:59392\n"
    "v_mfma_f32_16x16x32_bf16 v[204:207], v[44:47], v[180:183], v[204:207]\n"
    "ds_read_b128 v[84:87], v121 offset:64512\n"
    "v_mfma_f32_16x16x32_bf16 v[204:207], v[48:51], v[184:187], v[204:207]\n"
    "buffer_load_dwordx4 v6, s[20:23], s77 offen offset:1024 sc1 lds\n"
    "v_mfma_f32_16x16x32_bf16 v[204:207], v[52:55], v[188:191], v[204:207]\n"
    "v_mfma_f32_16x16x32_bf16 v[208:211], v[56:59], v[160:163], v[208:211]\n"
    "ds_read_b128 v[40:43], v121 offset:50176\n"
    "v_mfma_f32_16x16x32_bf16 v[208:211], v[60:63], v[164:167], v[208:211]\n"
    "ds_read_b128 v[44:47], v121 offset:55296\n"
    "v_mfma_f32_16x16x32_bf16 v[208:211], v[64:67], v[168:171], v[208:211]\n"
    "buffer_load_dwordx4 v6, s[20:23], s77 offen offset:2048 sc1 lds\n"
    "v_mfma_f32_16x16x32_bf16 v[208:211], v[68:71], v[172:175], v[208:211]\n"
    "v_mfma_f32_16x16x32_bf16 v[116:119], v[56:59], v[176:179], v[116:119]\n"
    "ds_read_b128 v[48:51], v121 offset:60416\n"
    "v_mfma_f32_16x16x32_bf16 v[116:119], v[60:63], v[180:183], v[116:119]\n"
    "ds_read_b128 v[52:55], v122 offset:0\n"
    "v_mfma_f32_16x16x32_bf16 v[116:119], v[64:67], v[184:187], v[116:119]\n"
    "buffer_load_dwordx4 v6, s[20:23], s77 offen offset:3072 sc1 lds\n"
    "s_add_u32 m0, s83, 98304\n"
    "v_mfma_f32_16x16x32_bf16 v[116:119], v[68:71], v[188:191], v[116:119]\n"
    "s_waitcnt vmcnt(29) lgkmcnt(4)\n"
    "v_mfma_f32_16x16x32_bf16 v[224:227], v[72:75], a[0:3], v[224:227]\n"
    "ds_read_b128 v[56:59], v121 offset:51200\n"
    "v_mfma_f32_16x16x32_bf16 v[224:227], v[76:79], a[4:7], v[224:227]\n"
    "ds_read_b128 v[60:63], v121 offset:56320\n"
    "v_mfma_f32_16x16x32_bf16 v[224:227], v[80:83], a[8:11], v[224:227]\n"
    "v_mfma_f32_16x16x32_bf16 v[224:227], v[84:87], a[12:15], v[224:227]\n"
    "buffer_load_dwordx4 v7, s[20:23], s77 offen offset:0 sc1 lds\n"
    "s_waitcnt vmcnt(10)\n"
    "v_mfma_f32_16x16x32_bf16 v[228:231], v[72:75], a[16:19], v[228:231]\n"
    "ds_read_b128 v[64:67], v121 offset:61440\n"
    "v_xor_b32 v106, s40, v104\n"
    "v_mfma_f32_16x16x32_bf16 v[228:231], v[76:79], a[20:23], v[228:231]\n"
    "ds_read_b128 v[68:71], v122 offset:1024\n"
    "v_xor_b32 v107, s41, v105\n"
    "v_mfma_f32_16x16x32_bf16 v[228:231], v[80:83], a[24:27], v[228:231]\n"
    "v_mfma_f32_16x16x32_bf16 v[228:231], v[84:87], a[28:31], v[228:231]\n"
    "v_or_b32 v106, v106, v107\n"
    "s_waitcnt lgkmcnt(4)\n"
    "v_mfma_f32_16x16x32_bf16 v[232:235], v[40:43], a[0:3], v[232:235]\n"
    "ds_read_b128 v[72:75], v121 offset:52224\n"
    "v_cmp_ne_u32 vcc, 0, v106\n"
    "v_mfma_f32_16x16x32_bf16 v[232:235], v[44:47], a[4:7], v[232:235]\n"
    "ds_read_b128 v[76:79], v121 offset:57344\n"
    "s_nop 1\n"
    "v_mfma_f32_16x16x32_bf16 v[232:235], v[48:51], a[8:11], v[232:235]\n"
    "v_mfma_f32_16x16x32_bf16 v[232:235], v[52:55], a[12:15], v[232:235]\n"
    "s_and_b64 vcc, vcc, 16383\n"
    "s_cbranch_vccnz LG2_N_s\n"
    "LG2_N_r:\n"
    "v_mfma_f32_16x16x32_bf16 v[236:239], v[40:43], a[16:19], v[236:239]\n"
    "ds_read_b128 v[80:83], v121 offset:62464\n"
    "v_readlane_b32 s77, v5, 20\n"
    "v_mfma_f32_16x16x32_bf16 v[236:239], v[44:47], a[20:23], v[236:239]\n"
    "ds_read_b128 v[84:87], v122 offset:2048\n"
    "v_mfma_f32_16x16x32_bf16 v[236:239], v[48:51], a[24:27], v[236:239]\n"
    "v_mfma_f32_16x16x32_bf16 v[236:239], v[52:55], a[28:31], v[236:239]\n"
    "s_waitcnt lgkmcnt(4)\n"
    "v_mfma_f32_16x16x32_bf16 v[240:243], v[56:59], a[0:3], v[240:243]\n"
    "ds_read_b128 v[40:43], v121 offset:53248\n"
    "v_mfma_f32_16x16x32_bf16 v[240:243], v[60:63], a[4:7], v[240:243]\n"
    "ds_read_b128 v[44:47], v121 offset:58368\n"
    "v_mfma_f32_16x16x32_bf16 v[240:243], v[64:67], a[8:11], v[240:243]\n"
    "buffer_load_dwordx4 a[64:67], v222, s[20:23], s77 offen offset:0 sc1\n"
    "s_mul_i32 s50, s10, 1835008\n"
    "v_mfma_f32_16x16x32_bf16 v[240:243], v[68:71], a[12:15], v[240:243]\n"
    "buffer_load_dwordx4 a[80:83], v223, s[20:23], s77 offen offset:0 sc1\n"
    "s_sub_u32 s50, s13, s50\n"
    "v_mfma_f32_16x16x32_bf16 v[244:247], v[56:59], a[16:19], v[244:247]\n"
    "ds_read_b128 v[48:51], v121 offset:63488\n"
    "s_sub_u32 s52, s10, 1\n"
    "v_mfma_f32_16x16x32_bf16 v[244:247], v[60:63], a[20:23], v[244:247]\n"
    "ds_read_b128 v[52:55], v122 offset:3072\n"
    "s_and_b32 s52, s52, 3\n"
    "v_mfma_f32_16x16x32_bf16 v[244:247], v[64:67], a[24:27], v[244:247]\n"
    "buffer_load_dwordx4 a[68:71], v222, s[20:23], s77 offen offset:1024 sc1\n"
    "v_mfma_f32_16x16x32_bf16 v[244:247], v[68:71], a[28:31], v[244:247]\n"
    "buffer_load_dwordx4 a[84:87], v223, s[20:23], s77 offen offset:1024 sc1\n"
    "s_waitcnt lgkmcnt(4)\n"
    "v_mfma_f32_16x16x32_bf16 v[248:251], v[72:75], a[0:3], v[248:251]\n"
    "ds_read_b128 v[56:59], v121 offset:0\n"
    "v_mfma_f32_16x16x32_bf16 v[248:251], v[76:79], a[4:7], v[248:251]\n"
    "ds_read_b128 v[60:63], v121 offset:2048\n"
    "v_mfma_f32_16x16x32_bf16 v[248:251], v[80:83], a[8:11], v[248:251]\n"
    "s_mul_i32 s52, s52, 1835008\n"
    "v_mfma_f32_16x16x32_bf16 v[248:251], v[84:87], a[12:15], v[248:251]\n"
    "s_add_u32 s52, s52, s50\n"
    "v_mfma_f32_16x16x32_bf16 v[252:255], v[72:75], a[16:19], v[252:255]\n"
    "ds_read_b128 v[64:67], v121 offset:4096\n"
    "v_mfma_f32_16x16x32_bf16 v[252:255], v[76:79], a[20:23], v[252:255]\n"
    "ds_read_b128 v[68:71], v121 offset:6144\n"
    "v_mfma_f32_16x16x32_bf16 v[252:255], v[80:83], a[24:27], v[252:255]\n"
    "buffer_load_dwordx4 a[72:75], v222, s[20:23], s77 offen offset:2048 sc1\n"
    "v_mfma_f32_16x16x32_bf16 v[252:255], v[84:87], a[28:31], v[252:255]\n"
    "s_add_u32 s55, s52, 0\n"
    "s_waitcnt lgkmcnt(4)\n"
    "v_mfma_f32_16x16x32_bf16 v[192:195], v[40:43], a[0:3], v[192:195]\n"
    "ds_read_b128 v[72:75], v121 offset:1024\n"
    "v_mfma_f32_16x16x32_bf16 v[192:195], v[44:47], a[4:7], v[192:195]\n"
    "ds_read_b128 v[76:79], v121 offset:3072\n"
    "v_mfma_f32_16x16x32_bf16 v[192:195], v[48:51], a[8:11], v[192:195]\n"
    "buffer_load_dwordx4 a[88:91], v223, s[20:23], s77 offen offset:2048 sc1\n"
    "s_sub_u32 s53, s10, 2\n"
    "v_mfma_f32_16x16x32_bf16 v[192:195], v[52:55], a[12:15], v[192:195]\n"
    "v_mfma_f32_16x16x32_bf16 v[196:199], v[40:43], a[16:19], v[196:199]\n"
    "ds_read_b128 v[80:83], v121 offset:5120\n"
    "v_mfma_f32_16x16x32_bf16 v[196:199], v[44:47], a[20:23], v[196:199]\n"
    "ds_read_b128 v[84:87], v121 offset:7168\n"
    "s_and_b32 s53, s53, 3\n"
    "v_mfma_f32_16x16x32_bf16 v[196:199], v[48:51], a[24:27], v[196:199]\n"
    "buffer_load_dwordx4 a[76:79], v222, s[20:23], s77 offen offset:3072 sc1\n"
    "v_mfma_f32_16x16x32_bf16 v[196:199], v[52:55], a[28:31], v[196:199]\n"
    "s_waitcnt vmcnt(26) lgkmcnt(0)\n"
    "s_barrier\n"
    "v_mfma_f32_16x16x32_bf16 v[200:203], v[56:59], a[0:3], v[200:203]\n"
    "ds_read_b128 v[40:43], v122 offset:4096\n"
    "v_mfma_f32_16x16x32_bf16 v[200:203], v[60:63], a[4:7], v[200:203]\n"
    "ds_read_b128 v[44:47], v122 offset:9216\n"
    "v_mfma_f32_16x16x32_bf16 v[200:203], v[64:67], a[8:11], v[200:203]\n"
    "buffer_load_dwordx4 a[92:95], v223, s[20:23], s77 offen offset:3072 sc1\n"
    "v_readlane_b32 s77, v5, 21\n"
    "v_mfma_f32_16x16x32_bf16 v[200:203], v[68:71], a[12:15], v[200:203]\n"
    "s_mul_i32 s53, s53, 1835008\n"
    "v_mfma_f32_16x16x32_bf16 v[204:207], v[56:59], a[16:19], v[204:207]\n"
    "ds_read_b128 v[48:51], v122 offset:14336\n"
    "v_mfma_f32_16x16x32_bf16 v[204:207], v[60:63], a[20:23], v[204:207]\n"
    "ds_read_b128 v[52:55], v122 offset:19456\n"
    "s_add_u32 s53, s53, s50\n"
    "v_mfma_f32_16x16x32_bf16 v[204:207], v[64:67], a[24:27], v[204:207]\n"
    "v_mfma_f32_16x16x32_bf16 v[204:207], v[68:71], a[28:31], v[204:207]\n"
    "buffer_load_dwordx4 a[96:99], v222, s[20:23], s77 offen offset:0 sc1\n"
    "v_mfma_f32_16x16x32_bf16 v[208:211], v[72:75], a[0:3], v[208:211]\n"
    "ds_read_b128 v[56:59], v122 offset:5120\n"
    "s_add_u32 s56, s53, 2048\n"
    "v_mfma_f32_16x16x32_bf16 v[208:211], v[76:79], a[4:7], v[208:211]\n"
    "ds_read_b128 v[60:63], v122 offset:10240\n"
    "v_mfma_f32_16x16x32_bf16 v[208:211], v[80:83], a[8:11], v[208:211]\n"
    "buffer_load_dwordx4 a[112:115], v223, s[20:23], s77 offen offset:0 sc1\n"
    "v_mfma_f32_16x16x32_bf16 v[208:211], v[84:87], a[12:15], v[208:211]\n"
    "s_sub_u32 s54, s10, 3\n"
    "v_mfma_f32_16x16x32_bf16 v[116:119], v[72:75], a[16:19], v[116:119]\n"
    "ds_read_b128 v[64:67], v122 offset:15360\n"
    "v_mfma_f32_16x16x32_bf16 v[116:119], v[76:79], a[20:23], v[116:119]\n"
    "ds_read_b128 v[68:71], v122 offset:20480\n"
    "v_mfma_f32_16x16x32_bf16 v[116:119], v[80:83], a[24:27], v[116:119]\n"
    "s_and_b32 s54, s54, 3\n"
    "v_mfma_f32_16x16x32_bf16 v[116:119], v[84:87], a[28:31], v[116:119]\n"
    "buffer_load_dwordx4 a[100:103], v222, s[20:23], s77 offen offset:1024 sc1\n"
    "s_waitcnt vmcnt(32) lgkmcnt(4)\n"
    "v_mfma_f32_16x16x32_bf16 v[224:227], v[40:43], a[32:35], v[224:227]\n"
    "ds_read_b128 v[72:75], v122 offset:6144\n"
    "v_mfma_f32_16x16x32_bf16 v[224:227], v[44:47], a[36:39], v[224:227]\n"
    "ds_read_b128 v[76:79], v122 offset:11264\n"
    "s_mul_i32 s54, s54, 1835008\n"
    "v_mfma_f32_16x16x32_bf16 v[224:227], v[48:51], a[40:43], v[224:227]\n"
    "v_mfma_f32_16x16x32_bf16 v[224:227], v[52:55], a[44:47], v[224:227]\n"
    "buffer_load_dwordx4 a[116:119], v223, s[20:23], s77 offen offset:1024 sc1\n"
    "v_mfma_f32_16x16x32_bf16 v[228:231], v[40:43], a[48:51], v[228:231]\n"
    "ds_read_b128 v[80:83], v122 offset:16384\n"
    "s_add_u32 s54, s54, s50\n"
    "v_mfma_f32_16x16x32_bf16 v[228:231], v[44:47], a[52:55], v[228:231]\n"
    "ds_read_b128 v[84:87], v122 offset:21504\n"
    "v_mfma_f32_16x16x32_bf16 v[228:231], v[48:51], a[56:59], v[228:231]\n"
    "v_mfma_f32_16x16x32_bf16 v[228:231], v[52:55], a[60:63], v[228:231]\n"
    "s_add_u32 s57, s54, 4096\n"
    "s_waitcnt lgkmcnt(4)\n"
    "v_mfma_f32_16x16x32_bf16 v[232:235], v[56:59], a[32:35], v[232:235]\n"
    "ds_read_b128 v[40:43], v122 offset:7168\n"
    "v_mfma_f32_16x16x32_bf16 v[232:235], v[60:63], a[36:39], v[232:235]\n"
    "ds_read_b128 v[44:47], v122 offset:12288\n"
    "s_lshl_b32 s85, s2, 7\n"
    "v_mfma_f32_16x16x32_bf16 v[232:235], v[64:67], a[40:43], v[232:235]\n"
    "buffer_load_dwordx4 a[104:107], v222, s[20:23], s77 offen offset:2048 sc1\n"
    "s_add_u32 s85, s85, 188416\n"
    "v_mfma_f32_16x16x32_bf16 v[232:235], v[68:71], a[44:47], v[232:235]\n"
    "buffer_store_dwordx4 v[218:221], v215, s[20:23], s85 offen sc1\n"
    "v_mfma_f32_16x16x32_bf16 v[236:239], v[56:59], a[48:51], v[236:239]\n"
    "ds_read_b128 v[48:51], v122 offset:17408\n"
    "s_lshl_b32 s69, s11, 2\n"
    "v_mfma_f32_16x16x32_bf16 v[236:239], v[60:63], a[52:55], v[236:239]\n"
    "ds_read_b128 v[52:55], v122 offset:22528\n"
    "v_mfma_f32_16x16x32_bf16 v[236:239], v[64:67], a[56:59], v[236:239]\n"
    "buffer_load_dwordx4 a[120:123], v223, s[20:23], s77 offen offset:2048 sc1\n"
    "v_mfma_f32_16x16x32_bf16 v[236:239], v[68:71], a[60:63], v[236:239]\n"
    "s_waitcnt lgkmcnt(4)\n"
    "v_mfma_f32_16x16x32_bf16 v[240:243], v[72:75], a[32:35], v[240:243]\n"
    "ds_read_b128 v[56:59], v122 offset:8192\n"
    "s_add_u32 s69, s69, s32\n"
    "v_mfma_f32_16x16x32_bf16 v[240:243], v[76:79], a[36:39], v[240:243]\n"
    "ds_read_b128 v[60:63], v122 offset:13312\n"
    "v_mfma_f32_16x16x32_bf16 v[240:243], v[80:83], a[40:43], v[240:243]\n"
    "buffer_load_dwordx4 a[108:111], v222, s[20:23], s77 offen offset:3072 sc1\n"
    "v_mfma_f32_16x16x32_bf16 v[240:243], v[84:87], a[44:47], v[240:243]\n"
    "s_lshl_b32 s69, s69, 1\n"
    "v_mfma_f32_16x16x32_bf16 v[244:247], v[72:75], a[48:51], v[244:247]\n"
    "ds_read_b128 v[64:67], v122 offset:18432\n"
    "v_mfma_f32_16x16x32_bf16 v[244:247], v[76:79], a[52:55], v[244:247]\n"
    "ds_read_b128 v[68:71], v122 offset:23552\n"
    "v_mfma_f32_16x16x32_bf16 v[244:247], v[80:83], a[56:59], v[244:247]\n"
    "buffer_load_dwordx4 a[124:127], v223, s[20:23], s77 offen offset:3072 sc1\n"
    "v_readlane_b32 s77, v5, 22\n"
    "v_mfma_f32_16x16x32_bf16 v[244:247], v[84:87], a[60:63], v[244:247]\n"
    "s_mul_i32 s8, s69, 18432\n"
    "s_waitcnt lgkmcnt(4)\n"
    "v_mfma_f32_16x16x32_bf16 v[248:251], v[40:43], a[32:35], v[248:251]\n"
    "ds_read_b128 v[72:75], v4 offset:4096\n"
    "v_mfma_f32_16x16x32_bf16 v[248:251], v[44:47], a[36:39], v[248:251]\n"
    "ds_read_b128 v[76:79], v4 offset:6144\n"
    "v_mfma_f32_16x16x32_bf16 v[248:251], v[48:51], a[40:43], v[248:251]\n"
    "s_add_u32 s8, s8, 12984320\n"
    "v_mfma_f32_16x16x32_bf16 v[248:251], v[52:55], a[44:47], v[248:251]\n"
    "buffer_load_dwordx4 a[128:131], v222, s[20:23], s77 offen offset:0 sc1\n"
    "v_mfma_f32_16x16x32_bf16 v[252:255], v[40:43], a[48:51], v[252:255]\n"
    "ds_read_b128 v[80:83], v4 offset:8192\n"
    "v_mfma_f32_16x16x32_bf16 v[252:255], v[44:47], a[52:55], v[252:255]\n"
    "ds_read_b128 v[84:87], v4 offset:10240\n"
    "s_lshl_b32 s59, s12, 12\n"
    "v_mfma_f32_16x16x32_bf16 v[252:255], v[48:51], a[56:59], v[252:255]\n"
    "buffer_load_dwordx4 a[144:147], v223, s[20:23], s77 offen offset:0 sc1\n"
    "v_mfma_f32_16x16x32_bf16 v[252:255], v[52:55], a[60:63], v[252:255]\n"
    "s_waitcnt lgkmcnt(4)\n"
    "v_mfma_f32_16x16x32_bf16 v[192:195], v[56:59], a[32:35], v[192:195]\n"
    "ds_read_b128 v[40:43], v4 offset:5120\n"
    "v_mfma_f32_16x16x32_bf16 v[192:195], v[60:63], a[36:39], v[192:195]\n"
    "ds_read_b128 v[44:47], v4 offset:7168\n"
    "s_and_b32 s60, s10, 1\n"
    "v_mfma_f32_16x16x32_bf16 v[192:195], v[64:67], a[40:43], v[192:195]\n"
    "v_mfma_f32_16x16x32_bf16 v[192:195], v[68:71], a[44:47], v[192:195]\n"
    "buffer_load_dwordx4 a[132:135], v222, s[20:23], s77 offen offset:1024 sc1\n"
    "s_lshl_b32 s60, s60, 9\n"
    "v_mfma_f32_16x16x32_bf16 v[196:199], v[56:59], a[48:51], v[196:199]\n"
    "ds_read_b128 v[48:51], v4 offset:9216\n"
    "v_mfma_f32_16x16x32_bf16 v[196:199], v[60:63], a[52:55], v[196:199]\n"
    "ds_read_b128 v[52:55], v4 offset:11264\n"
    "v_mfma_f32_16x16x32_bf16 v[196:199], v[64:67], a[56:59], v[196:199]\n"
    "s_add_u32 s59, s59, s60\n"
    "v_mfma_f32_16x16x32_bf16 v[196:199], v[68:71], a[60:63], v[196:199]\n"
    "s_waitcnt vmcnt(63) lgkmcnt(0)\n"
    "s_barrier\n"
    "v_mfma_f32_16x16x32_bf16 v[200:203], v[72:75], a[32:35], v[200:203]\n"
    "ds_read_b128 v[56:59], v4 offset:36864\n"
    "v_mfma_f32_16x16x32_bf16 v[200:203], v[76:79], a[36:39], v[200:203]\n"
    "ds_read_b128 v[60:63], v4 offset:41984\n"
    "v_mfma_f32_16x16x32_bf16 v[200:203], v[80:83], a[40:43], v[200:203]\n"
    "buffer_load_dwordx4 a[148:151], v223, s[20:23], s77 offen offset:1024 sc1\n"
    "s_lshr_b32 s60, s10, 1\n"
    "v_mfma_f32_16x16x32_bf16 v[200:203], v[84:87], a[44:47], v[200:203]\n"
    "v_mfma_f32_16x16x32_bf16 v[204:207], v[72:75], a[48:51], v[204:207]\n"
    "ds_read_b128 v[64:67], v4 offset:47104\n"
    "v_mfma_f32_16x16x32_bf16 v[204:207], v[76:79], a[52:55], v[204:207]\n"
    "ds_read_b128 v[68:71], v4 offset:52224\n"
    "s_lshl_b32 s60, s60, 3\n"
    "v_mfma_f32_16x16x32_bf16 v[204:207], v[80:83], a[56:59], v[204:207]\n"
    "buffer_load_dwordx4 a[136:139], v222, s[20:23], s77 offen offset:2048 sc1\n"
    "v_mfma_f32_16x16x32_bf16 v[204:207], v[84:87], a[60:63], v[204:207]\n"
    "v_mfma_f32_16x16x32_bf16 v[208:211], v[40:43], a[32:35], v[208:211]\n"
    "ds_read_b128 v[72:75], v4 offset:37888\n"
    "s_add_u32 s59, s59, s60\n"
    "v_mfma_f32_16x16x32_bf16 v[208:211], v[44:47], a[36:39], v[208:211]\n"
    "ds_read_b128 v[76:79], v4 offset:43008\n"
    "v_mfma_f32_16x16x32_bf16 v[208:211], v[48:51], a[40:43], v[208:211]\n"
    "buffer_load_dwordx4 a[152:155], v223, s[20:23], s77 offen offset:2048 sc1\n"
    "v_mfma_f32_16x16x32_bf16 v[208:211], v[52:55], a[44:47], v[208:211]\n"
    "s_add_u32 s59, s59, s8\n"
    "v_mfma_f32_16x16x32_bf16 v[116:119], v[40:43], a[48:51], v[116:119]\n"
    "ds_read_b128 v[80:83], v4 offset:48128\n"
    "v_mfma_f32_16x16x32_bf16 v[116:119], v[44:47], a[52:55], v[116:119]\n"
    "ds_read_b128 v[84:87], v4 offset:53248\n"
    "v_mfma_f32_16x16x32_bf16 v[116:119], v[48:51], a[56:59], v[116:119]\n"
    "buffer_load_dwordx4 a[140:143], v222, s[20:23], s77 offen offset:3072 sc1\n"
    "s_add_u32 s60, s59, 18432\n"
    "v_mfma_f32_16x16x32_bf16 v[116:119], v[52:55], a[60:63], v[116:119]\n"
    "s_waitcnt vmcnt(16) lgkmcnt(4)\n"
    "v_mfma_f32_16x16x32_bf16 v[224:227], v[56:59], a[64:67], v[224:227]\n"
    "ds_read_b128 v[40:43], v4 offset:38912\n"
    "v_mfma_f32_16x16x32_bf16 v[224:227], v[60:63], a[68:71], v[224:227]\n"
    "ds_read_b128 v[44:47], v4 offset:44032\n"
    "s_mov_b32 s85, 122880\n"
    "v_mfma_f32_16x16x32_bf16 v[224:227], v[64:67], a[72:75], v[224:227]\n"
    "buffer_load_dwordx4 a[156:159], v223, s[20:23], s77 offen offset:3072 sc1\n"
    "v_readlane_b32 s77, v5, 7\n"
    "v_mfma_f32_16x16x32_bf16 v[224:227], v[68:71], a[76:79], v[224:227]\n"
    "s_add_u32 m0, s83, 114688\n"
    "LS_10:\n"
    "s_cbranch_execz LS_ret\n"
    "s_and_b32 s61, s12, 1\n"
    "v_mfma_f32_16x16x32_bf16 v[228:231], v[56:59], a[80:83], v[228:231]\n"
    "ds_read_b128 v[48:51], v4 offset:49152\n"
    "v_mfma_f32_16x16x32_bf16 v[228:231], v[60:63], a[84:87], v[228:231]\n"
    "ds_read_b128 v[52:55], v4 offset:54272\n"
    "v_mfma_f32_16x16x32_bf16 v[228:231], v[64:67], a[88:91], v[228:231]\n"
    "buffer_load_dwordx2 v[104:105], v110, s[20:23], s85 offen sc1\n"
    "v_mfma_f32_16x16x32_bf16 v[228:231], v[68:71], a[92:95], v[228:231]\n"
    "buffer_load_dwordx4 v6, s[20:23], s77 offen offset:0 sc1 lds\n"
    "s_waitcnt lgkmcnt(4)\n"
    "v_mfma_f32_16x16x32_bf16 v[232:235], v[72:75], a[64:67], v[232:235]\n"
    "ds_read_b128 v[56:59], v4 offset:39936\n"
    "s_lshl_b32 s61, s61, 8\n"
    "v_mfma_f32_16x16x32_bf16 v[232:235], v[76:79], a[68:71], v[232:235]\n"
    "ds_read_b128 v[60:63], v4 offset:45056\n"
    "v_mfma_f32_16x16x32_bf16 v[232:235], v[80:83], a[72:75], v[232:235]\n"
    "buffer_load_dwordx4 v6, s[20:23], s77 offen offset:1024 sc1 lds\n"
    "v_mfma_f32_16x16x32_bf16 v[232:235], v[84:87], a[76:79], v[232:235]\n"
    "s_lshr_b32 s62, s12, 1\n"
    "v_mfma_f32_16x16x32_bf16 v[236:239], v[72:75], a[80:83], v[236:239]\n"
    "ds_read_b128 v[64:67], v4 offset:50176\n"
    "v_mfma_f32_16x16x32_bf16 v[236:239], v[76:79], a[84:87], v[236:239]\n"
    "ds_read_b128 v[68:71], v4 offset:55296\n"
    "v_mfma_f32_16x16x32_bf16 v[236:239], v[80:83], a[88:91], v[236:239]\n"
    "s_lshl_b32 s62, s62, 3\n"
    "v_mfma_f32_16x16x32_bf16 v[236:239], v[84:87], a[92:95], v[236:239]\n"
    "buffer_load_dwordx4 v6, s[20:23], s77 offen offset:2048 sc1 lds\n"
    "s_waitcnt lgkmcnt(4)\n"
    "v_mfma_f32_16x16x32_bf16 v[240:243], v[40:43], a[64:67], v[240:243]\n"
    "ds_read_b128 v[72:75], v4 offset:40960\n"
    "v_mfma_f32_16x16x32_bf16 v[240:243], v[44:47], a[68:71], v[240:243]\n"
    "ds_read_b128 v[76:79], v4 offset:46080\n"
    "v_mfma_f32_16x16x32_bf16 v[240:243], v[48:51], a[72:75], v[240:243]\n"
    "s_add_u32 s61, s61, s62\n"
    "v_mfma_f32_16x16x32_bf16 v[240:243], v[52:55], a[76:79], v[240:243]\n"
    "buffer_load_dwordx4 v6, s[20:23], s77 offen offset:3072 sc1 lds\n"
    "s_add_u32 m0, s83, 118784\n"
    "v_mfma_f32_16x16x32_bf16 v[244:247], v[40:43], a[80:83], v[244:247]\n"
    "ds_read_b128 v[80:83], v4 offset:51200\n"
    "v_mfma_f32_16x16x32_bf16 v[244:247], v[44:47], a[84:87], v[244:247]\n"
    "ds_read_b128 v[84:87], v4 offset:56320\n"
    "s_add_u32 s61, s61, s8\n"
    "v_mfma_f32_16x16x32_bf16 v[244:247], v[48:51], a[88:91], v[244:247]\n"
    "v_mfma_f32_16x16x32_bf16 v[244:247], v[52:55], a[92:95], v[244:247]\n"
    "s_waitcnt vmcnt(41) lgkmcnt(0)\n"
    "s_barrier\n"
    "v_mfma_f32_16x16x32_bf16 v[248:251], v[56:59], a[64:67], v[248:251]\n"
    "ds_read_b128 v[40:43], v121 offset:8192\n"
    "v_mfma_f32_16x16x32_bf16 v[248:251], v[60:63], a[68:71], v[248:251]\n"
    "ds_read_b128 v[44:47], v121 offset:13312\n"
    "s_add_u32 s61, s61, 16384\n"
    "v_mfma_f32_16x16x32_bf16 v[248:251], v[64:67], a[72:75], v[248:251]\n"
    "buffer_load_dwordx4 v7, s[20:23], s77 offen offset:0 sc1 lds\n"
    "v_readlane_b32 s77, v5, 8\n"
    "v_mfma_f32_16x16x32_bf16 v[248:251], v[68:71], a[76:79], v[248:251]\n"
    "s_add_u32 m0, s83, 135168\n"
    "v_mfma_f32_16x16x32_bf16 v[252:255], v[56:59], a[80:83], v[252:255]\n"
    "ds_read_b128 v[48:51], v121 offset:18432\n"
    "v_mfma_f32_16x16x32_bf16 v[252:255], v[60:63], a[84:87], v[252:255]\n"
    "ds_read_b128 v[52:55], v121 offset:23552\n"
    "s_add_u32 s62, s61, 18432\n"
    "v_mfma_f32_16x16x32_bf16 v[252:255], v[64:67], a[88:91], v[252:255]\n"
    "v_mfma_f32_16x16x32_bf16 v[252:255], v[68:71], a[92:95], v[252:255]\n"
    "buffer_load_dwordx4 v6, s[20:23], s77 offen offset:0 sc1 lds\n"
    "v_mfma_f32_16x16x32_bf16 v[192:195], v[72:75], a[64:67], v[192:195]\n"
    "ds_read_b128 v[56:59], v121 offset:9216\n"
    "s_lshl_b32 s63, s32, 12\n"
    "v_mfma_f32_16x16x32_bf16 v[192:195], v[76:79], a[68:71], v[192:195]\n"
    "ds_read_b128 v[60:63], v121 offset:14336\n"
    "v_mfma_f32_16x16x32_bf16 v[192:195], v[80:83], a[72:75], v[192:195]\n"
    "buffer_load_dwordx4 v6, s[20:23], s77 offen offset:1024 sc1 lds\n"
    "v_mfma_f32_16x16x32_bf16 v[192:195], v[84:87], a[76:79], v[192:195]\n"
    "s_add_u32 s63, s63, 135168\n"
    "v_mfma_f32_16x16x32_bf16 v[196:199], v[72:75], a[80:83], v[196:199]\n"
    "ds_read_b128 v[64:67], v121 offset:19456\n"
    "v_mfma_f32_16x16x32_bf16 v[196:199], v[76:79], a[84:87], v[196:199]\n"
    "ds_read_b128 v[68:71], v121 offset:24576\n"
    "v_mfma_f32_16x16x32_bf16 v[196:199], v[80:83], a[88:91], v[196:199]\n"
    "buffer_load_dwordx4 v6, s[20:23], s77 offen offset:2048 sc1 lds\n"
    "s_lshr_b32 s64, s69, 3\n"
    "v_mfma_f32_16x16x32_bf16 v[196:199], v[84:87], a[92:95], v[196:199]\n"
    "s_waitcnt vmcnt(17) lgkmcnt(4)\n"
    "v_mfma_f32_16x16x32_bf16 v[224:227], v[40:43], a[96:99], v[224:227]\n"
    "ds_read_b128 v[72:75], v121 offset:10240\n"
    "v_mfma_f32_16x16x32_bf16 v[224:227], v[44:47], a[100:103], v[224:227]\n"
    "ds_read_b128 v[76:79], v121 offset:15360\n"
    "v_mfma_f32_16x16x32_bf16 v[224:227], v[48:51], a[104:107], v[224:227]\n"
    "buffer_load_dwordx4 v6, s[20:23], s77 offen offset:3072 sc1 lds\n"
    "s_add_u32 m0, s83, 139264\n"
    "v_mfma_f32_16x16x32_bf16 v[224:227], v[52:55], a[108:111], v[224:227]\n"
    "s_lshl_b32 s64, s64, 5\n"
    "v_mfma_f32_16x16x32_bf16 v[228:231], v[40:43], a[112:115], v[228:231]\n"
    "ds_read_b128 v[80:83], v121 offset:20480\n"
    "v_mfma_f32_16x16x32_bf16 v[228:231], v[44:47], a[116:119], v[228:231]\n"
    "ds_read_b128 v[84:87], v121 offset:25600\n"
    "s_and_b32 s65, s69, 7\n"
    "v_mfma_f32_16x16x32_bf16 v[228:231], v[48:51], a[120:123], v[228:231]\n"
    "buffer_load_dwordx4 v7, s[20:23], s77 offen offset:0 sc1 lds\n"
    "s_waitcnt vmcnt(10)\n"
    "v_mfma_f32_16x16x32_bf16 v[228:231], v[52:55], a[124:127], v[228:231]\n"
    "v_xor_b32 v106, s40, v104\n"
    "s_waitcnt lgkmcnt(4)\n"
    "v_mfma_f32_16x16x32_bf16 v[232:235], v[56:59], a[96:99], v[232:235]\n"
    "ds_read_b128 v[40:43], v121 offset:11264\n"
    "v_xor_b32 v107, s41, v105\n"
    "v_mfma_f32_16x16x32_bf16 v[232:235], v[60:63], a[100:103], v[232:235]\n"
    "ds_read_b128 v[44:47], v121 offset:16384\n"
    "v_or_b32 v106, v106, v107\n"
    "v_mfma_f32_16x16x32_bf16 v[232:235], v[64:67], a[104:107], v[232:235]\n"
    "v_mfma_f32_16x16x32_bf16 v[232:235], v[68:71], a[108:111], v[232:235]\n"
    "v_cmp_ne_u32 vcc, 0, v106\n"
    "s_nop 1\n"
    "v_mfma_f32_16x16x32_bf16 v[236:239], v[56:59], a[112:115], v[236:239]\n"
    "ds_read_b128 v[48:51], v121 offset:21504\n"
    "s_cbranch_vccnz LG2_H_s\n"
    "LG2_H_r:\n"
    "v_mfma_f32_16x16x32_bf16 v[236:239], v[60:63], a[116:119], v[236:239]\n"
    "ds_read_b128 v[52:55], v121 offset:26624\n"
    "v_readlane_b32 s77, v5, 23\n"
    "v_mfma_f32_16x16x32_bf16 v[236:239], v[64:67], a[120:123], v[236:239]\n"
    "s_add_u32 s64, s64, s65\n"
    "v_mfma_f32_16x16x32_bf16 v[236:239], v[68:71], a[124:127], v[236:239]\n"
    "s_waitcnt lgkmcnt(4)\n"
    "v_mfma_f32_16x16x32_bf16 v[240:243], v[72:75], a[96:99], v[240:243]\n"
    "ds_read_b128 v[56:59], v121 offset:12288\n"
    "v_mfma_f32_16x16x32_bf16 v[240:243], v[76:79], a[100:103], v[240:243]\n"
    "ds_read_b128 v[60:63], v121 offset:17408\n"
    "s_mul_i32 s64, s64, 4608\n"
    "v_mfma_f32_16x16x32_bf16 v[240:243], v[80:83], a[104:107], v[240:243]\n"
    "buffer_load_dwordx4 a[160:163], v222, s[20:23], s77 offen offset:0 sc1\n"
    "v_mfma_f32_16x16x32_bf16 v[240:243], v[84:87], a[108:111], v[240:243]\n"
    "buffer_load_dwordx4 a[176:179], v223, s[20:23], s77 offen offset:0 sc1\n"
    "v_mfma_f32_16x16x32_bf16 v[244:247], v[72:75], a[112:115], v[244:247]\n"
    "ds_read_b128 v[64:67], v121 offset:22528\n"
    "v_mfma_f32_16x16x32_bf16 v[244:247], v[76:79], a[116:119], v[244:247]\n"
    "ds_read_b128 v[68:71], v121 offset:27648\n"
    "s_lshl_b32 s66, s12, 5\n"
    "v_mfma_f32_16x16x32_bf16 v[244:247], v[80:83], a[120:123], v[244:247]\n"
    "buffer_load_dwordx4 a[164:167], v222, s[20:23], s77 offen offset:1024 sc1\n"
    "v_mfma_f32_16x16x32_bf16 v[244:247], v[84:87], a[124:127], v[244:247]\n"
    "s_waitcnt vmcnt(39) lgkmcnt(0)\n"
    "s_barrier\n"
    "v_mfma_f32_16x16x32_bf16 v[248:251], v[40:43], a[96:99], v[248:251]\n"
    "ds_read_b128 v[72:75], v121 offset:28672\n"
    "v_mfma_f32_16x16x32_bf16 v[248:251], v[44:47], a[100:103], v[248:251]\n"
    "ds_read_b128 v[76:79], v121 offset:33792\n"
    "v_mfma_f32_16x16x32_bf16 v[248:251], v[48:51], a[104:107], v[248:251]\n"
    "buffer_load_dwordx4 a[180:183], v223, s[20:23], s77 offen offset:1024 sc1\n"
    "s_add_u32 s66, s66, s64\n"
    "v_mfma_f32_16x16x32_bf16 v[248:251], v[52:55], a[108:111], v[248:251]\n"
    "v_mfma_f32_16x16x32_bf16 v[252:255], v[40:43], a[112:115], v[252:255]\n"
    "ds_read_b128 v[80:83], v121 offset:38912\n"
    "s_add_u32 s66, s66, 1024\n"
    "v_mfma_f32_16x16x32_bf16 v[252:255], v[44:47], a[116:119], v[252:255]\n"
    "ds_read_b128 v[84:87], v121 offset:44032\n"
    "v_mfma_f32_16x16x32_bf16 v[252:255], v[48:51], a[120:123], v[252:255]\n"
    "buffer_load_dwordx4 a[168:171], v222, s[20:23], s77 offen offset:2048 sc1\n"
    "v_mfma_f32_16x16x32_bf16 v[252:255], v[52:55], a[124:127], v[252:255]\n"
    "s_add_u32 s67, s66, 4608\n"
    "v_mfma_f32_16x16x32_bf16 v[192:195], v[56:59], a[96:99], v[192:195]\n"
    "ds_read_b128 v[40:43], v121 offset:29696\n"
    "v_mfma_f32_16x16x32_bf16 v[192:195], v[60:63], a[100:103], v[192:195]\n"
    "ds_read_b128 v[44:47], v121 offset:34816\n"
    "s_mov_b32 s85, 155648\n"
    "v_mfma_f32_16x16x32_bf16 v[192:195], v[64:67], a[104:107], v[192:195]\n"
    "buffer_load_dwordx4 a[184:187], v223, s[20:23], s77 offen offset:2048 sc1\n"
    "v_mfma_f32_16x16x32_bf16 v[192:195], v[68:71], a[108:111], v[192:195]\n"
    "buffer_load_dwordx2 v[104:105], v110, s[20:23], s85 offen sc1\n"
    "s_lshl_b32 s65, s12, 2\n"
    "v_mfma_f32_16x16x32_bf16 v[196:199], v[56:59], a[112:115], v[196:199]\n"
    "ds_read_b128 v[48:51], v121 offset:39936\n"
    "v_mfma_f32_16x16x32_bf16 v[196:199], v[60:63], a[116:119], v[196:199]\n"
    "ds_read_b128 v[52:55], v121 offset:45056\n"
    "v_mfma_f32_16x16x32_bf16 v[196:199], v[64:67], a[120:123], v[196:199]\n"
    "buffer_load_dwordx4 a[172:175], v222, s[20:23], s77 offen offset:3072 sc1\n"
    "v_mfma_f32_16x16x32_bf16 v[196:199], v[68:71], a[124:127], v[196:199]\n"
    "s_add_u32 s65, s65, s10\n"
    "s_waitcnt vmcnt(19) lgkmcnt(4)\n"
    "v_mfma_f32_16x16x32_bf16 v[224:227], v[72:75], a[128:131], v[224:227]\n"
    "ds_read_b128 v[56:59], v121 offset:30720\n"
    "v_mfma_f32_16x16x32_bf16 v[224:227], v[76:79], a[132:135], v[224:227]\n"
    "ds_read_b128 v[60:63], v121 offset:35840\n"
    "v_mfma_f32_16x16x32_bf16 v[224:227], v[80:83], a[136:139], v[224:227]\n"
    "buffer_load_dwordx4 a[188:191], v223, s[20:23], s77 offen offset:3072 sc1\n"
    "v_readlane_b32 s77, v5, 24\n"
    "v_mfma_f32_16x16x32_bf16 v[224:227], v[84:87], a[140:143], v[224:227]\n"
    "s_lshl_b32 s65, s65, 6\n"
    "v_mfma_f32_16x16x32_bf16 v[228:231], v[72:75], a[144:147], v[228:231]\n"
    "ds_read_b128 v[64:67], v121 offset:40960\n"
    "v_mfma_f32_16x16x32_bf16 v[228:231], v[76:79], a[148:151], v[228:231]\n"
    "ds_read_b128 v[68:71], v121 offset:46080\n"
    "v_mfma_f32_16x16x32_bf16 v[228:231], v[80:83], a[152:155], v[228:231]\n"
    "s_add_u32 s64, s64, s65\n"
    "v_mfma_f32_16x16x32_bf16 v[228:231], v[84:87], a[156:159], v[228:231]\n"
    "buffer_load_dwordx4 a[192:195], v222, s[20:23], s77 offen offset:0 sc1\n"
    "s_waitcnt lgkmcnt(4)\n"
    "v_mfma_f32_16x16x32_bf16 v[232:235], v[40:43], a[128:131], v[232:235]\n"
    "ds_read_b128 v[72:75], v121 offset:31744\n"
    "v_mfma_f32_16x16x32_bf16 v[232:235], v[44:47], a[132:135], v[232:235]\n"
    "ds_read_b128 v[76:79], v121 offset:36864\n"
    "v_mfma_f32_16x16x32_bf16 v[232:235], v[48:51], a[136:139], v[232:235]\n"
    "s_add_u32 s65, s64, 4608\n"
    "v_mfma_f32_16x16x32_bf16 v[232:235], v[52:55], a[140:143], v[232:235]\n"
    "buffer_load_dwordx4 a[208:211], v223, s[20:23], s77 offen offset:0 sc1\n"
    "v_mfma_f32_16x16x32_bf16 v[236:239], v[40:43], a[144:147], v[236:239]\n"
    "ds_read_b128 v[80:83], v121 offset:41984\n"
    "s_mov_b64 s[28:29], s[16:17]\n"
    "v_mfma_f32_16x16x32_bf16 v[236:239], v[44:47], a[148:151], v[236:239]\n"
    "ds_read_b128 v[84:87], v121 offset:47104\n"
    "v_mfma_f32_16x16x32_bf16 v[236:239], v[48:51], a[152:155], v[236:239]\n"
    "v_mfma_f32_16x16x32_bf16 v[236:239], v[52:55], a[156:159], v[236:239]\n"
    "s_mov_b32 s30, 2359296\n"
    "s_waitcnt lgkmcnt(4)\n"
    "v_mfma_f32_16x16x32_bf16 v[240:243], v[56:59], a[128:131], v[240:243]\n"
    "ds_read_b128 v[40:43], v121 offset:32768\n"
    "v_mfma_f32_16x16x32_bf16 v[240:243], v[60:63], a[132:135], v[240:243]\n"
    "ds_read_b128 v[44:47], v121 offset:37888\n"
    "v_mfma_f32_16x16x32_bf16 v[240:243], v[64:67], a[136:139], v[240:243]\n"
    "buffer_load_dwordx4 a[196:199], v222, s[20:23], s77 offen offset:1024 sc1\n"
    "v_mfma_f32_16x16x32_bf16 v[240:243], v[68:71], a[140:143], v[240:243]\n"
    "v_mfma_f32_16x16x32_bf16 v[244:247], v[56:59], a[144:147], v[244:247]\n"
    "ds_read_b128 v[48:51], v121 offset:43008\n"
    "v_mfma_f32_16x16x32_bf16 v[244:247], v[60:63], a[148:151], v[244:247]\n"
    "ds_read_b128 v[52:55], v121 offset:48128\n"
    "v_mfma_f32_16x16x32_bf16 v[244:247], v[64:67], a[152:155], v[244:247]\n"
    "buffer_load_dwordx4 a[212:215], v223, s[20:23], s77 offen offset:1024 sc1\n"
    "v_mfma_f32_16x16x32_bf16 v[244:247], v[68:71], a[156:159], v[244:247]\n"
    "s_waitcnt vmcnt(18) lgkmcnt(0)\n"
    "s_barrier\n"
    "v_mfma_f32_16x16x32_bf16 v[248:251], v[72:75], a[128:131], v[248:251]\n"
    "ds_read_b128 v[56:59], v121 offset:49152\n"
    "v_mfma_f32_16x16x32_bf16 v[248:251], v[76:79], a[132:135], v[248:251]\n"
    "ds_read_b128 v[60:63], v121 offset:54272\n"
    "v_mfma_f32_16x16x32_bf16 v[248:251], v[80:83], a[136:139], v[248:251]\n"
    "buffer_load_dwordx4 a[200:203], v222, s[20:23], s77 offen offset:2048 sc1\n"
    "v_mfma_f32_16x16x32_bf16 v[248:251], v[84:87], a[140:143], v[248:251]\n"
    "v_mfma_f32_16x16x32_bf16 v[252:255], v[72:75], a[144:147], v[252:255]\n"
    "ds_read_b128 v[64:67], v121 offset:59392\n"
    "v_mfma_f32_16x16x32_bf16 v[252:255], v[76:79], a[148:151], v[252:255]\n"
    "ds_read_b128 v[68:71], v121 offset:64512\n"
    "v_mfma_f32_16x16x32_bf16 v[252:255], v[80:83], a[152:155], v[252:255]\n"
    "buffer_load_dwordx4 a[216:219], v223, s[20:23], s77 offen offset:2048 sc1\n"
    "v_mfma_f32_16x16x32_bf16 v[252:255], v[84:87], a[156:159], v[252:255]\n"
    "v_mfma_f32_16x16x32_bf16 v[192:195], v[40:43], a[128:131], v[192:195]\n"
    "ds_read_b128 v[72:75], v121 offset:50176\n"
    "v_mfma_f32_16x16x32_bf16 v[192:195], v[44:47], a[132:135], v[192:195]\n"
    "ds_read_b128 v[76:79], v121 offset:55296\n"
    "v_mfma_f32_16x16x32_bf16 v[192:195], v[48:51], a[136:139], v[192:195]\n"
    "buffer_load_dwordx4 a[204:207], v222, s[20:23], s77 offen offset:3072 sc1\n"
    "v_mfma_f32_16x16x32_bf16 v[192:195], v[52:55], a[140:143], v[192:195]\n"
    "v_mfma_f32_16x16x32_bf16 v[196:199], v[40:43], a[144:147], v[196:199]\n"
    "ds_read_b128 v[80:83], v121 offset:60416\n"
    "v_mfma_f32_16x16x32_bf16 v[196:199], v[44:47], a[148:151], v[196:199]\n"
    "ds_read_b128 v[84:87], v122 offset:0\n"
    "v_mfma_f32_16x16x32_bf16 v[196:199], v[48:51], a[152:155], v[196:199]\n"
    "buffer_load_dwordx4 a[220:223], v223, s[20:23], s77 offen offset:3072 sc1\n"
    "s_waitcnt vmcnt(10)\n"
    "v_mfma_f32_16x16x32_bf16 v[196:199], v[52:55], a[156:159], v[196:199]\n"
    "v_xor_b32 v106, s40, v104\n"
    "s_waitcnt vmcnt(8) lgkmcnt(4)\n"
    "v_mfma_f32_16x16x32_bf16 v[224:227], v[56:59], a[160:163], v[224:227]\n"
    "ds_read_b128 v[40:43], v121 offset:51200\n"
    "v_xor_b32 v107, s41, v105\n"
    "v_mfma_f32_16x16x32_bf16 v[224:227], v[60:63], a[164:167], v[224:227]\n"
    "ds_read_b128 v[44:47], v121 offset:56320\n"
    "v_or_b32 v106, v106, v107\n"
    "v_mfma_f32_16x16x32_bf16 v[224:227], v[64:67], a[168:171], v[224:227]\n"
    "v_mfma_f32_16x16x32_bf16 v[224:227], v[68:71], a[172:175], v[224:227]\n"
    "v_cmp_ne_u32 vcc, 0, v106\n"
    "s_nop 1\n"
    "v_mfma_f32_16x16x32_bf16 v[228:231], v[56:59], a[176:179], v[228:231]\n"
    "ds_read_b128 v[48:51], v121 offset:61440\n"
    "s_and_b64 vcc, vcc, 3932160\n"
    "v_mfma_f32_16x16x32_bf16 v[228:231], v[60:63], a[180:183], v[228:231]\n"
    "ds_read_b128 v[52:55], v122 offset:1024\n"
    "s_cbranch_vccnz LG2_S_s\n"
    "LG2_S_r:\n"
    "v_mfma_f32_16x16x32_bf16 v[228:231], v[64:67], a[184:187], v[228:231]\n"
    "v_mfma_f32_16x16x32_bf16 v[228:231], v[68:71], a[188:191], v[228:231]\n"
    "v_readlane_b32 s77, v5, 7\n"
    "s_waitcnt lgkmcnt(4)\n"
    "v_mfma_f32_16x16x32_bf16 v[232:235], v[72:75], a[160:163], v[232:235]\n"
    "ds_read_b128 v[56:59], v121 offset:52224\n"
    "s_add_u32 m0, s72, 35840\n"
    "v_mfma_f32_16x16x32_bf16 v[232:235], v[76:79], a[164:167], v[232:235]\n"
    "ds_read_b128 v[60:63], v121 offset:57344\n"
    "v_mfma_f32_16x16x32_bf16 v[232:235], v[80:83], a[168:171], v[232:235]\n"
    "v_mfma_f32_16x16x32_bf16 v[232:235], v[84:87], a[172:175], v[232:235]\n"
    "v_mfma_f32_16x16x32_bf16 v[236:239], v[72:75], a[176:179], v[236:239]\n"
    "ds_read_b128 v[64:67], v121 offset:62464\n"
    "v_mfma_f32_16x16x32_bf16 v[236:239], v[76:79], a[180:183], v[236:239]\n"
    "ds_read_b128 v[68:71], v122 offset:2048\n"
    "v_mfma_f32_16x16x32_bf16 v[236:239], v[80:83], a[184:187], v[236:239]\n"
    "buffer_load_dwordx4 v7, s[20:23], s77 offen offset:1024 sc1 lds\n"
    "v_mfma_f32_16x16x32_bf16 v[236:239], v[84:87], a[188:191], v[236:239]\n"
    "buffer_load_dwordx4 v7, s[20:23], s77 offen offset:2048 sc1 lds\n"
    "s_waitcnt lgkmcnt(4)\n"
    "v_mfma_f32_16x16x32_bf16 v[240:243], v[40:43], a[160:163], v[240:243]\n"
    "ds_read_b128 v[72:75], v121 offset:53248\n"
    "v_readlane_b32 s77, v5, 8\n"
    "v_mfma_f32_16x16x32_bf16 v[240:243], v[44:47], a[164:167], v[240:243]\n"
    "ds_read_b128 v[76:79], v121 offset:58368\n"
    "s_add_u32 m0, s72, 44032\n"
    "v_mfma_f32_16x16x32_bf16 v[240:243], v[48:51], a[168:171], v[240:243]\n"
    "v_mfma_f32_16x16x32_bf16 v[240:243], v[52:55], a[172:175], v[240:243]\n"
    "v_mfma_f32_16x16x32_bf16 v[244:247], v[40:43], a[176:179], v[244:247]\n"
    "ds_read_b128 v[80:83], v121 offset:63488\n"
    "v_mfma_f32_16x16x32_bf16 v[244:247], v[44:47], a[180:183], v[244:247]\n"
    "ds_read_b128 v[84:87], v122 offset:3072\n"
    "v_mfma_f32_16x16x32_bf16 v[244:247], v[48:51], a[184:187], v[244:247]\n"
    "buffer_load_dwordx4 v7, s[20:23], s77 offen offset:1024 sc1 lds\n"
    "v_mfma_f32_16x16x32_bf16 v[244:247], v[52:55], a[188:191], v[244:247]\n"
    "s_waitcnt vmcnt(20) lgkmcnt(0)\n"
    "s_barrier\n"
    "v_mfma_f32_16x16x32_bf16 v[248:251], v[56:59], a[160:163], v[248:251]\n"
    "ds_read_b128 v[40:43], v122 offset:4096\n"
    "v_mfma_f32_16x16x32_bf16 v[248:251], v[60:63], a[164:167], v[248:251]\n"
    "ds_read_b128 v[44:47], v122 offset:9216\n"
    "v_mfma_f32_16x16x32_bf16 v[248:251], v[64:67], a[168:171], v[248:251]\n"
    "buffer_load_dwordx4 v7, s[20:23], s77 offen offset:2048 sc1 lds\n"
    "v_readlane_b32 s77, v5, 0\n"
    "v_mfma_f32_16x16x32_bf16 v[248:251], v[68:71], a[172:175], v[248:251]\n"
    "s_cmp_eq_u32 s32, s10\n"
    "s_cselect_b32 s26, 0, s22\n"
    "v_mfma_f32_16x16x32_bf16 v[252:255], v[56:59], a[176:179], v[252:255]\n"
    "ds_read_b128 v[48:51], v122 offset:14336\n"
    "s_add_u32 m0, s72, 73728\n"
    "v_mfma_f32_16x16x32_bf16 v[252:255], v[60:63], a[180:183], v[252:255]\n"
    "ds_read_b128 v[52:55], v122 offset:19456\n"
    "v_mfma_f32_16x16x32_bf16 v[252:255], v[64:67], a[184:187], v[252:255]\n"
    "v_mfma_f32_16x16x32_bf16 v[252:255], v[68:71], a[188:191], v[252:255]\n"
    "s_cmp_eq_u32 s32, s10\n"
    "s_cselect_b32 s6, s22, 0\n"
    "v_mfma_f32_16x16x32_bf16 v[192:195], v[72:75], a[160:163], v[192:195]\n"
    "ds_read_b128 v[56:59], v122 offset:5120\n"
    "v_mfma_f32_16x16x32_bf16 v[192:195], v[76:79], a[164:167], v[192:195]\n"
    "ds_read_b128 v[60:63], v122 offset:10240\n"
    "v_mfma_f32_16x16x32_bf16 v[192:195], v[80:83], a[168:171], v[192:195]\n"
    "buffer_load_dwordx4 v217, s[20:23], s77 offen offset:0 sc1 lds\n"
    "v_mfma_f32_16x16x32_bf16 v[192:195], v[84:87], a[172:175], v[192:195]\n"
    "buffer_load_dwordx4 v217, s[20:23], s77 offen offset:1024 sc1 lds\n"
    "v_readlane_b32 s77, v5, 1\n"
    "v_mfma_f32_16x16x32_bf16 v[196:199], v[72:75], a[176:179], v[196:199]\n"
    "ds_read_b128 v[64:67], v122 offset:15360\n"
    "s_add_u32 m0, s72, 81920\n"
    "v_mfma_f32_16x16x32_bf16 v[196:199], v[76:79], a[180:183], v[196:199]\n"
    "ds_read_b128 v[68:71], v122 offset:20480\n"
    "v_mfma_f32_16x16x32_bf16 v[196:199], v[80:83], a[184:187], v[196:199]\n"
    "v_mfma_f32_16x16x32_bf16 v[196:199], v[84:87], a[188:191], v[196:199]\n"
    "s_waitcnt vmcnt(6) lgkmcnt(4)\n"
    "v_mfma_f32_16x16x32_bf16 v[224:227], v[40:43], a[192:195], v[224:227]\n"
    "ds_read_b128 v[72:75], v122 offset:6144\n"
    "v_mfma_f32_16x16x32_bf16 v[224:227], v[44:47], a[196:199], v[224:227]\n"
    "ds_read_b128 v[76:79], v122 offset:11264\n"
    "v_mfma_f32_16x16x32_bf16 v[224:227], v[48:51], a[200:203], v[224:227]\n"
    "buffer_load_dwordx4 v217, s[20:23], s77 offen offset:0 sc1 lds\n"
    "v_mfma_f32_16x16x32_bf16 v[224:227], v[52:55], a[204:207], v[224:227]\n"
    "buffer_load_dwordx4 v217, s[20:23], s77 offen offset:1024 sc1 lds\n"
    "v_readlane_b32 s77, v5, 2\n"
    "v_mfma_f32_16x16x32_bf16 v[228:231], v[40:43], a[208:211], v[228:231]\n"
    "ds_read_b128 v[80:83], v122 offset:16384\n"
    "s_add_u32 m0, s72, 94208\n"
    "v_mfma_f32_16x16x32_bf16 v[228:231], v[44:47], a[212:215], v[228:231]\n"
    "ds_read_b128 v[84:87], v122 offset:21504\n"
    "v_mfma_f32_16x16x32_bf16 v[228:231], v[48:51], a[216:219], v[228:231]\n"
    "v_mfma_f32_16x16x32_bf16 v[228:231], v[52:55], a[220:223], v[228:231]\n"
    "s_waitcnt lgkmcnt(4)\n"
    "v_mfma_f32_16x16x32_bf16 v[232:235], v[56:59], a[192:195], v[232:235]\n"
    "ds_read_b128 v[40:43], v122 offset:7168\n"
    "v_mfma_f32_16x16x32_bf16 v[232:235], v[60:63], a[196:199], v[232:235]\n"
    "ds_read_b128 v[44:47], v122 offset:12288\n"
    "v_mfma_f32_16x16x32_bf16 v[232:235], v[64:67], a[200:203], v[232:235]\n"
    "buffer_load_dwordx4 v217, s[20:23], s77 offen offset:0 sc1 lds\n"
    "v_mfma_f32_16x16x32_bf16 v[232:235], v[68:71], a[204:207], v[232:235]\n"
    "buffer_load_dwordx4 v217, s[20:23], s77 offen offset:1024 sc1 lds\n"
    "v_readlane_b32 s77, v5, 3\n"
    "v_mfma_f32_16x16x32_bf16 v[236:239], v[56:59], a[208:211], v[236:239]\n"
    "ds_read_b128 v[48:51], v122 offset:17408\n"
    "s_add_u32 m0, s72, 102400\n"
    "v_mfma_f32_16x16x32_bf16 v[236:239], v[60:63], a[212:215], v[236:239]\n"
    "ds_read_b128 v[52:55], v122 offset:22528\n"
    "v_cvt_pk_bf16_f32 v24, v224, v225\n"
    "v_mfma_f32_16x16x32_bf16 v[236:239], v[64:67], a[216:219], v[236:239]\n"
    "v_mfma_f32_16x16x32_bf16 v[236:239], v[68:71], a[220:223], v[236:239]\n"
    "v_cvt_pk_bf16_f32 v25, v226, v227\n"
    "s_waitcnt lgkmcnt(4)\n"
    "v_mfma_f32_16x16x32_bf16 v[240:243], v[72:75], a[192:195], v[240:243]\n"
    "ds_read_b128 v[56:59], v122 offset:8192\n"
    "v_cvt_pk_bf16_f32 v26, v228, v229\n"
    "v_mfma_f32_16x16x32_bf16 v[240:243], v[76:79], a[196:199], v[240:243]\n"
    "ds_read_b128 v[60:63], v122 offset:13312\n"
    "v_cvt_pk_bf16_f32 v27, v230, v231\n"
    "v_mfma_f32_16x16x32_bf16 v[240:243], v[80:83], a[200:203], v[240:243]\n"
    "buffer_store_dwordx4 v[24:27], v4, s[24:27], s13 offen offset:0 sc1\n"
    "v_mfma_f32_16x16x32_bf16 v[240:243], v[84:87], a[204:207], v[240:243]\n"
    "buffer_load_dwordx4 v217, s[20:23], s77 offen offset:0 sc1 lds\n"
    "v_mfma_f32_16x16x32_bf16 v[244:247], v[72:75], a[208:211], v[244:247]\n"
    "ds_read_b128 v[64:67], v122 offset:18432\n"
    "v_mfma_f32_16x16x32_bf16 v[244:247], v[76:79], a[212:215], v[244:247]\n"
    "ds_read_b128 v[68:71], v122 offset:23552\n"
    "v_mfma_f32_16x16x32_bf16 v[244:247], v[80:83], a[216:219], v[244:247]\n"
    "buffer_load_dwordx4 v217, s[20:23], s77 offen offset:1024 sc1 lds\n"
    "v_readlane_b32 s77, v5, 4\n"
    "v_mfma_f32_16x16x32_bf16 v[244:247], v[84:87], a[220:223], v[244:247]\n"
    "s_waitcnt vmcnt(63) lgkmcnt(0)\n"
    "s_barrier\n"
    "v_mfma_f32_16x16x32_bf16 v[248:251], v[40:43], a[192:195], v[248:251]\n"
    "ds_read_b128 v[72:75], v4 offset:12288\n"
    "s_add_u32 m0, s72, 114688\n"
    "v_mfma_f32_16x16x32_bf16 v[248:251], v[44:47], a[196:199], v[248:251]\n"
    "ds_read_b128 v[76:79], v4 offset:14336\n"
    "v_mfma_f32_16x16x32_bf16 v[248:251], v[48:51], a[200:203], v[248:251]\n"
    "v_mfma_f32_16x16x32_bf16 v[248:251], v[52:55], a[204:207], v[248:251]\n"
    "buffer_load_dwordx4 v217, s[20:23], s77 offen offset:0 sc1 lds\n"
    "v_mfma_f32_16x16x32_bf16 v[252:255], v[40:43], a[208:211], v[252:255]\n"
    "ds_read_b128 v[80:83], v4 offset:16384\n"
    "v_mfma_f32_16x16x32_bf16 v[252:255], v[44:47], a[212:215], v[252:255]\n"
    "ds_read_b128 v[84:87], v4 offset:18432\n"
    "v_mfma_f32_16x16x32_bf16 v[252:255], v[48:51], a[216:219], v[252:255]\n"
    "buffer_load_dwordx4 v217, s[20:23], s77 offen offset:1024 sc1 lds\n"
    "v_readlane_b32 s77, v5, 5\n"
    "v_mfma_f32_16x16x32_bf16 v[252:255], v[52:55], a[220:223], v[252:255]\n"
    "s_add_u32 m0, s72, 122880\n"
    "v_mfma_f32_16x16x32_bf16 v[192:195], v[56:59], a[192:195], v[192:195]\n"
    "ds_read_b128 v[40:43], v4 offset:13312\n"
    "v_mfma_f32_16x16x32_bf16 v[192:195], v[60:63], a[196:199], v[192:195]\n"
    "ds_read_b128 v[44:47], v4 offset:15360\n"
    "v_mfma_f32_16x16x32_bf16 v[192:195], v[64:67], a[200:203], v[192:195]\n"
    "v_mfma_f32_16x16x32_bf16 v[192:195], v[68:71], a[204:207], v[192:195]\n"
    "v_cvt_pk_bf16_f32 v28, v232, v233\n"
    "v_cvt_pk_bf16_f32 v29, v234, v235\n"
    "v_mfma_f32_16x16x32_bf16 v[196:199], v[56:59], a[208:211], v[196:199]\n"
    "ds_read_b128 v[48:51], v4 offset:17408\n"
    "v_cvt_pk_bf16_f32 v30, v236, v237\n"
    "v_mfma_f32_16x16x32_bf16 v[196:199], v[60:63], a[212:215], v[196:199]\n"
    "ds_read_b128 v[52:55], v4 offset:19456\n"
    "v_cvt_pk_bf16_f32 v31, v238, v239\n"
    "v_mfma_f32_16x16x32_bf16 v[196:199], v[64:67], a[216:219], v[196:199]\n"
    "buffer_store_dwordx4 v[28:31], v4, s[20:23], s13 offen offset:1024 sc1\n"
    "v_mfma_f32_16x16x32_bf16 v[196:199], v[68:71], a[220:223], v[196:199]\n"
    "buffer_load_dwordx4 v217, s[20:23], s77 offen offset:0 sc1 lds\n"
    "s_waitcnt lgkmcnt(4)\n"
    "v_mfma_f32_16x16x32_bf16 v[200:203], v[72:75], a[64:67], v[200:203]\n"
    "ds_read_b128 v[56:59], v4 offset:20480\n"
    "v_mfma_f32_16x16x32_bf16 v[200:203], v[76:79], a[68:71], v[200:203]\n"
    "ds_read_b128 v[60:63], v4 offset:22528\n"
    "v_mfma_f32_16x16x32_bf16 v[200:203], v[80:83], a[72:75], v[200:203]\n"
    "buffer_load_dwordx4 v217, s[20:23], s77 offen offset:1024 sc1 lds\n"
    "v_readlane_b32 s77, v5, 6\n"
    "v_mfma_f32_16x16x32_bf16 v[200:203], v[84:87], a[76:79], v[200:203]\n"
    "s_add_u32 m0, s72, 155648\n"
    "v_mfma_f32_16x16x32_bf16 v[204:207], v[72:75], a[80:83], v[204:207]\n"
    "ds_read_b128 v[64:67], v4 offset:24576\n"
    "v_mfma_f32_16x16x32_bf16 v[204:207], v[76:79], a[84:87], v[204:207]\n"
    "ds_read_b128 v[68:71], v4 offset:26624\n"
    "v_mfma_f32_16x16x32_bf16 v[204:207], v[80:83], a[88:91], v[204:207]\n"
    "v_mfma_f32_16x16x32_bf16 v[204:207], v[84:87], a[92:95], v[204:207]\n"
    "buffer_load_dwordx4 v217, s[20:23], s77 offen offset:0 sc1 lds\n"
    "s_waitcnt lgkmcnt(4)\n"
    "v_mfma_f32_16x16x32_bf16 v[208:211], v[40:43], a[64:67], v[208:211]\n"
    "ds_read_b128 v[72:75], v4 offset:21504\n"
    "v_mfma_f32_16x16x32_bf16 v[208:211], v[44:47], a[68:71], v[208:211]\n"
    "ds_read_b128 v[76:79], v4 offset:23552\n"
    "v_mfma_f32_16x16x32_bf16 v[208:211], v[48:51], a[72:75], v[208:211]\n"
    "buffer_load_dwordx4 v217, s[20:23], s77 offen offset:1024 sc1 lds\n"
    "v_readlane_b32 s77, v5, 7\n"
    "v_mfma_f32_16x16x32_bf16 v[208:211], v[52:55], a[76:79], v[208:211]\n"
    "s_add_u32 m0, s72, 57344\n"
    "v_mfma_f32_16x16x32_bf16 v[116:119], v[40:43], a[80:83], v[116:119]\n"
    "ds_read_b128 v[80:83], v4 offset:25600\n"
    "v_mfma_f32_16x16x32_bf16 v[116:119], v[44:47], a[84:87], v[116:119]\n"
    "ds_read_b128 v[84:87], v4 offset:27648\n"
    "v_mfma_f32_16x16x32_bf16 v[116:119], v[48:51], a[88:91], v[116:119]\n"
    "v_mfma_f32_16x16x32_bf16 v[116:119], v[52:55], a[92:95], v[116:119]\n"
    "v_cvt_pk_bf16_f32 v32, v240, v241\n"
    "s_waitcnt lgkmcnt(4)\n"
    "v_mfma_f32_16x16x32_bf16 v[200:203], v[56:59], a[96:99], v[200:203]\n"
    "ds_read_b128 v[40:43], v4 offset:28672\n"
    "v_cvt_pk_bf16_f32 v33, v242, v243\n"
    "v_mfma_f32_16x16x32_bf16 v[200:203], v[60:63], a[100:103], v[200:203]\n"
    "ds_read_b128 v[44:47], v4 offset:30720\n"
    "v_cvt_pk_bf16_f32 v34, v244, v245\n"
    "v_mfma_f32_16x16x32_bf16 v[200:203], v[64:67], a[104:107], v[200:203]\n"
    "buffer_load_dwordx4 v217, s[20:23], s77 offen offset:0 sc1 lds\n"
    "v_mfma_f32_16x16x32_bf16 v[200:203], v[68:71], a[108:111], v[200:203]\n"
    "v_cvt_pk_bf16_f32 v35, v246, v247\n"
    "v_mfma_f32_16x16x32_bf16 v[204:207], v[56:59], a[112:115], v[204:207]\n"
    "ds_read_b128 v[48:51], v4 offset:32768\n"
    "v_mfma_f32_16x16x32_bf16 v[204:207], v[60:63], a[116:119], v[204:207]\n"
    "ds_read_b128 v[52:55], v4 offset:34816\n"
    "v_mfma_f32_16x16x32_bf16 v[204:207], v[64:67], a[120:123], v[204:207]\n"
    "buffer_store_dwordx4 v[32:35], v4, s[20:23], s13 offen offset:2048 sc1\n"
    "v_mfma_f32_16x16x32_bf16 v[204:207], v[68:71], a[124:127], v[204:207]\n"
    "buffer_load_dwordx4 v217, s[20:23], s77 offen offset:1024 sc1 lds\n"
    "s_waitcnt lgkmcnt(4)\n"
    "v_mfma_f32_16x16x32_bf16 v[208:211], v[72:75], a[96:99], v[208:211]\n"
    "ds_read_b128 v[56:59], v4 offset:29696\n"
    "v_readlane_b32 s77, v5, 8\n"
    "v_mfma_f32_16x16x32_bf16 v[208:211], v[76:79], a[100:103], v[208:211]\n"
    "ds_read_b128 v[60:63], v4 offset:31744\n"
    "s_add_u32 m0, s72, 65536\n"
    "v_mfma_f32_16x16x32_bf16 v[208:211], v[80:83], a[104:107], v[208:211]\n"
    "v_mfma_f32_16x16x32_bf16 v[208:211], v[84:87], a[108:111], v[208:211]\n"
    "v_mfma_f32_16x16x32_bf16 v[116:119], v[72:75], a[112:115], v[116:119]\n"
    "ds_read_b128 v[64:67], v4 offset:33792\n"
    "v_mfma_f32_16x16x32_bf16 v[116:119], v[76:79], a[116:119], v[116:119]\n"
    "ds_read_b128 v[68:71], v4 offset:35840\n"
    "v_mfma_f32_16x16x32_bf16 v[116:119], v[80:83], a[120:123], v[116:119]\n"
    "buffer_load_dwordx4 v217, s[20:23], s77 offen offset:0 sc1 lds\n"
    "v_mfma_f32_16x16x32_bf16 v[116:119], v[84:87], a[124:127], v[116:119]\n"
    "s_waitcnt vmcnt(18) lgkmcnt(0)\n"
    "s_barrier\n"
    "v_mfma_f32_16x16x32_bf16 v[200:203], v[40:43], a[128:131], v[200:203]\n"
    "ds_read_b128 v[72:75], v4 offset:36864\n"
    "v_mfma_f32_16x16x32_bf16 v[200:203], v[44:47], a[132:135], v[200:203]\n"
    "ds_read_b128 v[76:79], v4 offset:38912\n"
    "v_mfma_f32_16x16x32_bf16 v[200:203], v[48:51], a[136:139], v[200:203]\n"
    "buffer_load_dwordx4 v217, s[20:23], s77 offen offset:1024 sc1 lds\n"
    "v_mfma_f32_16x16x32_bf16 v[200:203], v[52:55], a[140:143], v[200:203]\n"
    "v_mfma_f32_16x16x32_bf16 v[204:207], v[40:43], a[144:147], v[204:207]\n"
    "ds_read_b128 v[80:83], v4 offset:40960\n"
    "v_mfma_f32_16x16x32_bf16 v[204:207], v[44:47], a[148:151], v[204:207]\n"
    "ds_read_b128 v[84:87], v4 offset:43008\n"
    "v_mfma_f32_16x16x32_bf16 v[204:207], v[48:51], a[152:155], v[204:207]\n"
    "v_mfma_f32_16x16x32_bf16 v[204:207], v[52:55], a[156:159], v[204:207]\n"
    "v_cvt_pk_bf16_f32 v36, v248, v249\n"
    "v_cvt_pk_bf16_f32 v37, v250, v251\n"
    "v_mfma_f32_16x16x32_bf16 v[208:211], v[56:59], a[128:131], v[208:211]\n"
    "ds_read_b128 v[40:43], v4 offset:37888\n"
    "v_cvt_pk_bf16_f32 v38, v252, v253\n"
    "v_mfma_f32_16x16x32_bf16 v[208:211], v[60:63], a[132:135], v[208:211]\n"
    "ds_read_b128 v[44:47], v4 offset:39936\n"
    "v_cvt_pk_bf16_f32 v39, v254, v255\n"
    "v_mfma_f32_16x16x32_bf16 v[208:211], v[64:67], a[136:139], v[208:211]\n"
    "buffer_store_dwordx4 v[36:39], v4, s[20:23], s13 offen offset:3072 sc1\n"
    "v_mfma_f32_16x16x32_bf16 v[208:211], v[68:71], a[140:143], v[208:211]\n"
    "v_mfma_f32_16x16x32_bf16 v[116:119], v[56:59], a[144:147], v[116:119]\n"
    "ds_read_b128 v[48:51], v4 offset:41984\n"
    "v_mfma_f32_16x16x32_bf16 v[116:119], v[60:63], a[148:151], v[116:119]\n"
    "ds_read_b128 v[52:55], v4 offset:44032\n"
    "v_mfma_f32_16x16x32_bf16 v[116:119], v[64:67], a[152:155], v[116:119]\n"
    "v_mfma_f32_16x16x32_bf16 v[116:119], v[68:71], a[156:159], v[116:119]\n"
    "s_waitcnt lgkmcnt(4)\n"
    "v_mfma_f32_16x16x32_bf16 v[200:203], v[72:75], a[160:163], v[200:203]\n"
    "ds_read_b128 v[56:59], v4 offset:45056\n"
    "v_mfma_f32_16x16x32_bf16 v[200:203], v[76:79], a[164:167], v[200:203]\n"
    "ds_read_b128 v[60:63], v4 offset:47104\n"
    "LS_11:\n"
    "s_cbranch_execz LS_ret\n"
    "v_mfma_f32_16x16x32_bf16 v[200:203], v[80:83], a[168:171], v[200:203]\n"
    "v_mfma_f32_16x16x32_bf16 v[200:203], v[84:87], a[172:175], v[200:203]\n"
    "v_mfma_f32_16x16x32_bf16 v[204:207], v[72:75], a[176:179], v[204:207]\n"
    "ds_read_b128 v[64:67], v4 offset:49152\n"
    "v_mfma_f32_16x16x32_bf16 v[204:207], v[76:79], a[180:183], v[204:207]\n"
    "ds_read_b128 v[68:71], v4 offset:51200\n"
    "v_mfma_f32_16x16x32_bf16 v[204:207], v[80:83], a[184:187], v[204:207]\n"
    "v_mfma_f32_16x16x32_bf16 v[204:207], v[84:87], a[188:191], v[204:207]\n"
    "s_waitcnt lgkmcnt(4)\n"
    "v_mfma_f32_16x16x32_bf16 v[208:211], v[40:43], a[160:163], v[208:211]\n"
    "ds_read_b128 v[72:75], v4 offset:46080\n"
    "v_mfma_f32_16x16x32_bf16 v[208:211], v[44:47], a[164:167], v[208:211]\n"
    "ds_read_b128 v[76:79], v4 offset:48128\n"
    "v_mfma_f32_16x16x32_bf16 v[208:211], v[48:51], a[168:171], v[208:211]\n"
    "v_mfma_f32_16x16x32_bf16 v[208:211], v[52:55], a[172:175], v[208:211]\n"
    "v_cvt_pk_bf16_f32 v24, v192, v193\n"
    "v_cvt_pk_bf16_f32 v25, v194, v195\n"
    "v_mfma_f32_16x16x32_bf16 v[116:119], v[40:43], a[176:179], v[116:119]\n"
    "ds_read_b128 v[80:83], v4 offset:50176\n"
    "v_cvt_pk_bf16_f32 v26, v196, v197\n"
    "v_mfma_f32_16x16x32_bf16 v[116:119], v[44:47], a[180:183], v[116:119]\n"
    "ds_read_b128 v[84:87], v4 offset:52224\n"
    "v_cvt_pk_bf16_f32 v27, v198, v199\n"
    "v_mfma_f32_16x16x32_bf16 v[116:119], v[48:51], a[184:187], v[116:119]\n"
    "buffer_store_dwordx4 v[24:27], v4, s[20:23], s15 offen offset:0 sc1\n"
    "v_mfma_f32_16x16x32_bf16 v[116:119], v[52:55], a[188:191], v[116:119]\n"
    "v_mov_b32 v192, 0\n"
    "s_waitcnt lgkmcnt(4)\n"
    "v_mfma_f32_16x16x32_bf16 v[200:203], v[56:59], a[192:195], v[200:203]\n"
    "ds_read_b128 v[40:43], v121 offset:8192\n"
    "v_mov_b32 v193, 0\n"
    "v_mfma_f32_16x16x32_bf16 v[200:203], v[60:63], a[196:199], v[200:203]\n"
    "ds_read_b128 v[44:47], v121 offset:10240\n"
    "v_mov_b32 v194, 0\n"
    "v_mfma_f32_16x16x32_bf16 v[200:203], v[64:67], a[200:203], v[200:203]\n"
    "v_mov_b32 v195, 0\n"
    "v_mov_b32 v196, 0\n"
    "v_mfma_f32_16x16x32_bf16 v[200:203], v[68:71], a[204:207], v[200:203]\n"
    "v_mov_b32 v197, 0\n"
    "v_mov_b32 v198, 0\n"
    "v_mfma_f32_16x16x32_bf16 v[204:207], v[56:59], a[208:211], v[204:207]\n"
    "ds_read_b128 v[48:51], v121 offset:12288\n"
    "v_mov_b32 v199, 0\n"
    "v_mfma_f32_16x16x32_bf16 v[204:207], v[60:63], a[212:215], v[204:207]\n"
    "ds_read_b128 v[52:55], v121 offset:14336\n"
    "v_mov_b32 v108, 0\n"
    "v_mfma_f32_16x16x32_bf16 v[204:207], v[64:67], a[216:219], v[204:207]\n"
    "v_mov_b32 v109, 0\n"
    "v_mfma_f32_16x16x32_bf16 v[204:207], v[68:71], a[220:223], v[204:207]\n"
    "v_mov_b32 v110, 0\n"
    "s_waitcnt lgkmcnt(4)\n"
    "v_mfma_f32_16x16x32_bf16 v[208:211], v[72:75], a[192:195], v[208:211]\n"
    "ds_read_b128 v[56:59], v121 offset:9216\n"
    "v_mfma_f32_16x16x32_bf16 v[208:211], v[76:79], a[196:199], v[208:211]\n"
    "ds_read_b128 v[60:63], v121 offset:11264\n"
    "v_mov_b32 v111, 0\n"
    "v_mfma_f32_16x16x32_bf16 v[208:211], v[80:83], a[200:203], v[208:211]\n"
    "v_mfma_f32_16x16x32_bf16 v[208:211], v[84:87], a[204:207], v[208:211]\n"
    "v_mfma_f32_16x16x32_bf16 v[116:119], v[72:75], a[208:211], v[116:119]\n"
    "ds_read_b128 v[64:67], v121 offset:13312\n"
    "v_mfma_f32_16x16x32_bf16 v[116:119], v[76:79], a[212:215], v[116:119]\n"
    "ds_read_b128 v[68:71], v121 offset:15360\n"
    "v_mfma_f32_16x16x32_bf16 v[116:119], v[80:83], a[216:219], v[116:119]\n"
    "v_mfma_f32_16x16x32_bf16 v[116:119], v[84:87], a[220:223], v[116:119]\n"
    "s_waitcnt vmcnt(14) lgkmcnt(0)\n"
    "s_barrier\n"
    "v_mfma_f32_16x16x32_bf16 v[8:11], v[40:43], v[128:131], 0\n"
    "ds_read_b128 v[72:75], v121 offset:16384\n"
    "v_cvt_pk_bf16_f32 v28, v200, v201\n"
    "v_mfma_f32_16x16x32_bf16 v[8:11], v[44:47], v[132:135], v[8:11]\n"
    "ds_read_b128 v[76:79], v121 offset:18432\n"
    "v_cvt_pk_bf16_f32 v29, v202, v203\n"
    "v_mfma_f32_16x16x32_bf16 v[8:11], v[48:51], v[136:139], v[8:11]\n"
    "v_mfma_f32_16x16x32_bf16 v[8:11], v[52:55], v[140:143], v[8:11]\n"
    "v_cvt_pk_bf16_f32 v30, v204, v205\n"
    "v_cvt_pk_bf16_f32 v31, v206, v207\n"
    "v_mfma_f32_16x16x32_bf16 v[12:15], v[40:43], v[144:147], 0\n"
    "ds_read_b128 v[80:83], v121 offset:20480\n"
    "v_mfma_f32_16x16x32_bf16 v[12:15], v[44:47], v[148:151], v[12:15]\n"
    "ds_read_b128 v[84:87], v121 offset:22528\n"
    "v_mfma_f32_16x16x32_bf16 v[12:15], v[48:51], v[152:155], v[12:15]\n"
    "buffer_store_dwordx4 v[28:31], v4, s[20:23], s15 offen offset:1024 sc1\n"
    "v_mfma_f32_16x16x32_bf16 v[12:15], v[52:55], v[156:159], v[12:15]\n"
    "s_cmp_lg_u32 s32, s10\n"
    "s_cbranch_scc1 LG2_no\n"
    "buffer_load_dwordx4 v[192:195], v4, s[4:7], s52 offen sc1\n"
    "buffer_load_dwordx4 v[196:199], v4, s[4:7], s53 offen sc1\n"
    "buffer_load_dwordx4 v[108:111], v4, s[4:7], s54 offen sc1\n"
    "LG2_no:\n"
    "v_mfma_f32_16x16x32_bf16 v[16:19], v[56:59], v[128:131], 0\n"
    "ds_read_b128 v[40:43], v121 offset:17408\n"
    "v_mfma_f32_16x16x32_bf16 v[16:19], v[60:63], v[132:135], v[16:19]\n"
    "ds_read_b128 v[44:47], v121 offset:19456\n"
    "v_mfma_f32_16x16x32_bf16 v[16:19], v[64:67], v[136:139], v[16:19]\n"
    "v_mfma_f32_16x16x32_bf16 v[16:19], v[68:71], v[140:143], v[16:19]\n"
    "v_mfma_f32_16x16x32_bf16 v[20:23], v[56:59], v[144:147], 0\n"
    "ds_read_b128 v[48:51], v121 offset:21504\n"
    "v_mfma_f32_16x16x32_bf16 v[20:23], v[60:63], v[148:151], v[20:23]\n"
    "ds_read_b128 v[52:55], v121 offset:23552\n"
    "v_mfma_f32_16x16x32_bf16 v[20:23], v[64:67], v[152:155], v[20:23]\n"
    "v_mfma_f32_16x16x32_bf16 v[20:23], v[68:71], v[156:159], v[20:23]\n"
    "s_waitcnt lgkmcnt(4)\n"
    "v_mfma_f32_16x16x32_bf16 v[8:11], v[72:75], v[160:163], v[8:11]\n"
    "ds_read_b128 v[56:59], v121 offset:28672\n"
    "v_mfma_f32_16x16x32_bf16 v[8:11], v[76:79], v[164:167], v[8:11]\n"
    "ds_read_b128 v[60:63], v121 offset:30720\n"
    "v_mfma_f32_16x16x32_bf16 v[8:11], v[80:83], v[168:171], v[8:11]\n"
    "v_mfma_f32_16x16x32_bf16 v[8:11], v[84:87], v[172:175], v[8:11]\n"
    "v_mfma_f32_16x16x32_bf16 v[12:15], v[72:75], v[176:179], v[12:15]\n"
    "ds_read_b128 v[64:67], v121 offset:32768\n"
    "v_mfma_f32_16x16x32_bf16 v[12:15], v[76:79], v[180:183], v[12:15]\n"
    "ds_read_b128 v[68:71], v121 offset:34816\n"
    "v_mfma_f32_16x16x32_bf16 v[12:15], v[80:83], v[184:187], v[12:15]\n"
    "buffer_load_dwordx4 v[232:235], v4, s[20:23], s55 offen offset:1024 sc1\n"
    "v_mfma_f32_16x16x32_bf16 v[12:15], v[84:87], v[188:191], v[12:15]\n"
    "v_cvt_pk_bf16_f32 v32, v208, v209\n"
    "s_waitcnt lgkmcnt(4)\n"
    "v_mfma_f32_16x16x32_bf16 v[16:19], v[40:43], v[160:163], v[16:19]\n"
    "ds_read_b128 v[72:75], v121 offset:29696\n"
    "v_cvt_pk_bf16_f32 v33, v210, v211\n"
    "v_mfma_f32_16x16x32_bf16 v[16:19], v[44:47], v[164:167], v[16:19]\n"
    "ds_read_b128 v[76:79], v121 offset:31744\n"
    "v_cvt_pk_bf16_f32 v34, v116, v117\n"
    "v_mfma_f32_16x16x32_bf16 v[16:19], v[48:51], v[168:171], v[16:19]\n"
    "v_mfma_f32_16x16x32_bf16 v[16:19], v[52:55], v[172:175], v[16:19]\n"
    "v_cvt_pk_bf16_f32 v35, v118, v119\n"
    "v_mfma_f32_16x16x32_bf16 v[20:23], v[40:43], v[176:179], v[20:23]\n"
    "ds_read_b128 v[80:83], v121 offset:33792\n"
    "v_mfma_f32_16x16x32_bf16 v[20:23], v[44:47], v[180:183], v[20:23]\n"
    "ds_read_b128 v[84:87], v121 offset:35840\n"
    "v_mfma_f32_16x16x32_bf16 v[20:23], v[48:51], v[184:187], v[20:23]\n"
    "buffer_store_dwordx4 v[32:35], v4, s[20:23], s15 offen offset:2048 sc1\n"
    "v_mfma_f32_16x16x32_bf16 v[20:23], v[52:55], v[188:191], v[20:23]\n"
    "s_waitcnt lgkmcnt(4)\n"
    "v_mfma_f32_16x16x32_bf16 v[8:11], v[56:59], a[0:3], v[8:11]\n"
    "ds_read_b128 v[40:43], v121 offset:36864\n"
    "v_mfma_f32_16x16x32_bf16 v[8:11], v[60:63], a[4:7], v[8:11]\n"
    "ds_read_b128 v[44:47], v121 offset:38912\n"
    "v_mfma_f32_16x16x32_bf16 v[8:11], v[64:67], a[8:11], v[8:11]\n"
    "v_mfma_f32_16x16x32_bf16 v[8:11], v[68:71], a[12:15], v[8:11]\n"
    "v_and_b32 v172, 1, v3\n"
    "v_lshrrev_b32 v173, 1, v3\n"
    "v_mfma_f32_16x16x32_bf16 v[12:15], v[56:59], a[16:19], v[12:15]\n"
    "ds_read_b128 v[48:51], v121 offset:40960\n"
    "v_lshlrev_b32 v164, 4, v2\n"
    "v_mfma_f32_16x16x32_bf16 v[12:15], v[60:63], a[20:23], v[12:15]\n"
    "ds_read_b128 v[52:55], v121 offset:43008\n"
    "v_lshl_add_u32 v165, v3, 9, v164\n"
    "v_mfma_f32_16x16x32_bf16 v[12:15], v[64:67], a[24:27], v[12:15]\n"
    "buffer_load_dwordx4 v[236:239], v4, s[20:23], s55 offen offset:2048 sc1\n"
    "v_lshl_add_u32 v164, v172, 11, v164\n"
    "v_mfma_f32_16x16x32_bf16 v[12:15], v[68:71], a[28:31], v[12:15]\n"
    "v_lshl_add_u32 v164, v173, 8, v164\n"
    "s_waitcnt lgkmcnt(4)\n"
    "v_mfma_f32_16x16x32_bf16 v[16:19], v[72:75], a[0:3], v[16:19]\n"
    "ds_read_b128 v[56:59], v121 offset:37888\n"
    "v_mfma_f32_16x16x32_bf16 v[16:19], v[76:79], a[4:7], v[16:19]\n"
    "ds_read_b128 v[60:63], v121 offset:39936\n"
    "v_mfma_f32_16x16x32_bf16 v[16:19], v[80:83], a[8:11], v[16:19]\n"
    "buffer_load_dwordx2 v[128:129], v164, s[20:23], s59 offen offset:0 sc1\n"
    "v_mfma_f32_16x16x32_bf16 v[16:19], v[84:87], a[12:15], v[16:19]\n"
    "v_mfma_f32_16x16x32_bf16 v[20:23], v[72:75], a[16:19], v[20:23]\n"
    "ds_read_b128 v[64:67], v121 offset:41984\n"
    "v_mfma_f32_16x16x32_bf16 v[20:23], v[76:79], a[20:23], v[20:23]\n"
    "ds_read_b128 v[68:71], v121 offset:44032\n"
    "v_mfma_f32_16x16x32_bf16 v[20:23], v[80:83], a[24:27], v[20:23]\n"
    "buffer_load_dwordx2 v[130:131], v164, s[20:23], s60 offen offset:0 sc1\n"
    "v_mfma_f32_16x16x32_bf16 v[20:23], v[84:87], a[28:31], v[20:23]\n"
    "s_waitcnt vmcnt(13) lgkmcnt(0)\n"
    "s_barrier\n"
    "v_mfma_f32_16x16x32_bf16 v[8:11], v[40:43], a[32:35], v[8:11]\n"
    "ds_read_b128 v[72:75], v121 offset:49152\n"
    "v_mfma_f32_16x16x32_bf16 v[8:11], v[44:47], a[36:39], v[8:11]\n"
    "ds_read_b128 v[76:79], v121 offset:51200\n"
    "v_mfma_f32_16x16x32_bf16 v[8:11], v[48:51], a[40:43], v[8:11]\n"
    "buffer_load_dwordx2 v[132:133], v164, s[20:23], s59 offen offset:1024 sc1\n"
    "v_mfma_f32_16x16x32_bf16 v[8:11], v[52:55], a[44:47], v[8:11]\n"
    "v_mfma_f32_16x16x32_bf16 v[12:15], v[40:43], a[48:51], v[12:15]\n"
    "ds_read_b128 v[80:83], v121 offset:53248\n"
    "v_mfma_f32_16x16x32_bf16 v[12:15], v[44:47], a[52:55], v[12:15]\n"
    "ds_read_b128 v[84:87], v121 offset:55296\n"
    "v_mfma_f32_16x16x32_bf16 v[12:15], v[48:51], a[56:59], v[12:15]\n"
    "buffer_load_dwordx4 v[240:243], v4, s[20:23], s56 offen offset:1024 sc1\n"
    "v_mfma_f32_16x16x32_bf16 v[12:15], v[52:55], a[60:63], v[12:15]\n"
    "buffer_load_dwordx2 v[134:135], v164, s[20:23], s60 offen offset:1024 sc1\n"
    "v_mfma_f32_16x16x32_bf16 v[16:19], v[56:59], a[32:35], v[16:19]\n"
    "ds_read_b128 v[40:43], v121 offset:50176\n"
    "v_mfma_f32_16x16x32_bf16 v[16:19], v[60:63], a[36:39], v[16:19]\n"
    "ds_read_b128 v[44:47], v121 offset:52224\n"
    "v_mfma_f32_16x16x32_bf16 v[16:19], v[64:67], a[40:43], v[16:19]\n"
    "buffer_load_dwordx2 v[136:137], v165, s[20:23], s61 offen sc1\n"
    "v_mfma_f32_16x16x32_bf16 v[16:19], v[68:71], a[44:47], v[16:19]\n"
    "v_mfma_f32_16x16x32_bf16 v[20:23], v[56:59], a[48:51], v[20:23]\n"
    "ds_read_b128 v[48:51], v121 offset:54272\n"
    "v_mfma_f32_16x16x32_bf16 v[20:23], v[60:63], a[52:55], v[20:23]\n"
    "ds_read_b128 v[52:55], v121 offset:56320\n"
    "v_mfma_f32_16x16x32_bf16 v[20:23], v[64:67], a[56:59], v[20:23]\n"
    "buffer_load_dwordx2 v[138:139], v165, s[20:23], s62 offen sc1\n"
    "v_bfe_u32 v172, v1, 2, 2\n"
    "v_mfma_f32_16x16x32_bf16 v[20:23], v[68:71], a[60:63], v[20:23]\n"
    "v_lshl_add_u32 v172, v172, 5, v3\n"
    "s_waitcnt lgkmcnt(4)\n"
    "v_mfma_f32_16x16x32_bf16 v[8:11], v[72:75], a[64:67], v[8:11]\n"
    "ds_read_b128 v[56:59], v121 offset:57344\n"
    "v_mul_u32_u24 v167, 0x480, v172\n"
    "v_mfma_f32_16x16x32_bf16 v[8:11], v[76:79], a[68:71], v[8:11]\n"
    "ds_read_b128 v[60:63], v121 offset:59392\n"
    "v_and_b32 v173, 3, v1\n"
    "v_mfma_f32_16x16x32_bf16 v[8:11], v[80:83], a[72:75], v[8:11]\n"
    "v_mfma_f32_16x16x32_bf16 v[8:11], v[84:87], a[76:79], v[8:11]\n"
    "v_lshl_add_u32 v166, v173, 4, v167\n"
    "v_lshl_add_u32 v167, v173, 3, v167\n"
    "v_mfma_f32_16x16x32_bf16 v[12:15], v[72:75], a[80:83], v[12:15]\n"
    "ds_read_b128 v[64:67], v121 offset:61440\n"
    "v_lshl_add_u32 v172, v2, 2, v3\n"
    "v_mfma_f32_16x16x32_bf16 v[12:15], v[76:79], a[84:87], v[12:15]\n"
    "ds_read_b128 v[68:71], v121 offset:63488\n"
    "v_lshl_add_u32 v168, v172, 4, s63\n"
    "v_mfma_f32_16x16x32_bf16 v[12:15], v[80:83], a[88:91], v[12:15]\n"
    "buffer_load_dwordx4 v[244:247], v4, s[20:23], s56 offen offset:2048 sc1\n"
    "v_lshl_add_u32 v169, v172, 3, s63\n"
    "v_mfma_f32_16x16x32_bf16 v[12:15], v[84:87], a[92:95], v[12:15]\n"
    "v_add_u32 v170, s63, v4\n"
    "s_waitcnt lgkmcnt(4)\n"
    "v_mfma_f32_16x16x32_bf16 v[16:19], v[40:43], a[64:67], v[16:19]\n"
    "ds_read_b128 v[72:75], v121 offset:58368\n"
    "v_lshl_add_u32 v171, v1, 3, s63\n"
    "v_mfma_f32_16x16x32_bf16 v[16:19], v[44:47], a[68:71], v[16:19]\n"
    "ds_read_b128 v[76:79], v121 offset:60416\n"
    "s_mov_b64 s[24:25], s[16:17]\n"
    "v_mfma_f32_16x16x32_bf16 v[16:19], v[48:51], a[72:75], v[16:19]\n"
    "v_mfma_f32_16x16x32_bf16 v[16:19], v[52:55], a[76:79], v[16:19]\n"
    "s_min_u32 s26, s6, s30\n"
    "v_and_b32 v172, 1, v2\n"
    "v_mfma_f32_16x16x32_bf16 v[20:23], v[40:43], a[80:83], v[20:23]\n"
    "ds_read_b128 v[80:83], v121 offset:62464\n"
    "v_lshlrev_b32 v172, 4, v172\n"
    "v_mfma_f32_16x16x32_bf16 v[20:23], v[44:47], a[84:87], v[20:23]\n"
    "ds_read_b128 v[84:87], v121 offset:64512\n"
    "v_mov_b32 v173, 0x3f80\n"
    "v_mfma_f32_16x16x32_bf16 v[20:23], v[48:51], a[88:91], v[20:23]\n"
    "v_mfma_f32_16x16x32_bf16 v[20:23], v[52:55], a[92:95], v[20:23]\n"
    "v_lshlrev_b32 v172, v172, v173\n"
    "s_waitcnt lgkmcnt(4)\n"
    "v_mfma_f32_16x16x32_bf16 v[8:11], v[56:59], a[96:99], v[8:11]\n"
    "ds_read_b128 v[40:43], v122 offset:24576\n"
    "v_lshrrev_b32 v173, 1, v2\n"
    "v_mfma_f32_16x16x32_bf16 v[8:11], v[60:63], a[100:103], v[8:11]\n"
    "ds_read_b128 v[44:47], v122 offset:26624\n"
    "v_lshlrev_b32 v142, 1, v3\n"
    "v_mfma_f32_16x16x32_bf16 v[8:11], v[64:67], a[104:107], v[8:11]\n"
    "v_mfma_f32_16x16x32_bf16 v[8:11], v[68:71], a[108:111], v[8:11]\n"
    "v_cmp_eq_u32 vcc, v173, v142\n"
    "v_add_u32 v142, 1, v142\n"
    "v_mfma_f32_16x16x32_bf16 v[12:15], v[56:59], a[112:115], v[12:15]\n"
    "ds_read_b128 v[48:51], v122 offset:28672\n"
    "v_mov_b32 v143, 0\n"
    "v_mfma_f32_16x16x32_bf16 v[12:15], v[60:63], a[116:119], v[12:15]\n"
    "ds_read_b128 v[52:55], v122 offset:30720\n"
    "v_cndmask_b32 v140, 0, v172, vcc\n"
    "v_mfma_f32_16x16x32_bf16 v[12:15], v[64:67], a[120:123], v[12:15]\n"
    "buffer_load_dwordx4 v[248:251], v4, s[20:23], s57 offen offset:1024 sc1\n"
    "v_cmp_eq_u32 vcc, v173, v142\n"
    "v_mfma_f32_16x16x32_bf16 v[12:15], v[68:71], a[124:127], v[12:15]\n"
    "v_mov_b32 v142, 0\n"
    "s_waitcnt lgkmcnt(4)\n"
    "v_mfma_f32_16x16x32_bf16 v[16:19], v[72:75], a[96:99], v[16:19]\n"
    "ds_read_b128 v[56:59], v122 offset:25600\n"
    "v_mov_b32 v144, 0\n"
    "v_mfma_f32_16x16x32_bf16 v[16:19], v[76:79], a[100:103], v[16:19]\n"
    "ds_read_b128 v[60:63], v122 offset:27648\n"
    "v_cndmask_b32 v141, 0, v172, vcc\n"
    "v_mfma_f32_16x16x32_bf16 v[16:19], v[80:83], a[104:107], v[16:19]\n"
    "v_mfma_f32_16x16x32_bf16 v[16:19], v[84:87], a[108:111], v[16:19]\n"
    "v_mov_b32 v145, 0\n"
    "v_mov_b32 v146, v140\n"
    "v_mfma_f32_16x16x32_bf16 v[20:23], v[72:75], a[112:115], v[20:23]\n"
    "ds_read_b128 v[64:67], v122 offset:29696\n"
    "v_mov_b32 v147, v141\n"
    "v_mfma_f32_16x16x32_bf16 v[20:23], v[76:79], a[116:119], v[20:23]\n"
    "ds_read_b128 v[68:71], v122 offset:31744\n"
    "v_mfma_f32_16x16x32_bf16 v[20:23], v[80:83], a[120:123], v[20:23]\n"
    "v_mfma_f32_16x16x32_bf16 v[20:23], v[84:87], a[124:127], v[20:23]\n"
    "s_waitcnt vmcnt(15) lgkmcnt(0)\n"
    "s_barrier\n"
    "v_mfma_f32_16x16x32_bf16 v[8:11], v[40:43], a[128:131], v[8:11]\n"
    "ds_read_b128 v[72:75], v4 offset:57344\n"
    "v_mfma_f32_16x16x32_bf16 v[8:11], v[44:47], a[132:135], v[8:11]\n"
    "ds_read_b128 v[76:79], v4 offset:59392\n"
    "v_mfma_f32_16x16x32_bf16 v[8:11], v[48:51], a[136:139], v[8:11]\n"
    "v_mfma_f32_16x16x32_bf16 v[8:11], v[52:55], a[140:143], v[8:11]\n"
    "v_mfma_f32_16x16x32_bf16 v[12:15], v[40:43], a[144:147], v[12:15]\n"
    "ds_read_b128 v[80:83], v4 offset:61440\n"
    "v_mfma_f32_16x16x32_bf16 v[12:15], v[44:47], a[148:151], v[12:15]\n"
    "ds_read_b128 v[84:87], v4 offset:63488\n"
    "v_mfma_f32_16x16x32_bf16 v[12:15], v[48:51], a[152:155], v[12:15]\n"
    "v_mfma_f32_16x16x32_bf16 v[12:15], v[52:55], a[156:159], v[12:15]\n"
    "v_mfma_f32_16x16x32_bf16 v[16:19], v[56:59], a[128:131], v[16:19]\n"
    "ds_read_b128 v[40:43], v4 offset:58368\n"
    "v_mfma_f32_16x16x32_bf16 v[16:19], v[60:63], a[132:135], v[16:19]\n"
    "ds_read_b128 v[44:47], v4 offset:60416\n"
    "v_mfma_f32_16x16x32_bf16 v[16:19], v[64:67], a[136:139], v[16:19]\n"
    "v_mfma_f32_16x16x32_bf16 v[16:19], v[68:71], a[140:143], v[16:19]\n"
    "v_mfma_f32_16x16x32_bf16 v[20:23], v[56:59], a[144:147], v[20:23]\n"
    "ds_read_b128 v[48:51], v4 offset:62464\n"
    "v_mfma_f32_16x16x32_bf16 v[20:23], v[60:63], a[148:151], v[20:23]\n"
    "ds_read_b128 v[52:55], v4 offset:64512\n"
    "v_mfma_f32_16x16x32_bf16 v[20:23], v[64:67], a[152:155], v[20:23]\n"
    "buffer_load_dwordx4 v[252:255], v4, s[20:23], s57 offen offset:2048 sc1\n"
    "v_mfma_f32_16x16x32_bf16 v[20:23], v[68:71], a[156:159], v[20:23]\n"
    "s_waitcnt lgkmcnt(4)\n"
    "v_mfma_f32_16x16x32_bf16 v[8:11], v[72:75], a[160:163], v[8:11]\n"
    "ds_read_b128 v[56:59], v121 offset:0\n"
    "v_mfma_f32_16x16x32_bf16 v[8:11], v[76:79], a[164:167], v[8:11]\n"
    "ds_read_b128 v[60:63], v121 offset:2048\n"
    "v_mfma_f32_16x16x32_bf16 v[8:11], v[80:83], a[168:171], v[8:11]\n"
    "v_mfma_f32_16x16x32_bf16 v[8:11], v[84:87], a[172:175], v[8:11]\n"
    "v_mfma_f32_16x16x32_bf16 v[12:15], v[72:75], a[176:179], v[12:15]\n"
    "ds_read_b128 v[64:67], v121 offset:4096\n"
    "v_mfma_f32_16x16x32_bf16 v[12:15], v[76:79], a[180:183], v[12:15]\n"
    "ds_read_b128 v[68:71], v121 offset:6144\n"
    "v_mfma_f32_16x16x32_bf16 v[12:15], v[80:83], a[184:187], v[12:15]\n"
    "v_mfma_f32_16x16x32_bf16 v[12:15], v[84:87], a[188:191], v[12:15]\n"
    "s_waitcnt lgkmcnt(4)\n"
    "v_mfma_f32_16x16x32_bf16 v[16:19], v[40:43], a[160:163], v[16:19]\n"
    "ds_read_b128 v[72:75], v121 offset:1024\n"
    "v_mfma_f32_16x16x32_bf16 v[16:19], v[44:47], a[164:167], v[16:19]\n"
    "ds_read_b128 v[76:79], v121 offset:3072\n"
    "v_mfma_f32_16x16x32_bf16 v[16:19], v[48:51], a[168:171], v[16:19]\n"
    "v_mfma_f32_16x16x32_bf16 v[16:19], v[52:55], a[172:175], v[16:19]\n"
    "v_mfma_f32_16x16x32_bf16 v[20:23], v[40:43], a[176:179], v[20:23]\n"
    "ds_read_b128 v[80:83], v121 offset:5120\n"
    "v_mfma_f32_16x16x32_bf16 v[20:23], v[44:47], a[180:183], v[20:23]\n"
    "ds_read_b128 v[84:87], v121 offset:7168\n"
    "v_mfma_f32_16x16x32_bf16 v[20:23], v[48:51], a[184:187], v[20:23]\n"
    "v_mfma_f32_16x16x32_bf16 v[20:23], v[52:55], a[188:191], v[20:23]\n"
    "s_waitcnt lgkmcnt(4)\n"
    "v_mfma_f32_16x16x32_bf16 v[8:11], v[56:59], a[192:195], v[8:11]\n"
    "v_mfma_f32_16x16x32_bf16 v[8:11], v[60:63], a[196:199], v[8:11]\n"
    "v_mfma_f32_16x16x32_bf16 v[8:11], v[64:67], a[200:203], v[8:11]\n"
    "v_mfma_f32_16x16x32_bf16 v[8:11], v[68:71], a[204:207], v[8:11]\n"
    "v_mfma_f32_16x16x32_bf16 v[12:15], v[56:59], a[208:211], v[12:15]\n"
    "v_mfma_f32_16x16x32_bf16 v[12:15], v[60:63], a[212:215], v[12:15]\n"
    "v_mfma_f32_16x16x32_bf16 v[12:15], v[64:67], a[216:219], v[12:15]\n"
    "v_mfma_f32_16x16x32_bf16 v[12:15], v[68:71], a[220:223], v[12:15]\n"
    "s_waitcnt lgkmcnt(0)\n"
    "v_mfma_f32_16x16x32_bf16 v[16:19], v[72:75], a[192:195], v[16:19]\n"
    "v_mfma_f32_16x16x32_bf16 v[16:19], v[76:79], a[196:199], v[16:19]\n"
    "v_mfma_f32_16x16x32_bf16 v[16:19], v[80:83], a[200:203], v[16:19]\n"
    "v_mfma_f32_16x16x32_bf16 v[16:19], v[84:87], a[204:207], v[16:19]\n"
    "v_mfma_f32_16x16x32_bf16 v[20:23], v[72:75], a[208:211], v[20:23]\n"
    "v_mfma_f32_16x16x32_bf16 v[20:23], v[76:79], a[212:215], v[20:23]\n"
    "v_mfma_f32_16x16x32_bf16 v[20:23], v[80:83], a[216:219], v[20:23]\n"
    "v_mfma_f32_16x16x32_bf16 v[20:23], v[84:87], a[220:223], v[20:23]\n"
    "s_branch LM_X_33\n"
    "LG1_a_s:\n"
    "s_add_u32 m0, s58, 0\n"
    "s_nop 0\n"
    "buffer_load_dwordx4 v4, s[20:23], s59 offen offset:0 sc1 lds\n"
    "buffer_load_dwordx4 v4, s[20:23], s59 offen offset:1024 sc1 lds\n"
    "buffer_load_dwordx4 v4, s[20:23], s59 offen offset:2048 sc1 lds\n"
    "buffer_load_dwordx4 v4, s[20:23], s59 offen offset:3072 sc1 lds\n"
    "buffer_load_dwordx4 v4, s[20:23], s59 offen offset:3584 sc1 lds\n"
    "s_waitcnt vmcnt(0)\n"
    "ds_read_b128 v[128:131], v213 offset:0\n"
    "ds_read_b128 v[132:135], v213 offset:1024\n"
    "ds_read_b128 v[136:139], v213 offset:2048\n"
    "ds_read_b128 v[140:143], v213 offset:3072\n"
    "ds_read_b128 v[144:147], v213 offset:3584\n"
    "s_waitcnt lgkmcnt(0)\n"
    "v_max3_u32 v106, v128, v129, v130\n"
    "v_max3_u32 v106, v106, v131, v132\n"
    "v_max3_u32 v106, v106, v133, v134\n"
    "v_max3_u32 v106, v106, v135, v136\n"
    "v_max3_u32 v106, v106, v137, v138\n"
    "v_max3_u32 v106, v106, v139, v140\n"
    "v_max3_u32 v106, v106, v141, v142\n"
    "v_max3_u32 v106, v106, v143, v144\n"
    "v_max3_u32 v106, v106, v145, v146\n"
    "v_max_u32 v106, v106, v147\n"
    "v_cmp_eq_u32 vcc, s45, v106\n"
    "s_nop 1\n"
    "s_cbranch_vccz LG1_a_r\n"
    "s_sub_u32 s39, s39, 1\n"
    "s_cbranch_scc0 LG1_a_s\n"
    "s_mov_b32 s39, 0\n"
    "s_branch LG1_a_r\n"
    "LG1_b_s:\n"
    "s_add_u32 m0, s58, 18432\n"
    "s_nop 0\n"
    "buffer_load_dwordx4 v4, s[20:23], s60 offen offset:0 sc1 lds\n"
    "buffer_load_dwordx4 v4, s[20:23], s60 offen offset:1024 sc1 lds\n"
    "buffer_load_dwordx4 v4, s[20:23], s60 offen offset:2048 sc1 lds\n"
    "buffer_load_dwordx4 v4, s[20:23], s60 offen offset:3072 sc1 lds\n"
    "buffer_load_dwordx4 v4, s[20:23], s60 offen offset:3584 sc1 lds\n"
    "s_waitcnt vmcnt(0)\n"
    "ds_read_b128 v[128:131], v213 offset:18432\n"
    "ds_read_b128 v[132:135], v213 offset:19456\n"
    "ds_read_b128 v[136:139], v213 offset:20480\n"
    "ds_read_b128 v[140:143], v213 offset:21504\n"
    "ds_read_b128 v[144:147], v213 offset:22016\n"
    "s_waitcnt lgkmcnt(0)\n"
    "v_max3_u32 v106, v128, v129, v130\n"
    "v_max3_u32 v106, v106, v131, v132\n"
    "v_max3_u32 v106, v106, v133, v134\n"
    "v_max3_u32 v106, v106, v135, v136\n"
    "v_max3_u32 v106, v106, v137, v138\n"
    "v_max3_u32 v106, v106, v139, v140\n"
    "v_max3_u32 v106, v106, v141, v142\n"
    "v_max3_u32 v106, v106, v143, v144\n"
    "v_max3_u32 v106, v106, v145, v146\n"
    "v_max_u32 v106, v106, v147\n"
    "v_cmp_eq_u32 vcc, s45, v106\n"
    "s_nop 1\n"
    "s_cbranch_vccz LG1_b_r\n"
    "s_sub_u32 s39, s39, 1\n"
    "s_cbranch_scc0 LG1_b_s\n"
    "s_mov_b32 s39, 0\n"
    "s_branch LG1_b_r\n"
    "LG1_hx_s:\n"
    "s_sub_u32 s39, s39, 1\n"
    "s_cbranch_scc1 LG1_hx_g\n"
    "s_sleep 1\n"
    "s_mov_b32 s85, 90112\n"
    "buffer_load_dwordx2 v[104:105], v212, s[20:23], s85 offen sc1\n"
    "s_waitcnt vmcnt(0)\n"
    "v_xor_b32 v106, s40, v104\n"
    "v_xor_b32 v107, s41, v105\n"
    "v_or_b32 v106, v106, v107\n"
    "v_cmp_ne_u32 vcc, 0, v106\n"
    "s_nop 1\n"
    "s_cbranch_vccnz LG1_hx_s\n"
    "s_branch LG1_hx_r\n"
    "LG1_hx_g:\n"
    "s_mov_b32 s39, 0\n"
    "s_branch LG1_hx_r\n"
    "LG1_fx_s:\n"
    "s_sub_u32 s39, s39, 1\n"
    "s_cbranch_scc1 LG1_fx_g\n"
    "s_sleep 1\n"
    "s_mov_b32 s85, 106496\n"
    "buffer_load_dwordx2 v[104:105], v108, s[20:23], s85 offen sc1\n"
    "s_waitcnt vmcnt(0)\n"
    "v_xor_b32 v106, s40, v104\n"
    "v_xor_b32 v107, s41, v105\n"
    "v_or_b32 v106, v106, v107\n"
    "v_cmp_ne_u32 vcc, 0, v106\n"
    "s_nop 1\n"
    "s_cbranch_vccnz LG1_fx_s\n"
    "s_branch LG1_fx_r\n"
    "LG1_fx_g:\n"
    "s_mov_b32 s39, 0\n"
    "s_branch LG1_fx_r\n"
    "LG1_fr_s:\n"
    "s_sub_u32 s39, s39, 1\n"
    "s_cbranch_scc1 LG1_fr_g\n"
    "s_sleep 1\n"
    "s_mov_b32 s85, 335872\n"
    "buffer_load_dwordx2 v[104:105], v109, s[20:23], s85 offen sc1\n"
    "s_waitcnt vmcnt(0)\n"
    "v_xor_b32 v106, s40, v104\n"
    "v_xor_b32 v107, s41, v105\n"
    "v_or_b32 v106, v106, v107\n"
    "v_cmp_ne_u32 vcc, 0, v106\n"
    "s_nop 1\n"
    "s_cbranch_vccnz LG1_fr_s\n"
    "s_branch LG1_fr_r\n"
    "LG1_fr_g:\n"
    "s_mov_b32 s39, 0\n"
    "s_branch LG1_fr_r\n"
    "LG1_ep:\n"
    "s_add_u32 s85, s37, 1\n"
    "v_mov_b32 v106, s85\n"
    "s_mov_b64 exec, 1\n"
    "buffer_store_dword v106, off, s[20:23], 0 sc1\n"
    "s_mov_b64 exec, -1\n"
    "s_branch LG1_ep_r\n"
    "LG1_fk_s:\n"
    "s_sub_u32 s39, s39, 1\n"
    "s_cbranch_scc1 LG1_fk_g\n"
    "s_sleep 1\n"
    "s_mov_b32 s85, 303104\n"
    "buffer_load_dwordx2 v[104:105], v110, s[20:23], s85 offen sc1\n"
    "s_waitcnt vmcnt(0)\n"
    "v_xor_b32 v106, s40, v104\n"
    "v_xor_b32 v107, s41, v105\n"
    "v_or_b32 v106, v106, v107\n"
    "v_cmp_ne_u32 vcc, 0, v106\n"
    "s_nop 1\n"
    "s_and_b64 vcc, vcc, 15\n"
    "s_cbranch_vccnz LG1_fk_s\n"
    "s_branch LG1_fk_r\n"
    "LG1_fk_g:\n"
    "s_mov_b32 s39, 0\n"
    "s_branch LG1_fk_r\n"
    "LG1_fm_s:\n"
    "s_sub_u32 s39, s39, 1\n"
    "s_cbranch_scc1 LG1_fm_g\n"
    "s_sleep 1\n"
    "s_mov_b32 s85, 270336\n"
    "buffer_load_dwordx2 v[104:105], v110, s[20:23], s85 offen sc1\n"
    "s_waitcnt vmcnt(0)\n"
    "v_xor_b32 v106, s40, v104\n"
    "v_xor_b32 v107, s41, v105\n"
    "v_or_b32 v106, v106, v107\n"
    "v_cmp_ne_u32 vcc, 0, v106\n"
    "s_nop 1\n"
    "s_and_b64 vcc, vcc, 255\n"
    "s_cbranch_vccnz LG1_fm_s\n"
    "s_branch LG1_fm_r\n"
    "LG1_fm_g:\n"
    "s_mov_b32 s39, 0\n"
    "s_branch LG1_fm_r\n"
    "LG2_N_s:\n"
    "s_sub_u32 s39, s39, 1\n"
    "s_cbranch_scc1 LG2_N_g\n"
    "s_sleep 1\n"
    "s_mov_b32 s85, 237568\n"
    "buffer_load_dwordx2 v[104:105], v110, s[20:23], s85 offen sc1\n"
    "s_waitcnt vmcnt(0)\n"
    "v_xor_b32 v106, s40, v104\n"
    "v_xor_b32 v107, s41, v105\n"
    "v_or_b32 v106, v106, v107\n"
    "v_cmp_ne_u32 vcc, 0, v106\n"
    "s_nop 1\n"
    "s_and_b64 vcc, vcc, 16383\n"
    "s_cbranch_vccnz LG2_N_s\n"
    "s_branch LG2_N_r\n"
    "LG2_N_g:\n"
    "s_mov_b32 s39, 0\n"
    "s_branch LG2_N_r\n"
    "LG2_H_s:\n"
    "s_sub_u32 s39, s39, 1\n"
    "s_cbranch_scc1 LG2_H_g\n"
    "s_sleep 1\n"
    "s_mov_b32 s85, 122880\n"
    "buffer_load_dwordx2 v[104:105], v110, s[20:23], s85 offen sc1\n"
    "s_waitcnt vmcnt(0)\n"
    "v_xor_b32 v106, s40, v104\n"
    "v_xor_b32 v107, s41, v105\n"
    "v_or_b32 v106, v106, v107\n"
    "v_cmp_ne_u32 vcc, 0, v106\n"
    "s_nop 1\n"
    "s_cbranch_vccnz LG2_H_s\n"
    "s_branch LG2_H_r\n"
    "LG2_H_g:\n"
    "s_mov_b32 s39, 0\n"
    "s_branch LG2_H_r\n"
    "LG2_S_s:\n"
    "s_sub_u32 s39, s39, 1\n"
    "s_cbranch_scc1 LG2_S_g\n"
    "s_sleep 1\n"
    "s_mov_b32 s85, 155648\n"
    "buffer_load_dwordx2 v[104:105], v110, s[20:23], s85 offen sc1\n"
    "s_waitcnt vmcnt(0)\n"
    "v_xor_b32 v106, s40, v104\n"
    "v_xor_b32 v107, s41, v105\n"
    "v_or_b32 v106, v106, v107\n"
    "v_cmp_ne_u32 vcc, 0, v106\n"
    "s_nop 1\n"
    "s_and_b64 vcc, vcc, 3932160\n"
    "s_cbranch_vccnz LG2_S_s\n"
    "s_branch LG2_S_r\n"
    "LG2_S_g:\n"
    "s_mov_b32 s39, 0\n"
    "s_branch LG2_S_r\n"
    "LM_NG_30:\n"
    "s_mov_b32 s26, 0\n"
    "s_add_u32 s33, s10, 252\n"
    "s_add_u32 s78, s86, 0\n"
    "s_add_u32 m0, s84, 0\n"
    "s_nop 0\n"
    "buffer_load_dwordx4 v111, s[28:31], s78 offen lds\n"
    "s_add_u32 s78, s86, 9216\n"
    "s_add_u32 m0, s84, 1024\n"
    "s_nop 0\n"
    "buffer_load_dwordx4 v111, s[28:31], s78 offen lds\n"
    "s_add_u32 s78, s86, 18432\n"
    "s_add_u32 m0, s84, 2048\n"
    "s_nop 0\n"
    "buffer_load_dwordx4 v111, s[28:31], s78 offen lds\n"
    "s_add_u32 s78, s86, 27648\n"
    "s_add_u32 m0, s84, 3072\n"
    "s_nop 0\n"
    "buffer_load_dwordx4 v111, s[28:31], s78 offen lds\n"
    "s_add_u32 s78, s87, 0\n"
    "s_add_u32 m0, s84, 4096\n"
    "s_nop 0\n"
    "buffer_load_dwordx4 v111, s[28:31], s78 offen lds\n"
    "s_add_u32 s78, s87, 9216\n"
    "s_add_u32 m0, s84, 5120\n"
    "s_nop 0\n"
    "buffer_load_dwordx4 v111, s[28:31], s78 offen lds\n"
    "s_add_u32 s78, s87, 18432\n"
    "s_add_u32 m0, s84, 6144\n"
    "s_nop 0\n"
    "buffer_load_dwordx4 v111, s[28:31], s78 offen lds\n"
    "s_add_u32 s78, s87, 27648\n"
    "s_add_u32 m0, s84, 7168\n"
    "s_nop 0\n"
    "buffer_load_dwordx4 v111, s[28:31], s78 offen lds\n"
    "buffer_store_dwordx4 v[116:119], v4, s[20:23], s13 offen offset:0 sc1\n"
    "buffer_store_dwordx4 v[116:119], v4, s[20:23], s13 offen offset:1024 sc1\n"
    "buffer_store_dwordx4 v[116:119], v4, s[20:23], s13 offen offset:2048 sc1\n"
    "buffer_store_dwordx4 v[116:119], v4, s[20:23], s13 offen offset:3072 sc1\n"
    "buffer_store_dwordx4 v[116:119], v4, s[20:23], s15 offen offset:0 sc1\n"
    "buffer_store_dwordx4 v[116:119], v4, s[20:23], s15 offen offset:1024 sc1\n"
    "buffer_store_dwordx4 v[116:119], v4, s[20:23], s15 offen offset:2048 sc1\n"
    "buffer_store_dwordx4 v[192:195], v4, s[52:55], s56 offen sc1\n"
    "s_add_u32 s56, s56, 4096\n"
    "buffer_store_dwordx4 v[196:199], v4, s[52:55], s56 offen sc1\n"
    "s_add_u32 s56, s56, 4096\n"
    "buffer_store_dwordx4 v[200:203], v4, s[52:55], s56 offen sc1\n"
    "s_add_u32 s56, s56, 4096\n"
    "buffer_store_dwordx4 v[204:207], v4, s[52:55], s56 offen sc1\n"
    "s_add_u32 s56, s56, 4096\n"
    "buffer_store_dwordx4 v[208:211], v125, s[52:55], s57 offen sc1\n"
    "s_waitcnt vmcnt(0)\n"
    "ds_read_b64_tr_b16 v[128:129], v114 offset:0\n"
    "ds_read_b64_tr_b16 v[130:131], v114 offset:2048\n"
    "ds_read_b64_tr_b16 v[132:133], v126 offset:0\n"
    "ds_read_b64_tr_b16 v[134:135], v126 offset:2048\n"
    "ds_read_b64_tr_b16 v[136:137], v114 offset:64\n"
    "ds_read_b64_tr_b16 v[138:139], v114 offset:2112\n"
    "ds_read_b64_tr_b16 v[140:141], v126 offset:64\n"
    "ds_read_b64_tr_b16 v[142:143], v126 offset:2112\n"
    "ds_read_b64_tr_b16 v[144:145], v115 offset:4096\n"
    "ds_read_b64_tr_b16 v[146:147], v115 offset:6144\n"
    "ds_read_b64_tr_b16 v[148:149], v127 offset:4096\n"
    "ds_read_b64_tr_b16 v[150:151], v127 offset:6144\n"
    "ds_read_b64_tr_b16 v[152:153], v115 offset:4160\n"
    "ds_read_b64_tr_b16 v[154:155], v115 offset:6208\n"
    "ds_read_b64_tr_b16 v[156:157], v127 offset:4160\n"
    "ds_read_b64_tr_b16 v[158:159], v127 offset:6208\n"
    "s_waitcnt lgkmcnt(0)\n"
    "LS_12:\n"
    "s_cbranch_execz LS_ret\n"
    "buffer_store_dwordx4 v[128:131], v4, s[20:23], s88 offen sc1\n"
    "buffer_store_dwordx4 v[132:135], v4, s[20:23], s89 offen sc1\n"
    "buffer_store_dwordx4 v[136:139], v4, s[20:23], s90 offen sc1\n"
    "buffer_store_dwordx4 v[140:143], v4, s[20:23], s91 offen sc1\n"
    "buffer_store_dwordx4 v[144:147], v4, s[4:7], s92 offen sc1\n"
    "buffer_store_dwordx4 v[148:151], v4, s[4:7], s93 offen sc1\n"
    "buffer_store_dwordx4 v[152:155], v4, s[4:7], s94 offen sc1\n"
    "buffer_store_dwordx4 v[156:159], v4, s[4:7], s95 offen sc1\n"
    "s_waitcnt vmcnt(0)\n"
    "s_barrier\n"
    "s_lshl_b32 s85, s2, 7\n"
    "s_add_u32 s85, s85, 335872\n"
    "buffer_store_dwordx4 v[218:221], v215, s[20:23], s85 offen sc1\n"
    "s_lshl_b32 s85, s2, 7\n"
    "s_add_u32 s85, s85, 155648\n"
    "buffer_store_dwordx4 v[218:221], v215, s[20:23], s85 offen sc1\n"
    "s_add_u32 s85, s9, 114688\n"
    "buffer_store_dwordx4 v[218:221], v215, s[52:55], s85 offen sc1\n"
    "s_branch LNG_fr_s\n"
    "LNG_fr_r:\n"
    "s_barrier\n"
    "v_readlane_b32 s77, v5, 0\n"
    "s_add_u32 m0, s83, 73728\n"
    "s_nop 4\n"
    "buffer_load_dwordx4 v6, s[20:23], s77 offen offset:0 sc1 lds\n"
    "buffer_load_dwordx4 v6, s[20:23], s77 offen offset:1024 sc1 lds\n"
    "buffer_load_dwordx4 v6, s[20:23], s77 offen offset:2048 sc1 lds\n"
    "buffer_load_dwordx4 v6, s[20:23], s77 offen offset:3072 sc1 lds\n"
    "s_add_u32 m0, s83, 77824\n"
    "s_nop 0\n"
    "buffer_load_dwordx4 v7, s[20:23], s77 offen offset:0 sc1 lds\n"
    "v_readlane_b32 s77, v5, 1\n"
    "s_add_u32 m0, s83, 94208\n"
    "s_nop 4\n"
    "buffer_load_dwordx4 v6, s[20:23], s77 offen offset:0 sc1 lds\n"
    "buffer_load_dwordx4 v6, s[20:23], s77 offen offset:1024 sc1 lds\n"
    "buffer_load_dwordx4 v6, s[20:23], s77 offen offset:2048 sc1 lds\n"
    "buffer_load_dwordx4 v6, s[20:23], s77 offen offset:3072 sc1 lds\n"
    "s_add_u32 m0, s83, 98304\n"
    "s_nop 0\n"
    "buffer_load_dwordx4 v7, s[20:23], s77 offen offset:0 sc1 lds\n"
    "v_readlane_b32 s77, v5, 2\n"
    "s_add_u32 m0, s83, 114688\n"
    "s_nop 4\n"
    "buffer_load_dwordx4 v6, s[20:23], s77 offen offset:0 sc1 lds\n"
    "buffer_load_dwordx4 v6, s[20:23], s77 offen offset:1024 sc1 lds\n"
    "buffer_load_dwordx4 v6, s[20:23], s77 offen offset:2048 sc1 lds\n"
    "buffer_load_dwordx4 v6, s[20:23], s77 offen offset:3072 sc1 lds\n"
    "s_add_u32 m0, s83, 118784\n"
    "s_nop 0\n"
    "buffer_load_dwordx4 v7, s[20:23], s77 offen offset:0 sc1 lds\n"
    "v_readlane_b32 s77, v5, 3\n"
    "s_add_u32 m0, s83, 135168\n"
    "s_nop 4\n"
    "buffer_load_dwordx4 v6, s[20:23], s77 offen offset:0 sc1 lds\n"
    "buffer_load_dwordx4 v6, s[20:23], s77 offen offset:1024 sc1 lds\n"
    "buffer_load_dwordx4 v6, s[20:23], s77 offen offset:2048 sc1 lds\n"
    "buffer_load_dwordx4 v6, s[20:23], s77 offen offset:3072 sc1 lds\n"
    "s_add_u32 m0, s83, 139264\n"
    "s_nop 0\n"
    "buffer_load_dwordx4 v7, s[20:23], s77 offen offset:0 sc1 lds\n"
    "v_readlane_b32 s77, v5, 4\n"
    "s_add_u32 m0, s83, 36864\n"
    "s_nop 4\n"
    "buffer_load_dwordx4 v6, s[20:23], s77 offen offset:0 sc1 lds\n"
    "buffer_load_dwordx4 v6, s[20:23], s77 offen offset:1024 sc1 lds\n"
    "buffer_load_dwordx4 v6, s[20:23], s77 offen offset:2048 sc1 lds\n"
    "buffer_load_dwordx4 v6, s[20:23], s77 offen offset:3072 sc1 lds\n"
    "s_add_u32 m0, s83, 40960\n"
    "s_nop 0\n"
    "buffer_load_dwordx4 v7, s[20:23], s77 offen offset:0 sc1 lds\n"
    "v_readlane_b32 s77, v5, 0\n"
    "s_add_u32 m0, s72, 154624\n"
    "s_nop 4\n"
    "buffer_load_dwordx4 v7, s[20:23], s77 offen offset:1024 sc1 lds\n"
    "buffer_load_dwordx4 v7, s[20:23], s77 offen offset:2048 sc1 lds\n"
    "v_readlane_b32 s77, v5, 1\n"
    "s_add_u32 m0, s72, 56320\n"
    "s_nop 4\n"
    "buffer_load_dwordx4 v7, s[20:23], s77 offen offset:1024 sc1 lds\n"
    "buffer_load_dwordx4 v7, s[20:23], s77 offen offset:2048 sc1 lds\n"
    "v_readlane_b32 s77, v5, 2\n"
    "s_add_u32 m0, s72, 64512\n"
    "s_nop 4\n"
    "buffer_load_dwordx4 v7, s[20:23], s77 offen offset:1024 sc1 lds\n"
    "buffer_load_dwordx4 v7, s[20:23], s77 offen offset:2048 sc1 lds\n"
    "s_branch LNG_fm_s\n"
    "LNG_fm_r:\n"
    "v_readlane_b32 s77, v5, 16\n"
    "s_nop 4\n"
    "buffer_load_dwordx4 v[128:131], v222, s[20:23], s77 offen offset:0 sc1\n"
    "buffer_load_dwordx4 v[144:147], v223, s[20:23], s77 offen offset:0 sc1\n"
    "buffer_load_dwordx4 v[132:135], v222, s[20:23], s77 offen offset:1024 sc1\n"
    "buffer_load_dwordx4 v[148:151], v223, s[20:23], s77 offen offset:1024 sc1\n"
    "buffer_load_dwordx4 v[136:139], v222, s[20:23], s77 offen offset:2048 sc1\n"
    "buffer_load_dwordx4 v[152:155], v223, s[20:23], s77 offen offset:2048 sc1\n"
    "buffer_load_dwordx4 v[140:143], v222, s[20:23], s77 offen offset:3072 sc1\n"
    "buffer_load_dwordx4 v[156:159], v223, s[20:23], s77 offen offset:3072 sc1\n"
    "v_readlane_b32 s77, v5, 17\n"
    "s_nop 4\n"
    "buffer_load_dwordx4 v[160:163], v222, s[20:23], s77 offen offset:0 sc1\n"
    "buffer_load_dwordx4 v[176:179], v223, s[20:23], s77 offen offset:0 sc1\n"
    "buffer_load_dwordx4 v[164:167], v222, s[20:23], s77 offen offset:1024 sc1\n"
    "buffer_load_dwordx4 v[180:183], v223, s[20:23], s77 offen offset:1024 sc1\n"
    "buffer_load_dwordx4 v[168:171], v222, s[20:23], s77 offen offset:2048 sc1\n"
    "buffer_load_dwordx4 v[184:187], v223, s[20:23], s77 offen offset:2048 sc1\n"
    "buffer_load_dwordx4 v[172:175], v222, s[20:23], s77 offen offset:3072 sc1\n"
    "buffer_load_dwordx4 v[188:191], v223, s[20:23], s77 offen offset:3072 sc1\n"
    "v_readlane_b32 s77, v5, 18\n"
    "s_nop 4\n"
    "buffer_load_dwordx4 a[0:3], v222, s[20:23], s77 offen offset:0 sc1\n"
    "buffer_load_dwordx4 a[16:19], v223, s[20:23], s77 offen offset:0 sc1\n"
    "buffer_load_dwordx4 a[4:7], v222, s[20:23], s77 offen offset:1024 sc1\n"
    "buffer_load_dwordx4 a[20:23], v223, s[20:23], s77 offen offset:1024 sc1\n"
    "buffer_load_dwordx4 a[8:11], v222, s[20:23], s77 offen offset:2048 sc1\n"
    "buffer_load_dwordx4 a[24:27], v223, s[20:23], s77 offen offset:2048 sc1\n"
    "buffer_load_dwordx4 a[12:15], v222, s[20:23], s77 offen offset:3072 sc1\n"
    "buffer_load_dwordx4 a[28:31], v223, s[20:23], s77 offen offset:3072 sc1\n"
    "v_readlane_b32 s77, v5, 19\n"
    "s_nop 4\n"
    "buffer_load_dwordx4 a[32:35], v222, s[20:23], s77 offen offset:0 sc1\n"
    "buffer_load_dwordx4 a[48:51], v223, s[20:23], s77 offen offset:0 sc1\n"
    "buffer_load_dwordx4 a[36:39], v222, s[20:23], s77 offen offset:1024 sc1\n"
    "buffer_load_dwordx4 a[52:55], v223, s[20:23], s77 offen offset:1024 sc1\n"
    "buffer_load_dwordx4 a[40:43], v222, s[20:23], s77 offen offset:2048 sc1\n"
    "buffer_load_dwordx4 a[56:59], v223, s[20:23], s77 offen offset:2048 sc1\n"
    "buffer_load_dwordx4 a[44:47], v222, s[20:23], s77 offen offset:3072 sc1\n"
    "buffer_load_dwordx4 a[60:63], v223, s[20:23], s77 offen offset:3072 sc1\n"
    "s_waitcnt vmcnt(0)\n"
    "s_barrier\n"
    "ds_read_b128 v[40:43], v121 offset:8192\n"
    "ds_read_b128 v[44:47], v121 offset:13312\n"
    "ds_read_b128 v[48:51], v121 offset:18432\n"
    "ds_read_b128 v[52:55], v121 offset:23552\n"
    "s_branch LG2_go\n"
    "LNG_fr_s:\n"
    "s_sub_u32 s39, s39, 1\n"
    "s_cbranch_scc1 LNG_fr_g\n"
    "s_sleep 32\n"
    "s_mov_b32 s85, 335872\n"
    "buffer_load_dwordx2 v[104:105], v109, s[20:23], s85 offen sc1\n"
    "s_waitcnt vmcnt(0)\n"
    "v_xor_b32 v106, s40, v104\n"
    "v_xor_b32 v107, s41, v105\n"
    "v_or_b32 v106, v106, v107\n"
    "v_cmp_ne_u32 vcc, 0, v106\n"
    "s_nop 1\n"
    "s_cbranch_vccnz LNG_fr_s\n"
    "s_branch LNG_fr_r\n"
    "LNG_fr_g:\n"
    "s_mov_b32 s39, 0\n"
    "s_branch LNG_fr_r\n"
    "LNG_fm_s:\n"
    "s_sub_u32 s39, s39, 1\n"
    "s_cbranch_scc1 LNG_fm_g\n"
    "s_sleep 32\n"
    "s_mov_b32 s85, 270336\n"
    "buffer_load_dwordx2 v[104:105], v110, s[20:23], s85 offen sc1\n"
    "s_waitcnt vmcnt(0)\n"
    "v_xor_b32 v106, s40, v104\n"
    "v_xor_b32 v107, s41, v105\n"
    "v_or_b32 v106, v106, v107\n"
    "v_cmp_ne_u32 vcc, 0, v106\n"
    "s_nop 1\n"
    "s_and_b64 vcc, vcc, 255\n"
    "s_cbranch_vccnz LNG_fm_s\n"
    "s_branch LNG_fm_r\n"
    "LNG_fm_g:\n"
    "s_mov_b32 s39, 0\n"
    "s_branch LNG_fm_r\n"
    "LM_X_33:\n"
    "s_nop 7\n"
    "s_waitcnt vmcnt(2)\n"
    "s_mov_b32 s50, 0x4091afa4\n"
    "v_lshlrev_b32 v24, 16, v128\n"
    "v_and_b32 v25, 0xffff0000, v128\n"
    "v_lshlrev_b32 v26, 16, v129\n"
    "v_and_b32 v27, 0xffff0000, v129\n"
    "v_lshlrev_b32 v28, 16, v130\n"
    "v_and_b32 v29, 0xffff0000, v130\n"
    "v_lshlrev_b32 v30, 16, v131\n"
    "v_and_b32 v31, 0xffff0000, v131\n"
    "v_lshlrev_b32 v32, 16, v132\n"
    "v_and_b32 v33, 0xffff0000, v132\n"
    "v_lshlrev_b32 v34, 16, v133\n"
    "v_and_b32 v35, 0xffff0000, v133\n"
    "v_lshlrev_b32 v36, 16, v134\n"
    "v_and_b32 v37, 0xffff0000, v134\n"
    "v_lshlrev_b32 v38, 16, v135\n"
    "v_and_b32 v39, 0xffff0000, v135\n"
    "v_lshlrev_b32 v40, 16, v136\n"
    "v_and_b32 v41, 0xffff0000, v136\n"
    "v_lshlrev_b32 v42, 16, v137\n"
    "v_and_b32 v43, 0xffff0000, v137\n"
    "v_lshlrev_b32 v44, 16, v138\n"
    "v_and_b32 v45, 0xffff0000, v138\n"
    "v_lshlrev_b32 v46, 16, v139\n"
    "v_and_b32 v47, 0xffff0000, v139\n"
    "s_waitcnt vmcnt(0)\n"
    "LM_rx_34:\n"
    "v_mfma_f32_16x16x32_bf16 v[48:51], v[140:143], v[232:235], v[8:11]\n"
    "v_max3_u32 v107, v232, v233, v234\n"
    "v_max3_u32 v107, v107, v235, v236\n"
    "v_mfma_f32_16x16x32_bf16 v[48:51], v[140:143], v[240:243], v[48:51]\n"
    "v_max3_u32 v107, v107, v237, v238\n"
    "v_max3_u32 v107, v107, v239, v240\n"
    "v_mfma_f32_16x16x32_bf16 v[48:51], v[140:143], v[248:251], v[48:51]\n"
    "v_max3_u32 v107, v107, v241, v242\n"
    "v_max3_u32 v107, v107, v243, v244\n"
    "v_mfma_f32_16x16x32_bf16 v[52:55], v[140:143], v[236:239], v[16:19]\n"
    "v_max3_u32 v107, v107, v245, v246\n"
    "v_max3_u32 v107, v107, v247, v248\n"
    "v_mfma_f32_16x16x32_bf16 v[52:55], v[140:143], v[244:247], v[52:55]\n"
    "v_max_u32 v107, v107, v249\n"
    "v_max3_u32 v106, v250, v251, v252\n"
    "v_mfma_f32_16x16x32_bf16 v[52:55], v[140:143], v[252:255], v[52:55]\n"
    "v_max3_u32 v106, v106, v253, v254\n"
    "v_max3_u32 v106, v106, v255, v192\n"
    "v_mfma_f32_16x16x32_bf16 v[56:59], v[140:143], v[192:195], v[224:227]\n"
    "v_max3_u32 v106, v106, v193, v194\n"
    "v_max3_u32 v106, v106, v195, v196\n"
    "v_mfma_f32_16x16x32_bf16 v[56:59], v[140:143], v[196:199], v[56:59]\n"
    "v_max3_u32 v106, v106, v197, v198\n"
    "v_max3_u32 v106, v106, v199, v108\n"
    "v_mfma_f32_16x16x32_bf16 v[56:59], v[140:143], v[108:111], v[56:59]\n"
    "v_max3_u32 v106, v106, v109, v110\n"
    "v_max_u32 v106, v106, v111\n"
    "v_mfma_f32_16x16x32_bf16 v[60:63], v[144:147], v[232:235], v[12:15]\n"
    "v_max_u32 v107, v107, v106\n"
    "v_cmp_eq_u32 vcc, s45, v107\n"
    "v_mfma_f32_16x16x32_bf16 v[60:63], v[144:147], v[240:243], v[60:63]\n"
    "v_fma_f32 v48, v48, s50, v24\n"
    "v_fma_f32 v49, v49, s50, v25\n"
    "v_mfma_f32_16x16x32_bf16 v[60:63], v[144:147], v[248:251], v[60:63]\n"
    "v_fma_f32 v50, v50, s50, v26\n"
    "v_fma_f32 v51, v51, s50, v27\n"
    "v_mfma_f32_16x16x32_bf16 v[64:67], v[144:147], v[236:239], v[20:23]\n"
    "v_fma_f32 v52, v52, s50, v32\n"
    "v_fma_f32 v53, v53, s50, v33\n"
    "v_mfma_f32_16x16x32_bf16 v[64:67], v[144:147], v[244:247], v[64:67]\n"
    "v_fma_f32 v54, v54, s50, v34\n"
    "v_fma_f32 v55, v55, s50, v35\n"
    "v_mfma_f32_16x16x32_bf16 v[64:67], v[144:147], v[252:255], v[64:67]\n"
    "v_fma_f32 v56, v56, s50, v40\n"
    "v_fma_f32 v57, v57, s50, v41\n"
    "v_mfma_f32_16x16x32_bf16 v[68:71], v[144:147], v[192:195], v[228:231]\n"
    "v_fma_f32 v58, v58, s50, v42\n"
    "v_fma_f32 v59, v59, s50, v43\n"
    "v_mfma_f32_16x16x32_bf16 v[68:71], v[144:147], v[196:199], v[68:71]\n"
    "v_fma_f32 v60, v60, s50, v28\n"
    "v_fma_f32 v61, v61, s50, v29\n"
    "v_mfma_f32_16x16x32_bf16 v[68:71], v[144:147], v[108:111], v[68:71]\n"
    "v_fma_f32 v62, v62, s50, v30\n"
    "v_fma_f32 v63, v63, s50, v31\n"
    "v_fma_f32 v64, v64, s50, v36\n"
    "v_fma_f32 v65, v65, s50, v37\n"
    "v_fma_f32 v66, v66, s50, v38\n"
    "v_fma_f32 v67, v67, s50, v39\n"
    "v_cvt_pk_bf16_f32 v72, v48, v49\n"
    "v_cvt_pk_bf16_f32 v73, v50, v51\n"
    "v_cvt_pk_bf16_f32 v74, v52, v53\n"
    "v_fma_f32 v68, v68, s50, v44\n"
    "v_fma_f32 v69, v69, s50, v45\n"
    "v_fma_f32 v70, v70, s50, v46\n"
    "v_fma_f32 v71, v71, s50, v47\n"
    "v_cvt_pk_bf16_f32 v75, v54, v55\n"
    "ds_write_b128 v168, v[72:75] offset:0\n"
    "v_cvt_pk_bf16_f32 v106, v56, v57\n"
    "v_cvt_pk_bf16_f32 v107, v58, v59\n"
    "v_cvt_pk_bf16_f32 v76, v60, v61\n"
    "v_cvt_pk_bf16_f32 v77, v62, v63\n"
    "v_cvt_pk_bf16_f32 v78, v64, v65\n"
    "v_cvt_pk_bf16_f32 v79, v66, v67\n"
    "v_cvt_pk_bf16_f32 v104, v68, v69\n"
    "v_cvt_pk_bf16_f32 v105, v70, v71\n"
    "ds_read_b128 v[48:51], v170 offset:0\n"
    "ds_write_b64 v169, v[106:107] offset:2048\n"
    "ds_read_b64 v[102:103], v171 offset:2048\n"
    "ds_write_b128 v168, v[76:79] offset:1024\n"
    "ds_read_b128 v[52:55], v170 offset:1024\n"
    "ds_write_b64 v169, v[104:105] offset:2560\n"
    "ds_read_b64 v[100:101], v171 offset:2560\n"
    "s_cbranch_vccz LM_rxok_35\n"
    "s_sleep 4\n"
    "buffer_load_dwordx4 v[232:235], v4, s[20:23], s55 offen offset:1024 sc1\n"
    "buffer_load_dwordx4 v[236:239], v4, s[20:23], s55 offen offset:2048 sc1\n"
    "buffer_load_dwordx4 v[240:243], v4, s[20:23], s56 offen offset:1024 sc1\n"
    "buffer_load_dwordx4 v[244:247], v4, s[20:23], s56 offen offset:2048 sc1\n"
    "buffer_load_dwordx4 v[248:251], v4, s[20:23], s57 offen offset:1024 sc1\n"
    "buffer_load_dwordx4 v[252:255], v4, s[20:23], s57 offen offset:2048 sc1\n"
    "buffer_load_dwordx4 v[192:195], v4, s[4:7], s52 offen sc1\n"
    "buffer_load_dwordx4 v[196:199], v4, s[4:7], s53 offen sc1\n"
    "buffer_load_dwordx4 v[108:111], v4, s[4:7], s54 offen sc1\n"
    "s_waitcnt vmcnt(0)\n"
    "s_sub_u32 s39, s39, 1\n"
    "s_cbranch_scc0 LM_rx_34\n"
    "LM_rxok_35:\n"
    "s_waitcnt lgkmcnt(6)\n"
    "buffer_store_dwordx4 v[48:51], v166, s[28:31], s64 offen\n"
    "s_waitcnt lgkmcnt(4)\n"
    "buffer_store_dwordx2 v[102:103], v167, s[24:27], s66 offen\n"
    "s_waitcnt lgkmcnt(2)\n"
    "buffer_store_dwordx4 v[52:55], v166, s[28:31], s65 offen\n"
    "s_waitcnt lgkmcnt(0)\n"
    "buffer_store_dwordx2 v[100:101], v167, s[24:27], s67 offen\n"
    "s_endpgm\n"
    "LM_end:\n"
    ".if (LM_end-LG1_e0) < 32768\n"
    ".error \"code prefetch window\"\n"
    ".endif\n"
    :: "{s[0:1]}"(__builtin_amdgcn_kernarg_segment_ptr()), "{s2}"(blockIdx.x), "{v0}"(threadIdx.x)
    : "memory", "scc", "vcc", "v1","v2","v3","v4","v5","v6","v7","v8","v9","v10","v11","v12","v13","v14","v15","v16","v17","v18","v19","v20","v21","v22","v23","v24","v25","v26","v27","v28","v29","v30","v31","v32","v33","v34","v35","v36","v37","v38","v39","v40","v41","v42","v43","v44","v45","v46","v47","v48","v49","v50","v51","v52","v53","v54","v55","v56","v57","v58","v59","v60","v61","v62","v63","v64","v65","v66","v67","v68","v69","v70","v71","v72","v73","v74","v75","v76","v77","v78","v79","v80","v81","v82","v83","v84","v85","v86","v87","v88","v89","v90","v91","v92","v93","v94","v95","v96","v97","v98","v99","v100","v101","v102","v103","v104","v105","v106","v107","v108","v109","v110","v111","v112","v113","v114","v115","v116","v117","v118","v119","v120","v121","v122","v123","v124","v125","v126","v127","v128","v129","v130","v131","v132","v133","v134","v135","v136","v137","v138","v139","v140","v141","v142","v143","v144","v145","v146","v147","v148","v149","v150","v151","v152","v153","v154","v155","v156","v157","v158","v159","v160","v161","v162","v163","v164","v165","v166","v167","v168","v169","v170","v171","v172","v173","v174","v175","v176","v177","v178","v179","v180","v181","v182","v183","v184","v185","v186","v187","v188","v189","v190","v191","v192","v193","v194","v195","v196","v197","v198","v199","v200","v201","v202","v203","v204","v205","v206","v207","v208","v209","v210","v211","v212","v213","v214","v215","v216","v217","v218","v219","v220","v221","v222","v223","v224","v225","v226","v227","v228","v229","v230","v231","v232","v233","v234","v235","v236","v237","v238","v239","v240","v241","v242","v243","v244","v245","v246","v247","v248","v249","v250","v251","v252","v253","v254","v255","a0","a1","a2","a3","a4","a5","a6","a7","a8","a9","a10","a11","a12","a13","a14","a15","a16","a17","a18","a19","a20","a21","a22","a23","a24","a25","a26","a27","a28","a29","a30","a31","a32","a33","a34","a35","a36","a37","a38","a39","a40","a41","a42","a43","a44","a45","a46","a47","a48","a49","a50","a51","a52","a53","a54","a55","a56","a57","a58","a59","a60","a61","a62","a63","a64","a65","a66","a67","a68","a69","a70","a71","a72","a73","a74","a75","a76","a77","a78","a79","a80","a81","a82","a83","a84","a85","a86","a87","a88","a89","a90","a91","a92","a93","a94","a95","a96","a97","a98","a99","a100","a101","a102","a103","a104","a105","a106","a107","a108","a109","a110","a111","a112","a113","a114","a115","a116","a117","a118","a119","a120","a121","a122","a123","a124","a125","a126","a127","a128","a129","a130","a131","a132","a133","a134","a135","a136","a137","a138","a139","a140","a141","a142","a143","a144","a145","a146","a147","a148","a149","a150","a151","a152","a153","a154","a155","a156","a157","a158","a159","a160","a161","a162","a163","a164","a165","a166","a167","a168","a169","a170","a171","a172","a173","a174","a175","a176","a177","a178","a179","a180","a181","a182","a183","a184","a185","a186","a187","a188","a189","a190","a191","a192","a193","a194","a195","a196","a197","a198","a199","a200","a201","a202","a203","a204","a205","a206","a207","a208","a209","a210","a211","a212","a213","a214","a215","a216","a217","a218","a219","a220","a221","a222","a223","a224","a225","a226","a227","a228","a229","a230","a231","a232","a233","a234","a235","a236","a237","a238","a239","a240","a241","a242","a243","a244","a245","a246","a247","a248","a249","a250","a251","a252","a253","a254","a255","s3","s4","s5","s6","s7","s8","s9","s10","s11","s12","s13","s14","s15","s16","s17","s18","s19","s20","s21","s22","s23","s24","s25","s26","s27","s28","s29","s30","s31","s33","s34","s35","s36","s37","s38","s39","s40","s41","s42","s43","s44","s45","s46","s47","s48","s49","s50","s51","s52","s53","s54","s55","s56","s57","s58","s59","s60","s61","s62","s63","s64","s65","s66","s67","s68","s69","s70","s71","s72","s73","s74","s75","s76","s77","s78","s79","s80","s81","s82","s83","s84","s85","s86","s87","s88","s89","s90","s91","s92","s93","s94","s95","s96","s97","s98","s99");
}

extern "C" void kernel_launch(void* const* d_in, const int* in_sizes, int n_in, void* d_out, int out_size, void* d_ws, size_t ws_size, hipStream_t stream) {
  (void)hipFuncSetAttribute((const void*)mega_kernel, hipFuncAttributeMaxDynamicSharedMemorySize, 163840);
  mega_kernel<<<256, 256, 163840, stream>>>(d_in[0], d_in[1], d_in[2], d_in[4], d_in[5], d_in[6], d_out, d_ws);
}
```
